```python
import torch

def _softmax(x, axis=-1):
    return torch.softmax(x, dim=axis)
def _gelu(x):
    return torch.nn.functional.gelu(x, approximate="tanh")
def _silu(x):
    return torch.nn.functional.silu(x)
def _sigmoid(x):
    return torch.sigmoid(x)
def _layernorm(x, axis=-1, eps=1e-5):
    m = x.mean(dim=axis, keepdim=True)
    v = x.var(dim=axis, keepdim=True, unbiased=False)
    return (x - m) / torch.sqrt(v + eps)
def _scatter_add(tgt, idx, src, axis=0):
    return tgt.clone().index_add_(axis, idx, src)
def _segment_sum(src, seg, n):
    out = torch.zeros((n,) + src.shape[1:], dtype=src.dtype)
    return out.index_add_(0, seg, src)
def _pad(x, pw):
    flat = tuple(v for pair in reversed(pw) for v in pair)
    return torch.nn.functional.pad(x, flat)

kernel_name = 'gated_mix__compressed_kv_cla__mixer_s_mlp__prompt'

def setup_inputs():

    _w = (lambda a: (lambda t: t.to(torch.bfloat16) if t.is_floating_point() else t)(torch.as_tensor(a)))
    def _A_setup():
        B, H, Sq, Sk, D, M = 1, 16, 2048, 2048, 256, 448
        Dc = 4 * D
        return {
            "x": _w((lambda *s: torch.randn(*s, dtype=torch.bfloat16) * 0.05)(B, Sq, M)),
            "c_kv": _w((lambda *s: torch.randn(*s, dtype=torch.bfloat16) * 1.6)(B, Sk, Dc)),
            "w_q": _w((lambda *s: torch.randn(*s, dtype=torch.bfloat16) * 6.4)(M, H, D) * (M ** -0.5)),
            "w_uk": _w((lambda *s: torch.randn(*s, dtype=torch.bfloat16) * 6.4)(Dc, H, D) * (Dc ** -0.5)),
            "w_uv": _w((lambda *s: torch.randn(*s, dtype=torch.bfloat16) * 1.6)(Dc, H, D) * (Dc ** -0.5)),
            "w_o": _w((lambda *s: torch.randn(*s, dtype=torch.bfloat16) * 1.6)(H, D, M) * ((H * D) ** -0.5)),
        }
    _A_in = _A_setup()

    def _B_setup():
        B, S, D, F = 1, 2048, 448, 1792
        return {
            "x":  _w((lambda *s: torch.randn(*s, dtype=torch.bfloat16) * 0.05)(B, S, D)),
            "w1": _w((lambda *s: torch.randn(*s, dtype=torch.bfloat16) * 3.2)(D, D) * (D ** -0.5)),
            "w2": _w((lambda *s: torch.randn(*s, dtype=torch.bfloat16) * 3.2)(D, D) * (D ** -0.5)),
            "w3": _w((lambda *s: torch.randn(*s, dtype=torch.bfloat16) * 3.2)(D, F) * (D ** -0.5)),
            "w4": _w((lambda *s: torch.randn(*s, dtype=torch.bfloat16) * 1.6)(F, D) * (F ** -0.5)),
        }
    _B_in = _B_setup()

    return {
        "A_x": _A_in["x"],
        "A_c_kv": _A_in["c_kv"],
        "A_w_q": _A_in["w_q"],
        "A_w_uk": _A_in["w_uk"],
        "A_w_uv": _A_in["w_uv"],
        "A_w_o": _A_in["w_o"],
        "B_x": _B_in["x"],
        "B_w1": _B_in["w1"],
        "B_w2": _B_in["w2"],
        "B_w3": _B_in["w3"],
        "B_w4": _B_in["w4"],
        "gate_w": _w((lambda *s: torch.randn(*s, dtype=torch.bfloat16) * 0.05)(448) * 16.0),
    }

def reference(A_x, A_c_kv, A_w_q, A_w_uk, A_w_uv, A_w_o, B_x, B_w1, B_w2, B_w3, B_w4, gate_w):
    def _A_ref(x, c_kv, w_q, w_uk, w_uv, w_o):
        B, Sq, M = x.shape[0], x.shape[1], x.shape[2]
        Sk = c_kv.shape[1]
        H, D = w_q.shape[1], w_q.shape[2]
        q = torch.einsum("bqm,mhd->bhqd", x, w_q)
        k = torch.einsum("bkc,chd->bhkd", c_kv, w_uk)
        v = torch.einsum("bkc,chd->bhkd", c_kv, w_uv)
        scores = torch.einsum("bhqd,bhkd->bhqk", q, k) * (D ** -0.5)
        off = Sk - Sq
        qi = torch.arange(Sq)[:, None]
        kj = torch.arange(Sk)[None, :]
        cmask = kj <= (qi + off)
        scores = torch.where(cmask[None, None], scores, -1e9)
        p = _softmax(scores, axis=-1)
        ctx = torch.einsum("bhqk,bhkd->bhqd", p, v)
        return torch.einsum("bhqd,hdm->bqm", ctx, w_o)

    def _B_ref(x, w1, w2, w3, w4):
        B, S, D = x.shape
        h = _gelu(_layernorm(x, axis=-1) @ w1)
        c = D // 4
        g0 = _pad(h[:, :, 0*c:1*c], ((0, 0), (1, 0), (0, 0)))[:, :S, :]
        g1 = _pad(h[:, :, 1*c:2*c], ((0, 0), (0, 1), (0, 0)))[:, 1:, :]
        g2 = _pad(h[:, :, 2*c:3*c], ((0, 0), (2, 0), (0, 0)))[:, :S, :]
        g3 = _pad(h[:, :, 3*c:   ], ((0, 0), (0, 2), (0, 0)))[:, 2:, :]
        hs = torch.concatenate([g0, g1, g2, g3], axis=-1) @ w2
        x1 = x + hs
        h2 = _gelu(_layernorm(x1, axis=-1) @ w3) @ w4
        return x1 + h2

    x = A_x
    g = _sigmoid((x * gate_w).sum(-1, keepdims=True))
    a = _A_ref(x, A_c_kv, A_w_q, A_w_uk, A_w_uv, A_w_o)
    b = _B_ref(x, B_w1, B_w2, B_w3, B_w4)
    return g * a + (1 - g) * b
```

```cpp
#include <hip/hip_runtime.h>
#include <hip/hip_bf16.h>
typedef unsigned short u16;
typedef unsigned long long u64;
typedef unsigned u32x4 __attribute__((ext_vector_type(4)));
typedef unsigned u32x16 __attribute__((ext_vector_type(16)));
#define OFF_KV 96468992u
#define OFF_Q 33554432u
#define OFF_HS 50331648u
#define OFF_X1 52428800u
#define OFF_ST 54525952u
#define OFF_CS3 55050240u
#define OFF_G 55115776u
#define OFF_XT 2097152u
#define OFF_ML 55574528u
#define OFF_PO 56623104u
#define OFF_PART 65011712u
#define OFF_TS 95420416u
#define F1 0u
#define F2 8192u
#define WS_BYTES 138412032u
#define TSTR 65536u
#define HSTR (32u*TSTR + 0u)

__global__ __launch_bounds__(256, 1) void k1_fused(const u16* x, const u16* ckv, const u16* wq, const u16* wuk, const u16* wuv, const u16* wo, const u16* w1,
                                                   const u16* w2, const u16* w3, const u16* w4, const u16* gw, char* ws, u16* out) {
  __shared__ __attribute__((aligned(16))) char lds[163840];
  const unsigned t0 = 0;
  const unsigned w = __builtin_amdgcn_readfirstlane(threadIdx.x >> 6), l = threadIdx.x & 63;
  const unsigned r = l & 15, g = l >> 4, q = (l >> 2) & 3, p = l & 3;
  const unsigned xcd = blockIdx.x & 7, jb = blockIdx.x >> 3, h = 2 * xcd + (jb >> 4), jj = jb & 15;
  asm volatile("" ::"v"((unsigned)(size_t)(__attribute__((address_space(3))) char*)lds));

  const unsigned isV = jj >> 3, keyblk = jj & 7, wn = w & 1, wk = w >> 1;
  const unsigned isA = jj < 8, ii = jj & 7, nB = isA ? 99u : 15u - 2u * ii, qi = isA ? 15 - ii : ii;
  const unsigned rb2 = h * 4 + w, rbf = qi * 4 + w;
  const u16* wkv = isV ? wuv : wuk;
  const unsigned isB1 = h < 14, sidx = (h - 14) * 16 + qi;
  const unsigned strE = isB1 ? 896u : 3584u;
  const u16* eptr = isB1 ? w1 + (h >> 1) * 64 : w3 + (sidx < 28 ? sidx : 0) * 64;
  const unsigned ebytes = isB1 ? 448 * 896 - (h >> 1) * 128 : (sidx < 28 ? 448 * 3584 - sidx * 128 : 0);
  const unsigned eh = isB1 ? (h & 1) : wn;
  const unsigned kvbase = OFF_KV + h * HSTR + (keyblk * 4 + wk * 2) * TSTR + (isV ? 32768u + wn * 8192u : wn * 4096u);
  auto hsaddr = [&](int qb, int hf) {
    const int cb = 2 * h + hf, rr = rbf * 32 + qb * 16 + r + (cb < 7 ? 1 : cb < 14 ? -1 : cb < 21 ? 2 : -2);
    const unsigned z = rr & 2047;
    return (OFF_HS + (((z >> 5) * 14 + h) * 2 + ((z >> 4) & 1)) * 1024u + ((z & 15) + 16 * g) * 16u + hf * 8u) | ((rr < 0 || rr > 2047) ? 0x80000000u : 0u);
  };
  const unsigned hso0 = isB1 ? hsaddr(0, 0) : (r == 0 && wk == 0 && sidx < 28) ? OFF_CS3 + (sidx * 64 + wn * 32 + 4 * g) * 4u : 0xc0000000u;
  const unsigned hso1 = hsaddr(0, 1), hso2 = hsaddr(1, 0), hso3 = hsaddr(1, 1);
  const unsigned f1a = F1 + (g == 3 ? qi : (h + 15 + g) & 15) * 256u + (r < 14 ? r : 0) * 4u;
  const unsigned f2a = g % 3 == 0 ? F2 + qi * 256u + (r < 14 ? r : 0) * 4u : F1 + 4096u + ((g - 1) * 16 + r) * 4u;
  const u32x4 er48 = {(unsigned)__builtin_amdgcn_readfirstlane((unsigned)(u64)(x)), (unsigned)__builtin_amdgcn_readfirstlane((unsigned)((u64)(x)>>32)), (unsigned)__builtin_amdgcn_readfirstlane((unsigned)(2048*896)), (unsigned)__builtin_amdgcn_readfirstlane(0x20000u)};
  const u32x4 er56 = {(unsigned)__builtin_amdgcn_readfirstlane((unsigned)(u64)(eptr)), (unsigned)__builtin_amdgcn_readfirstlane((unsigned)((u64)(eptr)>>32)), (unsigned)__builtin_amdgcn_readfirstlane((unsigned)(ebytes)), (unsigned)__builtin_amdgcn_readfirstlane(0x20000u)};
  const u32x4 er60 = {(unsigned)__builtin_amdgcn_readfirstlane((unsigned)(u64)(gw)), (unsigned)__builtin_amdgcn_readfirstlane((unsigned)((u64)(gw)>>32)), (unsigned)__builtin_amdgcn_readfirstlane((unsigned)(896)), (unsigned)__builtin_amdgcn_readfirstlane(0x20000u)};
  const u32x4 er36 = {(unsigned)__builtin_amdgcn_readfirstlane((unsigned)(u64)(ws)), (unsigned)__builtin_amdgcn_readfirstlane((unsigned)((u64)(ws)>>32)), (unsigned)__builtin_amdgcn_readfirstlane((unsigned)(WS_BYTES)), (unsigned)__builtin_amdgcn_readfirstlane(0x20000u)};
  asm volatile(
R"ASM(
s_add_u32 m0, s68, 65536
s_mov_b32 s99, 195584
buffer_load_dwordx4 v236, s[60:63], 0 offen offset:0 lds
buffer_load_dwordx4 v236, s[36:39], s99 offen offset:1024 sc1 lds
s_add_u32 m0, s68, 131072
s_mov_b32 s99, 0
buffer_load_dwordx4 v212, s[48:51], s99 offen offset:0 lds
s_mov_b32 s99, 6144
buffer_load_dwordx4 v213, s[48:51], s99 offen offset:1024 lds
s_mov_b32 s99, 12288
buffer_load_dwordx4 v212, s[48:51], s99 offen offset:2048 lds
s_mov_b32 s99, 18432
buffer_load_dwordx4 v213, s[48:51], s99 offen offset:3072 lds
s_add_u32 m0, s68, 32768
s_mul_i32 s99, s72, 0
s_sub_u32 s99, s99, 0
buffer_load_dwordx4 v210, s[56:59], s99 offen offset:0 lds
s_mul_i32 s99, s72, 16
s_sub_u32 s99, s99, 1024
buffer_load_dwordx4 v210, s[56:59], s99 offen offset:1024 lds
s_mul_i32 s99, s72, 4
s_sub_u32 s99, s99, 2048
buffer_load_dwordx4 v210, s[56:59], s99 offen offset:2048 lds
s_mul_i32 s99, s72, 20
s_sub_u32 s99, s99, 3072
buffer_load_dwordx4 v210, s[56:59], s99 offen offset:3072 lds
s_add_u32 m0, s68, 36864
s_mul_i32 s99, s72, 32
s_sub_u32 s99, s99, 0
buffer_load_dwordx4 v210, s[56:59], s99 offen offset:0 lds
s_mul_i32 s99, s72, 48
s_sub_u32 s99, s99, 1024
buffer_load_dwordx4 v210, s[56:59], s99 offen offset:1024 lds
s_mul_i32 s99, s72, 36
s_sub_u32 s99, s99, 2048
buffer_load_dwordx4 v210, s[56:59], s99 offen offset:2048 lds
s_mul_i32 s99, s72, 52
s_sub_u32 s99, s99, 3072
buffer_load_dwordx4 v210, s[56:59], s99 offen offset:3072 lds
s_add_u32 m0, s68, 135168
s_mov_b32 s99, 128
buffer_load_dwordx4 v212, s[48:51], s99 offen offset:0 lds
s_mov_b32 s99, 6272
buffer_load_dwordx4 v213, s[48:51], s99 offen offset:1024 lds
s_mov_b32 s99, 12416
buffer_load_dwordx4 v212, s[48:51], s99 offen offset:2048 lds
s_mov_b32 s99, 18560
buffer_load_dwordx4 v213, s[48:51], s99 offen offset:3072 lds
)ASM"
  :
  : "{v212}"((rbf*32+(l>>3))*896u + (((l&7)^((0*4+(l>>4))&7))*16u)), "{v213}"((rbf*32+(l>>3))*896u + (((l&7)^((1*4+(l>>4))&7))*16u)), "{v210}"((w*64 + 8*(l>>5) + ((l>>3)&3))*strE + (((l&7)^(((l>>4)&3)<<1))*16u)), "{v236}"(l*16u), "{s68}"(__builtin_amdgcn_readfirstlane(w*8192u)), "{s72}"(__builtin_amdgcn_readfirstlane(strE)), "{s[48:51]}"(er48), "{s[56:59]}"(er56), "{s[60:63]}"(er60), "{s[36:39]}"(er36)
  : "memory","scc","s99");
  __builtin_amdgcn_sched_barrier(0);
  unsigned tab = 0;
  asm("v_writelane_b32 %0, %1, 0" : "+v"(tab) : "s"(__builtin_amdgcn_readfirstlane((unsigned)(u64)(wo + h*256*448))));
  asm("v_writelane_b32 %0, %1, 1" : "+v"(tab) : "s"(__builtin_amdgcn_readfirstlane((unsigned)((u64)(wo + h*256*448)>>32))));
  asm("v_writelane_b32 %0, %1, 2" : "+v"(tab) : "s"(__builtin_amdgcn_readfirstlane(256*896)));
  asm("v_writelane_b32 %0, %1, 3" : "+v"(tab) : "s"(__builtin_amdgcn_readfirstlane((unsigned)(u64)(w4 + h*112*448))));
  asm("v_writelane_b32 %0, %1, 4" : "+v"(tab) : "s"(__builtin_amdgcn_readfirstlane((unsigned)((u64)(w4 + h*112*448)>>32))));
  asm("v_writelane_b32 %0, %1, 5" : "+v"(tab) : "s"(__builtin_amdgcn_readfirstlane(112*896)));
  asm("v_writelane_b32 %0, %1, 6" : "+v"(tab) : "s"(__builtin_amdgcn_readfirstlane((unsigned)(u64)(out))));
  asm("v_writelane_b32 %0, %1, 7" : "+v"(tab) : "s"(__builtin_amdgcn_readfirstlane((unsigned)((u64)(out)>>32))));
  asm("v_writelane_b32 %0, %1, 8" : "+v"(tab) : "s"(__builtin_amdgcn_readfirstlane(2048*896)));
  asm("v_writelane_b32 %0, %1, 9" : "+v"(tab) : "s"(__builtin_amdgcn_readfirstlane((unsigned)(u64)(ws))));
  asm("v_writelane_b32 %0, %1, 10" : "+v"(tab) : "s"(__builtin_amdgcn_readfirstlane((unsigned)((u64)(ws)>>32))));
  asm("v_writelane_b32 %0, %1, 11" : "+v"(tab) : "s"(__builtin_amdgcn_readfirstlane(isA ? WS_BYTES : 0)));
  asm("v_writelane_b32 %0, %1, 12" : "+v"(tab) : "s"(__builtin_amdgcn_readfirstlane(OFF_KV + h*HSTR + w*8192u)));
  asm("v_writelane_b32 %0, %1, 13" : "+v"(tab) : "s"(__builtin_amdgcn_readfirstlane(isA ? 0u : 17u*TSTR)));
  asm("v_writelane_b32 %0, %1, 14" : "+v"(tab) : "s"(__builtin_amdgcn_readfirstlane(nB)));
  asm("v_writelane_b32 %0, %1, 15" : "+v"(tab) : "s"(__builtin_amdgcn_readfirstlane(w ? 0xc0000000u : 65536u + (h*16+jj)*256u)));
  asm("v_writelane_b32 %0, %1, 16" : "+v"(tab) : "s"(__builtin_amdgcn_readfirstlane(isA ? 262144u + ((h*8+ii)*4u + w)*256u : 0xc0000000u)));
  asm("v_writelane_b32 %0, %1, 17" : "+v"(tab) : "s"(__builtin_amdgcn_readfirstlane(OFF_Q + (h*64 + (15-ii)*4 + w)*16384u)));
  asm("v_writelane_b32 %0, %1, 18" : "+v"(tab) : "s"(__builtin_amdgcn_readfirstlane(isA ? (ii == 7 ? 1u << 16 : 0u) : (3u << 15) | ((3u << nB) >> 2))));
  asm("v_writelane_b32 %0, %1, 19" : "+v"(tab) : "s"(__builtin_amdgcn_readfirstlane(isA ? 0u : (7u << nB) >> 1)));
  asm("v_writelane_b32 %0, %1, 20" : "+v"(tab) : "s"(__builtin_amdgcn_readfirstlane(65536u + h*4096u)));
  asm("v_writelane_b32 %0, %1, 21" : "+v"(tab) : "s"(__builtin_amdgcn_readfirstlane(262144u + ((h*8+ii)*4u + w)*256u)));
  asm("v_writelane_b32 %0, %1, 22" : "+v"(tab) : "s"(__builtin_amdgcn_readfirstlane(OFF_PO + ((h*8+ii)*4u + w)*16384u)));
  asm("v_writelane_b32 %0, %1, 23" : "+v"(tab) : "s"(__builtin_amdgcn_readfirstlane(OFF_ML + ((h*8+ii)*4u + w)*1024u)));
  asm("v_writelane_b32 %0, %1, 24" : "+v"(tab) : "s"(__builtin_amdgcn_readfirstlane(OFF_PART + (h*64 + rbf)*28672u)));
  asm("v_writelane_b32 %0, %1, 25" : "+v"(tab) : "s"(__builtin_amdgcn_readfirstlane(isA)));
  asm("v_writelane_b32 %0, %1, 26" : "+v"(tab) : "s"(__builtin_amdgcn_readfirstlane(w ? 0xc0000000u : 131072u + (qi*16+h)*256u)));
  asm("v_writelane_b32 %0, %1, 27" : "+v"(tab) : "s"(__builtin_amdgcn_readfirstlane(131072u + qi*4096u)));
  asm("v_writelane_b32 %0, %1, 28" : "+v"(tab) : "s"(__builtin_amdgcn_readfirstlane((qi*4 + (h>>2))*28672u + (h&3)*1024u + w*4096u)));
  asm("v_writelane_b32 %0, %1, 29" : "+v"(tab) : "s"(__builtin_amdgcn_readfirstlane(OFF_G + blockIdx.x*512u + (h>>2)*128u + (h&1)*64u)));
  asm("v_writelane_b32 %0, %1, 30" : "+v"(tab) : "s"(__builtin_amdgcn_readfirstlane((blockIdx.x == 0 && w == 0) + 2*(w == 3))));
  asm("v_writelane_b32 %0, %1, 31" : "+v"(tab) : "s"(__builtin_amdgcn_readfirstlane(OFF_TS + ((isA*128 + h*8 + ii)*4 + w)*1024u)));
  u32x16 va0;
  va0[0] = 32768u + (4*g+q)*128u + (((2*(eh*2+0)+(p>>1))^(((2*g+(q>>1))&3)<<1))*16u) + (p&1)*8u;
  va0[1] = 32768u + (4*g+q)*128u + (((2*(eh*2+1)+(p>>1))^(((2*g+(q>>1))&3)<<1))*16u) + (p&1)*8u;
  va0[2] = (w*64 + 8*(l>>5) + ((l>>3)&3))*strE + (((l&7)^(((l>>4)&3)<<1))*16u);
  va0[3] = 0u;
  va0[4] = (rbf*32+(l>>3))*896u + (((l&7)^((0*4+(l>>4))&7))*16u);
  va0[5] = (rbf*32+(l>>3))*896u + (((l&7)^((1*4+(l>>4))&7))*16u);
  va0[6] = 131072u + w*8192u + r*128u + (((0+g)^((r>>1)&7))*16u);
  va0[7] = 131072u + w*8192u + r*128u + (((4+g)^((r>>1)&7))*16u);
  va0[8] = (rbf*32+r)*896u + g*16u;
  va0[9] = hso0;
  va0[10] = hso1;
  va0[11] = hso2;
  va0[12] = hso3;
  va0[13] = (64*w+(l>>3))*2048u + (((l&7)^((0*4+(l>>4))&7))*16u);
  va0[14] = (64*w+(l>>3))*2048u + (((l&7)^((1*4+(l>>4))&7))*16u);
  va0[15] = (8*(l>>5)+((l>>3)&3))*8192u + w*128u + (((l&7)^(((l>>4)&3)<<1))*16u);
  u32x16 va1;
  va1[0] = (wk*128+r)*128u + (((0+g)^((r>>1)&7))*16u);
  va1[1] = (wk*128+r)*128u + (((4+g)^((r>>1)&7))*16u);
  va1[2] = (w*128+(l>>2))*896u + (((l&3)^(((l>>4)&1)<<1))*16u);
  va1[3] = 131072u + (4*g+q)*64u + ((0^(g&1))*32u) + p*8u;
  va1[4] = 131072u + (4*g+q)*64u + ((1^(g&1))*32u) + p*8u;
  va1[5] = r - 8*g;
  va1[6] = OFF_ST + (jj*2048u + rb2*32 + r)*8u + (g ? 0x40000000u : 0u);
  va1[7] = f1a;
  va1[8] = 65536u + wn*16384u + (4*g+q)*128u + (((2*(0)+(p>>1))^(((2*g+(q>>1))&3)<<1))*16u) + (p&1)*8u;
  va1[9] = 65536u + wn*16384u + (4*g+q)*128u + (((2*(1)+(p>>1))^(((2*g+(q>>1))&3)<<1))*16u) + (p&1)*8u;
  va1[10] = 65536u + wn*16384u + (4*g+q)*128u + (((2*(2)+(p>>1))^(((2*g+(q>>1))&3)<<1))*16u) + (p&1)*8u;
  va1[11] = 65536u + wn*16384u + (4*g+q)*128u + (((2*(3)+(p>>1))^(((2*g+(q>>1))&3)<<1))*16u) + (p&1)*8u;
  va1[12] = l*16u;
  va1[13] = tab;
  va1[14] = 0u;
  va1[15] = 0u;
  u32x16 va2;
  va2[0] = 0u;
  va2[1] = 0u;
  va2[2] = (w*64+(l>>3))*3584u + (((l&7)^(((l>>4)&3)<<1))*16u);
  va2[3] = f2a;
  va2[4] = (w*64+(l>>3))*896u + (((l&7)^(((l>>4)&3)<<1))*16u);
  va2[5] = ((w&1)*56+(l>>3))*896u + (w>>1)*128u + (((l&7)^(((l>>4)&3)<<1))*16u);
  va2[6] = r - 4*g;
  va2[7] = r*4u;
  va2[8] = OFF_ST + (g*2048u + rbf*32 + r)*8u;
  va2[9] = OFF_CS3 + (h*112 + 4*g)*4u;
  va2[10] = (qi*128 + (h>>2)*32 + (h&1)*16 + r)*896u + ((h>>1)&1)*64u + g*8u + w*128u;
  va2[11] = (w*64 + 8*(l>>5) + ((l>>3)&3))*8192u + (((l&7)^(((l>>4)&3)<<1))*16u);
  va2[12] = 0u;
  va2[13] = 0u;
  va2[14] = 0u;
  va2[15] = 0u;
  u32x16 sa0;
  sa0[0] = __builtin_amdgcn_readfirstlane((unsigned)(u64)(ws));
  sa0[1] = __builtin_amdgcn_readfirstlane((unsigned)((u64)(ws)>>32));
  sa0[2] = __builtin_amdgcn_readfirstlane((unsigned)(WS_BYTES));
  sa0[3] = __builtin_amdgcn_readfirstlane(0x20000u);
  sa0[4] = __builtin_amdgcn_readfirstlane((unsigned)(u64)(ckv + keyblk*256*1024));
  sa0[5] = __builtin_amdgcn_readfirstlane((unsigned)((u64)(ckv + keyblk*256*1024)>>32));
  sa0[6] = __builtin_amdgcn_readfirstlane((unsigned)(256*2048));
  sa0[7] = __builtin_amdgcn_readfirstlane(0x20000u);
  sa0[8] = __builtin_amdgcn_readfirstlane((unsigned)(u64)(wkv + h*256));
  sa0[9] = __builtin_amdgcn_readfirstlane((unsigned)((u64)(wkv + h*256)>>32));
  sa0[10] = __builtin_amdgcn_readfirstlane((unsigned)(1024*8192 - h*512));
  sa0[11] = __builtin_amdgcn_readfirstlane(0x20000u);
  sa0[12] = __builtin_amdgcn_readfirstlane((unsigned)(u64)(x));
  sa0[13] = __builtin_amdgcn_readfirstlane((unsigned)((u64)(x)>>32));
  sa0[14] = __builtin_amdgcn_readfirstlane((unsigned)(2048*896));
  sa0[15] = __builtin_amdgcn_readfirstlane(0x20000u);
  u32x16 sa1;
  sa1[0] = __builtin_amdgcn_readfirstlane((unsigned)(u64)(wq + h*256));
  sa1[1] = __builtin_amdgcn_readfirstlane((unsigned)((u64)(wq + h*256)>>32));
  sa1[2] = __builtin_amdgcn_readfirstlane((unsigned)(448*8192 - h*512));
  sa1[3] = __builtin_amdgcn_readfirstlane(0x20000u);
  sa1[4] = __builtin_amdgcn_readfirstlane((unsigned)(u64)(eptr));
  sa1[5] = __builtin_amdgcn_readfirstlane((unsigned)((u64)(eptr)>>32));
  sa1[6] = __builtin_amdgcn_readfirstlane((unsigned)(ebytes));
  sa1[7] = __builtin_amdgcn_readfirstlane(0x20000u);
  sa1[8] = __builtin_amdgcn_readfirstlane((unsigned)(u64)(gw));
  sa1[9] = __builtin_amdgcn_readfirstlane((unsigned)((u64)(gw)>>32));
  sa1[10] = __builtin_amdgcn_readfirstlane((unsigned)(896));
  sa1[11] = __builtin_amdgcn_readfirstlane(0x20000u);
  sa1[12] = __builtin_amdgcn_readfirstlane((unsigned)(u64)(w2 + jj*32));
  sa1[13] = __builtin_amdgcn_readfirstlane((unsigned)((u64)(w2 + jj*32)>>32));
  sa1[14] = __builtin_amdgcn_readfirstlane((unsigned)(jj<14 ? 448*896 - jj*64 : 0));
  sa1[15] = __builtin_amdgcn_readfirstlane(0x20000u);
  u32x16 sa2;
  sa2[0] = __builtin_amdgcn_readfirstlane(w*8192u);
  sa2[1] = __builtin_amdgcn_readfirstlane(isB1 ? OFF_XT + rbf*28672u + h*2048u : 0xc0000000u);
  sa2[2] = __builtin_amdgcn_readfirstlane(isB1 ? h*8u : 0u);
  sa2[3] = __builtin_amdgcn_readfirstlane(w*7168u);
  sa2[4] = __builtin_amdgcn_readfirstlane(strE);
  sa2[5] = __builtin_amdgcn_readfirstlane((keyblk*8 + w*2)*8192u);
  sa2[6] = __builtin_amdgcn_readfirstlane(isV);
  sa2[7] = __builtin_amdgcn_readfirstlane(isB1);
  sa2[8] = __builtin_amdgcn_readfirstlane(kvbase);
  sa2[9] = __builtin_amdgcn_readfirstlane(isV ? 4096u : 8192u);
  sa2[10] = __builtin_amdgcn_readfirstlane(((h*2+isV)*4+w)*4096u);
  sa2[11] = __builtin_amdgcn_readfirstlane((jb*4+w)*1024u);
  sa2[12] = __builtin_amdgcn_readfirstlane(w ? 0xc0000000u : 0u + (isB1 ? qi*256u + h*4u : 4096u + sidx*4u));
  sa2[13] = __builtin_amdgcn_readfirstlane(w ? 0xc0000000u : 8192u + h*256u + jj*4u);
  sa2[14] = __builtin_amdgcn_readfirstlane(OFF_HS + rb2*28672u);
  sa2[15] = __builtin_amdgcn_readfirstlane(OFF_X1 + rb2*28672u + jj*2048u);
  u32x16 sa3;
  sa3[0] = __builtin_amdgcn_readfirstlane(OFF_X1 + rbf*28672u);
  sa3[1] = __builtin_amdgcn_readfirstlane(jj<14);
  sa3[2] = __builtin_amdgcn_readfirstlane(OFF_G + blockIdx.x*512u + w*128u);
  sa3[3] = __builtin_amdgcn_readfirstlane((jj*4+w)*28u);
  sa3[4] = __builtin_amdgcn_readfirstlane((unsigned)(u64)(w3 + h*112));
  sa3[5] = __builtin_amdgcn_readfirstlane((unsigned)((u64)(w3 + h*112)>>32));
  sa3[6] = __builtin_amdgcn_readfirstlane((unsigned)(448*3584 - h*224));
  sa3[7] = __builtin_amdgcn_readfirstlane(0x20000u);
  sa3[8] = __builtin_amdgcn_readfirstlane(0u);
  sa3[9] = __builtin_amdgcn_readfirstlane(0u);
  sa3[10] = __builtin_amdgcn_readfirstlane(0u);
  sa3[11] = __builtin_amdgcn_readfirstlane(0u);
  sa3[12] = __builtin_amdgcn_readfirstlane(0u);
  sa3[13] = __builtin_amdgcn_readfirstlane(0u);
  sa3[14] = __builtin_amdgcn_readfirstlane(0u);
  sa3[15] = __builtin_amdgcn_readfirstlane(0u);
  asm volatile(
R"ASM(
.Lbeg:
s_mov_b32 s4, 0
s_mov_b32 s12, 0
s_mov_b32 s5, 15360
s_mov_b32 s13, 130048
s_mov_b32 s6, 30720
s_mov_b32 s14, 30720
s_mov_b32 s7, 46080
s_mov_b32 s15, 160768
s_mov_b32 s8, 65536
s_mov_b32 s16, 262144
s_mov_b32 s9, 80896
s_mov_b32 s17, 392192
s_mov_b32 s10, 96256
s_mov_b32 s18, 292864
s_mov_b32 s11, 111616
s_mov_b32 s19, 422912
s_mov_b32 s27, 0
s_mov_b32 s96, 524288
s_add_u32 m0, s68, 0
s_mov_b32 s99, 256
buffer_load_dwordx4 v212, s[48:51], s99 offen offset:0 lds
s_mov_b32 s99, 6400
buffer_load_dwordx4 v213, s[48:51], s99 offen offset:1024 lds
s_mov_b32 s99, 12544
buffer_load_dwordx4 v212, s[48:51], s99 offen offset:2048 lds
s_mov_b32 s99, 18688
buffer_load_dwordx4 v213, s[48:51], s99 offen offset:3072 lds
s_add_u32 m0, s68, 4096
s_mov_b32 s99, 384
buffer_load_dwordx4 v212, s[48:51], s99 offen offset:0 lds
s_mov_b32 s99, 6528
buffer_load_dwordx4 v213, s[48:51], s99 offen offset:1024 lds
s_mov_b32 s99, 12672
buffer_load_dwordx4 v212, s[48:51], s99 offen offset:2048 lds
s_mov_b32 s99, 18816
buffer_load_dwordx4 v213, s[48:51], s99 offen offset:3072 lds
v_mov_b32 v0, 0x3f803f80
v_lshrrev_b32 v1, 4, v236
v_and_b32 v1, 48, v1
s_add_u32 s28, s68, 65536
v_add_u32 v1, s28, v1
v_accvgpr_write_b32 a40, v0
v_accvgpr_write_b32 a41, v0
v_accvgpr_write_b32 a42, v0
v_accvgpr_write_b32 a43, v0
v_accvgpr_write_b32 a44, v0
v_accvgpr_write_b32 a45, v0
v_accvgpr_write_b32 a46, v0
v_accvgpr_write_b32 a47, v0
v_accvgpr_write_b32 a48, v0
v_accvgpr_write_b32 a49, v0
v_accvgpr_write_b32 a50, v0
v_accvgpr_write_b32 a51, v0
v_accvgpr_write_b32 a52, v0
v_accvgpr_write_b32 a53, v0
v_accvgpr_write_b32 a54, v0
v_accvgpr_write_b32 a55, v0
v_accvgpr_write_b32 a56, v0
v_accvgpr_write_b32 a57, v0
v_accvgpr_write_b32 a58, v0
v_accvgpr_write_b32 a59, v0
v_accvgpr_write_b32 a60, v0
v_accvgpr_write_b32 a61, v0
v_accvgpr_write_b32 a62, v0
v_accvgpr_write_b32 a63, v0
v_accvgpr_write_b32 a64, v0
v_accvgpr_write_b32 a65, v0
v_accvgpr_write_b32 a66, v0
v_accvgpr_write_b32 a67, v0
v_accvgpr_write_b32 a68, v0
v_accvgpr_write_b32 a69, v0
v_accvgpr_write_b32 a70, v0
v_accvgpr_write_b32 a71, v0
v_accvgpr_write_b32 a72, v0
v_accvgpr_write_b32 a73, v0
v_accvgpr_write_b32 a74, v0
v_accvgpr_write_b32 a75, v0
v_accvgpr_write_b32 a76, v0
v_accvgpr_write_b32 a77, v0
v_accvgpr_write_b32 a78, v0
v_accvgpr_write_b32 a79, v0
v_accvgpr_write_b32 a80, v0
v_accvgpr_write_b32 a81, v0
v_accvgpr_write_b32 a82, v0
v_accvgpr_write_b32 a83, v0
v_accvgpr_write_b32 a84, v0
v_accvgpr_write_b32 a85, v0
v_accvgpr_write_b32 a86, v0
v_accvgpr_write_b32 a87, v0
v_accvgpr_write_b32 a88, v0
v_accvgpr_write_b32 a89, v0
v_accvgpr_write_b32 a90, v0
v_accvgpr_write_b32 a91, v0
v_accvgpr_write_b32 a92, v0
v_accvgpr_write_b32 a93, v0
v_accvgpr_write_b32 a94, v0
v_accvgpr_write_b32 a95, v0
v_accvgpr_write_b32 a96, v0
v_accvgpr_write_b32 a97, v0
v_accvgpr_write_b32 a98, v0
v_accvgpr_write_b32 a99, v0
s_waitcnt vmcnt(24)
ds_read_b32 v239, v1 offset:1024
s_mov_b32 exec_lo, 0x22222222
s_mov_b32 exec_hi, 0x22222222
ds_read_b128 a[44:47], v1 offset:0
ds_read_b128 a[48:51], v1 offset:64
ds_read_b128 a[52:55], v1 offset:128
ds_read_b128 a[56:59], v1 offset:192
ds_read_b128 a[60:63], v1 offset:256
ds_read_b128 a[64:67], v1 offset:320
ds_read_b128 a[68:71], v1 offset:384
ds_read_b128 a[72:75], v1 offset:448
ds_read_b128 a[76:79], v1 offset:512
ds_read_b128 a[80:83], v1 offset:576
ds_read_b128 a[84:87], v1 offset:640
ds_read_b128 a[88:91], v1 offset:704
ds_read_b128 a[92:95], v1 offset:768
ds_read_b128 a[96:99], v1 offset:832
s_mov_b64 exec, -1
s_waitcnt vmcnt(8)
s_waitcnt lgkmcnt(0)
s_barrier
v_readfirstlane_b32 s29, v239
ds_read_b128 v[80:83], v214 offset:0
ds_read_b128 v[84:87], v214 offset:2048
ds_read_b64_tr_b16 v[32:33], v208 offset:0
ds_read_b64_tr_b16 v[34:35], v208 offset:2048
ds_read_b64_tr_b16 v[36:37], v209 offset:0
ds_read_b64_tr_b16 v[38:39], v209 offset:2048
ds_read_b128 v[88:91], v215 offset:0
ds_read_b128 v[92:95], v215 offset:2048
ds_read_b64_tr_b16 v[40:41], v208 offset:4096
ds_read_b64_tr_b16 v[42:43], v208 offset:6144
ds_read_b64_tr_b16 v[44:45], v209 offset:4096
ds_read_b64_tr_b16 v[46:47], v209 offset:6144
s_mul_i32 s29, s29, 0x9e3779b1
s_add_u32 s29, s29, 0x7f4a7c15
s_waitcnt lgkmcnt(8)
v_mfma_f32_16x16x32_bf16 a[16:19], v[32:35], a[40:43], 0
s_add_u32 m0, s68, 98304
s_mul_i32 s99, s72, 256
s_sub_u32 s99, s99, 0
buffer_load_dwordx4 v210, s[56:59], s99 offen offset:0 lds
s_waitcnt lgkmcnt(6)
v_mfma_f32_16x16x32_bf16 a[20:23], v[36:39], a[40:43], 0
v_accvgpr_write_b32 a100, 0
v_accvgpr_write_b32 a101, 0
v_mfma_f32_16x16x32_bf16 a[24:27], a[44:47], v[80:83], 0
ds_read_b128 v[96:99], v214 offset:4096
ds_read_b128 v[100:103], v214 offset:6144
s_mul_i32 s99, s72, 272
s_sub_u32 s99, s99, 1024
buffer_load_dwordx4 v210, s[56:59], s99 offen offset:1024 lds
v_mfma_f32_16x16x32_bf16 a[28:31], a[44:47], v[84:87], 0
v_accvgpr_write_b32 a102, 0
v_accvgpr_write_b32 a103, 0
v_mfma_f32_16x16x32_bf16 a[32:35], v[80:83], v[80:83], 0
ds_read_b64_tr_b16 v[48:49], v208 offset:8192
ds_read_b64_tr_b16 v[50:51], v208 offset:10240
v_mfma_f32_16x16x32_bf16 a[36:39], v[84:87], v[84:87], 0
s_mul_i32 s99, s72, 260
s_sub_u32 s99, s99, 2048
buffer_load_dwordx4 v210, s[56:59], s99 offen offset:2048 lds
v_mfma_f32_16x16x32_bf16 a[0:3], v[32:35], v[80:83], 0
ds_read_b64_tr_b16 v[52:53], v209 offset:8192
ds_read_b64_tr_b16 v[54:55], v209 offset:10240
v_mfma_f32_16x16x32_bf16 a[8:11], v[32:35], v[84:87], 0
v_accvgpr_write_b32 a104, 0
v_accvgpr_write_b32 a105, 0
v_mfma_f32_16x16x32_bf16 a[4:7], v[36:39], v[80:83], 0
s_mul_i32 s99, s72, 276
s_sub_u32 s99, s99, 3072
buffer_load_dwordx4 v210, s[56:59], s99 offen offset:3072 lds
v_mfma_f32_16x16x32_bf16 a[12:15], v[36:39], v[84:87], 0
v_accvgpr_write_b32 a106, 0
v_accvgpr_write_b32 a107, 0
s_waitcnt lgkmcnt(8)
v_mfma_f32_16x16x32_bf16 a[16:19], v[40:43], a[40:43], a[16:19]
v_accvgpr_write_b32 a108, 0
v_accvgpr_write_b32 a109, 0
s_waitcnt lgkmcnt(6)
v_mfma_f32_16x16x32_bf16 a[20:23], v[44:47], a[40:43], a[20:23]
s_add_u32 m0, s68, 102400
s_mul_i32 s99, s72, 288
s_sub_u32 s99, s99, 0
buffer_load_dwordx4 v210, s[56:59], s99 offen offset:0 lds
v_mfma_f32_16x16x32_bf16 a[24:27], a[48:51], v[88:91], a[24:27]
ds_read_b128 v[104:107], v215 offset:4096
ds_read_b128 v[108:111], v215 offset:6144
v_mfma_f32_16x16x32_bf16 a[28:31], a[48:51], v[92:95], a[28:31]
v_accvgpr_write_b32 a110, 0
v_accvgpr_write_b32 a111, 0
v_mfma_f32_16x16x32_bf16 a[32:35], v[88:91], v[88:91], a[32:35]
ds_read_b64_tr_b16 v[56:57], v208 offset:12288
ds_read_b64_tr_b16 v[58:59], v208 offset:14336
s_mul_i32 s99, s72, 304
s_sub_u32 s99, s99, 1024
buffer_load_dwordx4 v210, s[56:59], s99 offen offset:1024 lds
)ASM"
R"ASM(
v_mfma_f32_16x16x32_bf16 a[36:39], v[92:95], v[92:95], a[36:39]
v_accvgpr_write_b32 a112, 0
v_accvgpr_write_b32 a113, 0
v_mfma_f32_16x16x32_bf16 a[0:3], v[40:43], v[88:91], a[0:3]
ds_read_b64_tr_b16 v[60:61], v209 offset:12288
ds_read_b64_tr_b16 v[62:63], v209 offset:14336
s_mul_i32 s99, s72, 292
s_sub_u32 s99, s99, 2048
buffer_load_dwordx4 v210, s[56:59], s99 offen offset:2048 lds
v_mfma_f32_16x16x32_bf16 a[8:11], v[40:43], v[92:95], a[8:11]
v_accvgpr_write_b32 a114, 0
v_accvgpr_write_b32 a115, 0
v_mfma_f32_16x16x32_bf16 a[4:7], v[44:47], v[88:91], a[4:7]
v_accvgpr_write_b32 a116, 0
v_accvgpr_write_b32 a117, 0
v_mfma_f32_16x16x32_bf16 a[12:15], v[44:47], v[92:95], a[12:15]
s_mul_i32 s99, s72, 308
s_sub_u32 s99, s99, 3072
buffer_load_dwordx4 v210, s[56:59], s99 offen offset:3072 lds
s_waitcnt lgkmcnt(8)
v_mfma_f32_16x16x32_bf16 a[16:19], v[48:51], a[40:43], a[16:19]
v_accvgpr_write_b32 a118, 0
v_accvgpr_write_b32 a119, 0
s_waitcnt lgkmcnt(6)
v_mfma_f32_16x16x32_bf16 a[20:23], v[52:55], a[40:43], a[20:23]
v_accvgpr_write_b32 a120, 0
v_accvgpr_write_b32 a121, 0
v_mfma_f32_16x16x32_bf16 a[24:27], a[52:55], v[96:99], a[24:27]
s_waitcnt vmcnt(8)
v_add_u32 v214, -131072, v214
v_add_u32 v215, -131072, v215
ds_read_b128 v[112:115], v214 offset:0
ds_read_b128 v[116:119], v214 offset:2048
s_add_u32 m0, s68, 65536
s_mov_b32 s99, 512
buffer_load_dwordx4 v212, s[48:51], s99 offen offset:0 lds
v_mfma_f32_16x16x32_bf16 a[28:31], a[52:55], v[100:103], a[28:31]
v_accvgpr_write_b32 a122, 0
v_accvgpr_write_b32 a123, 0
v_mfma_f32_16x16x32_bf16 a[32:35], v[96:99], v[96:99], a[32:35]
ds_read_b64_tr_b16 v[32:33], v208 offset:16384
ds_read_b64_tr_b16 v[34:35], v208 offset:18432
v_mfma_f32_16x16x32_bf16 a[36:39], v[100:103], v[100:103], a[36:39]
s_mov_b32 s99, 6656
buffer_load_dwordx4 v213, s[48:51], s99 offen offset:1024 lds
v_mfma_f32_16x16x32_bf16 a[0:3], v[48:51], v[96:99], a[0:3]
ds_read_b64_tr_b16 v[36:37], v209 offset:16384
ds_read_b64_tr_b16 v[38:39], v209 offset:18432
v_mfma_f32_16x16x32_bf16 a[8:11], v[48:51], v[100:103], a[8:11]
v_accvgpr_write_b32 a124, 0
v_accvgpr_write_b32 a125, 0
v_mfma_f32_16x16x32_bf16 a[4:7], v[52:55], v[96:99], a[4:7]
s_mov_b32 s99, 12800
buffer_load_dwordx4 v212, s[48:51], s99 offen offset:2048 lds
v_mfma_f32_16x16x32_bf16 a[12:15], v[52:55], v[100:103], a[12:15]
v_accvgpr_write_b32 a126, 0
v_accvgpr_write_b32 a127, 0
s_waitcnt lgkmcnt(8)
v_mfma_f32_16x16x32_bf16 a[16:19], v[56:59], a[40:43], a[16:19]
s_mov_b32 s99, 18944
buffer_load_dwordx4 v213, s[48:51], s99 offen offset:3072 lds
s_waitcnt lgkmcnt(6)
v_mfma_f32_16x16x32_bf16 a[20:23], v[60:63], a[40:43], a[20:23]
v_accvgpr_write_b32 a128, 0
v_accvgpr_write_b32 a129, 0
v_mfma_f32_16x16x32_bf16 a[24:27], a[56:59], v[104:107], a[24:27]
ds_read_b128 v[120:123], v215 offset:0
ds_read_b128 v[124:127], v215 offset:2048
v_mfma_f32_16x16x32_bf16 a[28:31], a[56:59], v[108:111], a[28:31]
s_add_u32 m0, s68, 69632
s_mov_b32 s99, 640
buffer_load_dwordx4 v212, s[48:51], s99 offen offset:0 lds
v_mfma_f32_16x16x32_bf16 a[32:35], v[104:107], v[104:107], a[32:35]
ds_read_b64_tr_b16 v[40:41], v208 offset:20480
ds_read_b64_tr_b16 v[42:43], v208 offset:22528
v_mfma_f32_16x16x32_bf16 a[36:39], v[108:111], v[108:111], a[36:39]
v_accvgpr_write_b32 a130, 0
v_accvgpr_write_b32 a131, 0
v_mfma_f32_16x16x32_bf16 a[0:3], v[56:59], v[104:107], a[0:3]
ds_read_b64_tr_b16 v[44:45], v209 offset:20480
ds_read_b64_tr_b16 v[46:47], v209 offset:22528
s_mov_b32 s99, 6784
buffer_load_dwordx4 v213, s[48:51], s99 offen offset:1024 lds
v_mfma_f32_16x16x32_bf16 a[8:11], v[56:59], v[108:111], a[8:11]
v_accvgpr_write_b32 a132, 0
v_accvgpr_write_b32 a133, 0
v_mfma_f32_16x16x32_bf16 a[4:7], v[60:63], v[104:107], a[4:7]
v_accvgpr_write_b32 a134, 0
v_accvgpr_write_b32 a135, 0
v_mfma_f32_16x16x32_bf16 a[12:15], v[60:63], v[108:111], a[12:15]
s_mov_b32 s99, 12928
buffer_load_dwordx4 v212, s[48:51], s99 offen offset:2048 lds
s_waitcnt lgkmcnt(8)
v_mfma_f32_16x16x32_bf16 a[16:19], v[32:35], a[40:43], a[16:19]
v_accvgpr_write_b32 a136, 0
v_accvgpr_write_b32 a137, 0
s_waitcnt lgkmcnt(6)
v_mfma_f32_16x16x32_bf16 a[20:23], v[36:39], a[40:43], a[20:23]
v_accvgpr_write_b32 a138, 0
v_accvgpr_write_b32 a139, 0
v_mfma_f32_16x16x32_bf16 a[24:27], a[60:63], v[112:115], a[24:27]
ds_read_b128 v[128:131], v214 offset:4096
ds_read_b128 v[132:135], v214 offset:6144
s_mov_b32 s99, 19072
buffer_load_dwordx4 v213, s[48:51], s99 offen offset:3072 lds
v_mfma_f32_16x16x32_bf16 a[28:31], a[60:63], v[116:119], a[28:31]
v_accvgpr_write_b32 a140, 0
v_accvgpr_write_b32 a141, 0
v_mfma_f32_16x16x32_bf16 a[32:35], v[112:115], v[112:115], a[32:35]
ds_read_b64_tr_b16 v[48:49], v208 offset:24576
ds_read_b64_tr_b16 v[50:51], v208 offset:26624
s_add_u32 m0, s68, 131072
s_mov_b32 s99, 768
buffer_load_dwordx4 v212, s[48:51], s99 offen offset:0 lds
v_mfma_f32_16x16x32_bf16 a[36:39], v[116:119], v[116:119], a[36:39]
v_accvgpr_write_b32 a142, 0
v_accvgpr_write_b32 a143, 0
v_mfma_f32_16x16x32_bf16 a[0:3], v[32:35], v[112:115], a[0:3]
ds_read_b64_tr_b16 v[52:53], v209 offset:24576
ds_read_b64_tr_b16 v[54:55], v209 offset:26624
v_mfma_f32_16x16x32_bf16 a[8:11], v[32:35], v[116:119], a[8:11]
s_mov_b32 s99, 6912
buffer_load_dwordx4 v213, s[48:51], s99 offen offset:1024 lds
v_mfma_f32_16x16x32_bf16 a[4:7], v[36:39], v[112:115], a[4:7]
v_accvgpr_write_b32 a144, 0
v_accvgpr_write_b32 a145, 0
v_mfma_f32_16x16x32_bf16 a[12:15], v[36:39], v[116:119], a[12:15]
v_accvgpr_write_b32 a146, 0
v_accvgpr_write_b32 a147, 0
s_waitcnt lgkmcnt(8)
v_mfma_f32_16x16x32_bf16 a[16:19], v[40:43], a[40:43], a[16:19]
s_mov_b32 s99, 13056
buffer_load_dwordx4 v212, s[48:51], s99 offen offset:2048 lds
s_waitcnt lgkmcnt(6)
v_mfma_f32_16x16x32_bf16 a[20:23], v[44:47], a[40:43], a[20:23]
v_accvgpr_write_b32 a148, 0
v_accvgpr_write_b32 a149, 0
v_mfma_f32_16x16x32_bf16 a[24:27], a[64:67], v[120:123], a[24:27]
ds_read_b128 v[136:139], v215 offset:4096
ds_read_b128 v[140:143], v215 offset:6144
v_mfma_f32_16x16x32_bf16 a[28:31], a[64:67], v[124:127], a[28:31]
s_mov_b32 s99, 19200
buffer_load_dwordx4 v213, s[48:51], s99 offen offset:3072 lds
v_mfma_f32_16x16x32_bf16 a[32:35], v[120:123], v[120:123], a[32:35]
ds_read_b64_tr_b16 v[56:57], v208 offset:28672
ds_read_b64_tr_b16 v[58:59], v208 offset:30720
v_mfma_f32_16x16x32_bf16 a[36:39], v[124:127], v[124:127], a[36:39]
v_accvgpr_write_b32 a150, 0
v_accvgpr_write_b32 a151, 0
v_mfma_f32_16x16x32_bf16 a[0:3], v[40:43], v[120:123], a[0:3]
ds_read_b64_tr_b16 v[60:61], v209 offset:28672
ds_read_b64_tr_b16 v[62:63], v209 offset:30720
v_mfma_f32_16x16x32_bf16 a[8:11], v[40:43], v[124:127], a[8:11]
v_accvgpr_write_b32 a152, 0
v_accvgpr_write_b32 a153, 0
v_mfma_f32_16x16x32_bf16 a[4:7], v[44:47], v[120:123], a[4:7]
v_accvgpr_write_b32 a154, 0
v_accvgpr_write_b32 a155, 0
v_mfma_f32_16x16x32_bf16 a[12:15], v[44:47], v[124:127], a[12:15]
v_accvgpr_write_b32 a156, 0
v_accvgpr_write_b32 a157, 0
s_waitcnt lgkmcnt(8)
v_mfma_f32_16x16x32_bf16 a[16:19], v[48:51], a[40:43], a[16:19]
v_accvgpr_write_b32 a158, 0
v_accvgpr_write_b32 a159, 0
s_waitcnt lgkmcnt(6)
v_mfma_f32_16x16x32_bf16 a[20:23], v[52:55], a[40:43], a[20:23]
v_accvgpr_write_b32 a160, 0
v_accvgpr_write_b32 a161, 0
v_mfma_f32_16x16x32_bf16 a[24:27], a[68:71], v[128:131], a[24:27]
s_waitcnt vmcnt(4)
v_add_u32 v214, 65536, v214
v_add_u32 v215, 65536, v215
ds_read_b128 v[144:147], v214 offset:0
ds_read_b128 v[148:151], v214 offset:2048
v_mfma_f32_16x16x32_bf16 a[28:31], a[68:71], v[132:135], a[28:31]
v_accvgpr_write_b32 a162, 0
v_accvgpr_write_b32 a163, 0
v_mfma_f32_16x16x32_bf16 a[32:35], v[128:131], v[128:131], a[32:35]
s_barrier
v_add_u32 v208, 65536, v208
v_add_u32 v209, 65536, v209
ds_read_b64_tr_b16 v[32:33], v208 offset:0
ds_read_b64_tr_b16 v[34:35], v208 offset:2048
v_mfma_f32_16x16x32_bf16 a[36:39], v[132:135], v[132:135], a[36:39]
v_accvgpr_write_b32 a164, 0
v_accvgpr_write_b32 a165, 0
v_mfma_f32_16x16x32_bf16 a[0:3], v[48:51], v[128:131], a[0:3]
ds_read_b64_tr_b16 v[36:37], v209 offset:0
ds_read_b64_tr_b16 v[38:39], v209 offset:2048
v_mfma_f32_16x16x32_bf16 a[8:11], v[48:51], v[132:135], a[8:11]
v_accvgpr_write_b32 a166, 0
v_accvgpr_write_b32 a167, 0
v_mfma_f32_16x16x32_bf16 a[4:7], v[52:55], v[128:131], a[4:7]
v_accvgpr_write_b32 a168, 0
v_accvgpr_write_b32 a169, 0
v_mfma_f32_16x16x32_bf16 a[12:15], v[52:55], v[132:135], a[12:15]
v_accvgpr_write_b32 a170, 0
v_accvgpr_write_b32 a171, 0
s_waitcnt lgkmcnt(8)
)ASM"
R"ASM(
v_mfma_f32_16x16x32_bf16 a[16:19], v[56:59], a[40:43], a[16:19]
v_accvgpr_write_b32 a172, 0
v_accvgpr_write_b32 a173, 0
s_waitcnt lgkmcnt(6)
v_mfma_f32_16x16x32_bf16 a[20:23], v[60:63], a[40:43], a[20:23]
v_accvgpr_write_b32 a174, 0
v_accvgpr_write_b32 a175, 0
v_mfma_f32_16x16x32_bf16 a[24:27], a[72:75], v[136:139], a[24:27]
ds_read_b128 v[152:155], v215 offset:0
ds_read_b128 v[156:159], v215 offset:2048
s_add_u32 m0, s27, s68
s_nop 0
buffer_load_dwordx4 v221, s[40:43], s4 offen offset:0 lds
v_mfma_f32_16x16x32_bf16 a[28:31], a[72:75], v[140:143], a[28:31]
v_accvgpr_write_b32 a176, 0
v_accvgpr_write_b32 a177, 0
v_mfma_f32_16x16x32_bf16 a[32:35], v[136:139], v[136:139], a[32:35]
ds_read_b64_tr_b16 v[40:41], v208 offset:4096
ds_read_b64_tr_b16 v[42:43], v208 offset:6144
v_mfma_f32_16x16x32_bf16 a[36:39], v[140:143], v[140:143], a[36:39]
v_accvgpr_write_b32 a178, 0
v_accvgpr_write_b32 a179, 0
v_mfma_f32_16x16x32_bf16 a[0:3], v[56:59], v[136:139], a[0:3]
ds_read_b64_tr_b16 v[44:45], v209 offset:4096
ds_read_b64_tr_b16 v[46:47], v209 offset:6144
buffer_load_dwordx4 v222, s[40:43], s5 offen offset:1024 lds
v_mfma_f32_16x16x32_bf16 a[8:11], v[56:59], v[140:143], a[8:11]
v_accvgpr_write_b32 a180, 0
v_accvgpr_write_b32 a181, 0
v_mfma_f32_16x16x32_bf16 a[4:7], v[60:63], v[136:139], a[4:7]
v_accvgpr_write_b32 a182, 0
v_accvgpr_write_b32 a183, 0
v_mfma_f32_16x16x32_bf16 a[12:15], v[60:63], v[140:143], a[12:15]
v_accvgpr_write_b32 a184, 0
v_accvgpr_write_b32 a185, 0
s_waitcnt lgkmcnt(8)
v_mfma_f32_16x16x32_bf16 a[16:19], v[32:35], a[40:43], a[16:19]
buffer_load_dwordx4 v221, s[40:43], s6 offen offset:2048 lds
s_waitcnt lgkmcnt(6)
v_mfma_f32_16x16x32_bf16 a[20:23], v[36:39], a[40:43], a[20:23]
v_accvgpr_write_b32 a186, 0
v_accvgpr_write_b32 a187, 0
v_mfma_f32_16x16x32_bf16 a[24:27], a[76:79], v[144:147], a[24:27]
ds_read_b128 v[160:163], v214 offset:4096
ds_read_b128 v[164:167], v214 offset:6144
v_mfma_f32_16x16x32_bf16 a[28:31], a[76:79], v[148:151], a[28:31]
v_accvgpr_write_b32 a188, 0
v_accvgpr_write_b32 a189, 0
v_mfma_f32_16x16x32_bf16 a[32:35], v[144:147], v[144:147], a[32:35]
ds_read_b64_tr_b16 v[48:49], v208 offset:8192
ds_read_b64_tr_b16 v[50:51], v208 offset:10240
buffer_load_dwordx4 v222, s[40:43], s7 offen offset:3072 lds
v_mfma_f32_16x16x32_bf16 a[36:39], v[148:151], v[148:151], a[36:39]
v_accvgpr_write_b32 a190, 0
v_accvgpr_write_b32 a191, 0
v_mfma_f32_16x16x32_bf16 a[0:3], v[32:35], v[144:147], a[0:3]
ds_read_b64_tr_b16 v[52:53], v209 offset:8192
ds_read_b64_tr_b16 v[54:55], v209 offset:10240
v_mfma_f32_16x16x32_bf16 a[8:11], v[32:35], v[148:151], a[8:11]
v_accvgpr_write_b32 a192, 0
v_accvgpr_write_b32 a193, 0
v_mfma_f32_16x16x32_bf16 a[4:7], v[36:39], v[144:147], a[4:7]
s_add_u32 m0, m0, 4096
s_nop 0
buffer_load_dwordx4 v221, s[40:43], s8 offen offset:0 lds
v_mfma_f32_16x16x32_bf16 a[12:15], v[36:39], v[148:151], a[12:15]
v_accvgpr_write_b32 a194, 0
v_accvgpr_write_b32 a195, 0
s_waitcnt lgkmcnt(8)
v_mfma_f32_16x16x32_bf16 a[16:19], v[40:43], a[40:43], a[16:19]
v_accvgpr_write_b32 a196, 0
v_accvgpr_write_b32 a197, 0
s_waitcnt lgkmcnt(6)
v_mfma_f32_16x16x32_bf16 a[20:23], v[44:47], a[40:43], a[20:23]
v_accvgpr_write_b32 a198, 0
v_accvgpr_write_b32 a199, 0
v_mfma_f32_16x16x32_bf16 a[24:27], a[80:83], v[152:155], a[24:27]
ds_read_b128 v[168:171], v215 offset:4096
ds_read_b128 v[172:175], v215 offset:6144
v_mfma_f32_16x16x32_bf16 a[28:31], a[80:83], v[156:159], a[28:31]
buffer_load_dwordx4 v222, s[40:43], s9 offen offset:1024 lds
v_mfma_f32_16x16x32_bf16 a[32:35], v[152:155], v[152:155], a[32:35]
ds_read_b64_tr_b16 v[56:57], v208 offset:12288
ds_read_b64_tr_b16 v[58:59], v208 offset:14336
v_mfma_f32_16x16x32_bf16 a[36:39], v[156:159], v[156:159], a[36:39]
v_accvgpr_write_b32 a200, 0
v_accvgpr_write_b32 a201, 0
v_mfma_f32_16x16x32_bf16 a[0:3], v[40:43], v[152:155], a[0:3]
ds_read_b64_tr_b16 v[60:61], v209 offset:12288
ds_read_b64_tr_b16 v[62:63], v209 offset:14336
v_mfma_f32_16x16x32_bf16 a[8:11], v[40:43], v[156:159], a[8:11]
buffer_load_dwordx4 v221, s[40:43], s10 offen offset:2048 lds
v_mfma_f32_16x16x32_bf16 a[4:7], v[44:47], v[152:155], a[4:7]
v_accvgpr_write_b32 a202, 0
v_accvgpr_write_b32 a203, 0
v_mfma_f32_16x16x32_bf16 a[12:15], v[44:47], v[156:159], a[12:15]
v_accvgpr_write_b32 a204, 0
v_accvgpr_write_b32 a205, 0
s_waitcnt lgkmcnt(8)
v_mfma_f32_16x16x32_bf16 a[16:19], v[48:51], a[40:43], a[16:19]
v_accvgpr_write_b32 a206, 0
v_accvgpr_write_b32 a207, 0
s_waitcnt lgkmcnt(6)
v_mfma_f32_16x16x32_bf16 a[20:23], v[52:55], a[40:43], a[20:23]
buffer_load_dwordx4 v222, s[40:43], s11 offen offset:3072 lds
v_mfma_f32_16x16x32_bf16 a[24:27], a[84:87], v[160:163], a[24:27]
s_waitcnt vmcnt(8)
v_add_u32 v214, 65536, v214
v_add_u32 v215, 65536, v215
ds_read_b128 v[176:179], v214 offset:0
ds_read_b128 v[180:183], v214 offset:2048
v_mfma_f32_16x16x32_bf16 a[28:31], a[84:87], v[164:167], a[28:31]
v_accvgpr_write_b32 a208, 0
v_accvgpr_write_b32 a209, 0
v_mfma_f32_16x16x32_bf16 a[32:35], v[160:163], v[160:163], a[32:35]
ds_read_b64_tr_b16 v[32:33], v208 offset:16384
ds_read_b64_tr_b16 v[34:35], v208 offset:18432
v_mfma_f32_16x16x32_bf16 a[36:39], v[164:167], v[164:167], a[36:39]
v_add_u32 v221, 128, v221
v_add_u32 v222, 128, v222
v_mfma_f32_16x16x32_bf16 a[0:3], v[48:51], v[160:163], a[0:3]
ds_read_b64_tr_b16 v[36:37], v209 offset:16384
ds_read_b64_tr_b16 v[38:39], v209 offset:18432
v_mfma_f32_16x16x32_bf16 a[8:11], v[48:51], v[164:167], a[8:11]
v_accvgpr_write_b32 a210, 0
v_accvgpr_write_b32 a211, 0
v_mfma_f32_16x16x32_bf16 a[4:7], v[52:55], v[160:163], a[4:7]
v_accvgpr_write_b32 a212, 0
v_accvgpr_write_b32 a213, 0
v_mfma_f32_16x16x32_bf16 a[12:15], v[52:55], v[164:167], a[12:15]
v_accvgpr_write_b32 a214, 0
v_accvgpr_write_b32 a215, 0
s_waitcnt lgkmcnt(8)
v_mfma_f32_16x16x32_bf16 a[16:19], v[56:59], a[40:43], a[16:19]
v_accvgpr_write_b32 a216, 0
v_accvgpr_write_b32 a217, 0
s_waitcnt lgkmcnt(6)
v_mfma_f32_16x16x32_bf16 a[20:23], v[60:63], a[40:43], a[20:23]
v_accvgpr_write_b32 a218, 0
v_accvgpr_write_b32 a219, 0
v_mfma_f32_16x16x32_bf16 a[24:27], a[88:91], v[168:171], a[24:27]
ds_read_b128 v[184:187], v215 offset:0
ds_read_b128 v[188:191], v215 offset:2048
s_add_u32 m0, s27, s68
s_add_u32 m0, m0, 65536
s_nop 0
buffer_load_dwordx4 v223, s[44:47], s12 offen offset:0 lds
v_mfma_f32_16x16x32_bf16 a[28:31], a[88:91], v[172:175], a[28:31]
v_accvgpr_write_b32 a220, 0
v_accvgpr_write_b32 a221, 0
v_mfma_f32_16x16x32_bf16 a[32:35], v[168:171], v[168:171], a[32:35]
ds_read_b64_tr_b16 v[40:41], v208 offset:20480
ds_read_b64_tr_b16 v[42:43], v208 offset:22528
buffer_load_dwordx4 v223, s[44:47], s13 offen offset:1024 lds
v_mfma_f32_16x16x32_bf16 a[36:39], v[172:175], v[172:175], a[36:39]
v_accvgpr_write_b32 a222, 0
v_accvgpr_write_b32 a223, 0
v_mfma_f32_16x16x32_bf16 a[0:3], v[56:59], v[168:171], a[0:3]
ds_read_b64_tr_b16 v[44:45], v209 offset:20480
ds_read_b64_tr_b16 v[46:47], v209 offset:22528
buffer_load_dwordx4 v223, s[44:47], s14 offen offset:2048 lds
v_mfma_f32_16x16x32_bf16 a[8:11], v[56:59], v[172:175], a[8:11]
v_accvgpr_write_b32 a224, 0
v_accvgpr_write_b32 a225, 0
v_mfma_f32_16x16x32_bf16 a[4:7], v[60:63], v[168:171], a[4:7]
buffer_load_dwordx4 v223, s[44:47], s15 offen offset:3072 lds
v_mfma_f32_16x16x32_bf16 a[12:15], v[60:63], v[172:175], a[12:15]
v_accvgpr_write_b32 a226, 0
v_accvgpr_write_b32 a227, 0
s_waitcnt lgkmcnt(8)
v_mfma_f32_16x16x32_bf16 a[16:19], v[32:35], a[40:43], a[16:19]
s_add_u32 m0, m0, 4096
s_nop 0
buffer_load_dwordx4 v223, s[44:47], s16 offen offset:0 lds
s_waitcnt lgkmcnt(6)
v_mfma_f32_16x16x32_bf16 a[20:23], v[36:39], a[40:43], a[20:23]
v_accvgpr_write_b32 a228, 0
v_accvgpr_write_b32 a229, 0
v_mfma_f32_16x16x32_bf16 a[24:27], a[92:95], v[176:179], a[24:27]
v_accvgpr_write_b32 a230, 0
v_accvgpr_write_b32 a231, 0
v_mfma_f32_16x16x32_bf16 a[28:31], a[92:95], v[180:183], a[28:31]
buffer_load_dwordx4 v223, s[44:47], s17 offen offset:1024 lds
v_mfma_f32_16x16x32_bf16 a[32:35], v[176:179], v[176:179], a[32:35]
v_accvgpr_write_b32 a232, 0
v_accvgpr_write_b32 a233, 0
v_accvgpr_write_b32 a234, 0
v_mfma_f32_16x16x32_bf16 a[36:39], v[180:183], v[180:183], a[36:39]
buffer_load_dwordx4 v223, s[44:47], s18 offen offset:2048 lds
v_accvgpr_write_b32 a235, 0
v_mfma_f32_16x16x32_bf16 a[0:3], v[32:35], v[176:179], a[0:3]
v_accvgpr_write_b32 a236, 0
v_accvgpr_write_b32 a237, 0
v_accvgpr_write_b32 a238, 0
v_mfma_f32_16x16x32_bf16 a[8:11], v[32:35], v[180:183], a[8:11]
buffer_load_dwordx4 v223, s[44:47], s19 offen offset:3072 lds
v_accvgpr_write_b32 a239, 0
v_mfma_f32_16x16x32_bf16 a[4:7], v[36:39], v[176:179], a[4:7]
v_accvgpr_write_b32 a240, 0
v_accvgpr_write_b32 a241, 0
)ASM"
R"ASM(
v_accvgpr_write_b32 a242, 0
v_mfma_f32_16x16x32_bf16 a[12:15], v[36:39], v[180:183], a[12:15]
v_add_u32 v223, s96, v223
s_xor_b32 s27, s27, 32768
v_accvgpr_write_b32 a243, 0
s_waitcnt lgkmcnt(2)
v_mfma_f32_16x16x32_bf16 a[16:19], v[40:43], a[40:43], a[16:19]
v_accvgpr_write_b32 a244, 0
v_accvgpr_write_b32 a245, 0
v_accvgpr_write_b32 a246, 0
s_waitcnt lgkmcnt(0)
v_mfma_f32_16x16x32_bf16 a[20:23], v[44:47], a[40:43], a[20:23]
v_accvgpr_write_b32 a247, 0
v_accvgpr_write_b32 a248, 0
v_accvgpr_write_b32 a249, 0
v_mfma_f32_16x16x32_bf16 a[24:27], a[96:99], v[184:187], a[24:27]
v_lshrrev_b32 v200, 4, v236
v_and_b32 v201, 15, v200
v_accvgpr_write_b32 a250, 0
v_mfma_f32_16x16x32_bf16 a[28:31], a[96:99], v[188:191], a[28:31]
v_lshrrev_b32 v200, 4, v200
v_mul_u32_u24 v201, 128, v201
v_accvgpr_write_b32 a251, 0
v_mfma_f32_16x16x32_bf16 a[32:35], v[184:187], v[184:187], a[32:35]
v_mul_u32_u24 v200, 2048, v200
v_add_u32 v200, v200, v201
v_accvgpr_write_b32 a252, 0
v_mfma_f32_16x16x32_bf16 a[36:39], v[188:191], v[188:191], a[36:39]
s_bfm_b64 exec, 32, 0
buffer_load_dword v201, v200, s[40:43], s78 offen offset:0 sc1
s_mov_b64 exec, -1
s_add_u32 s73, s73, 524288
v_mfma_f32_16x16x32_bf16 a[0:3], v[40:43], v[184:187], a[0:3]
v_lshrrev_b32 v202, 4, v236
v_and_b32 v203, 3, v202
v_accvgpr_write_b32 a253, 0
v_mfma_f32_16x16x32_bf16 a[8:11], v[40:43], v[188:191], a[8:11]
v_lshrrev_b32 v202, 2, v202
v_mul_u32_u24 v203, 128, v203
v_accvgpr_write_b32 a254, 0
v_mfma_f32_16x16x32_bf16 a[4:7], v[44:47], v[184:187], a[4:7]
v_mul_u32_u24 v202, 8192, v202
v_add_u32 v202, v202, v203
v_accvgpr_write_b32 a255, 0
v_mfma_f32_16x16x32_bf16 a[12:15], v[44:47], v[188:191], a[12:15]
s_bfm_b64 exec, 8, 0
buffer_load_dword v203, v202, s[44:47], s73 offen offset:0 sc1
s_mov_b64 exec, -1
s_getpc_b64 s[92:93]
.Lpc204:
s_sub_u32 s92, s92, .Lpc204-.Lbeg
s_subb_u32 s93, s93, 0
s_mov_b32 s94, .Lend-.Lbeg
s_mov_b32 s95, 0x20000
buffer_load_dwordx4 v[204:207], v236, s[92:95], s79 offen offset:0
s_set_gpr_idx_on s70, 1
s_nop 0
v_mov_b32 v192, v80
v_mov_b32 v193, v81
v_mov_b32 v194, v82
v_mov_b32 v195, v83
v_mov_b32 v196, v84
v_mov_b32 v197, v85
v_mov_b32 v198, v86
v_mov_b32 v199, v87
s_set_gpr_idx_off
buffer_store_dwordx4 v[192:195], v236, s[36:39], s69 offen offset:0 sc1
buffer_store_dwordx4 v[196:199], v236, s[36:39], s69 offen offset:1024 sc1
v_accvgpr_read_b32 v32, a24
v_accvgpr_read_b32 v33, a25
v_accvgpr_read_b32 v34, a26
v_accvgpr_read_b32 v35, a27
v_accvgpr_read_b32 v36, a28
v_accvgpr_read_b32 v37, a29
v_accvgpr_read_b32 v38, a30
v_accvgpr_read_b32 v39, a31
v_accvgpr_read_b32 v40, a32
v_accvgpr_read_b32 v41, a33
v_accvgpr_read_b32 v42, a34
v_accvgpr_read_b32 v43, a35
v_accvgpr_read_b32 v44, a36
v_accvgpr_read_b32 v45, a37
v_accvgpr_read_b32 v46, a38
v_accvgpr_read_b32 v47, a39
v_and_b32 v48, 12, v247
v_lshrrev_b32 v49, 8, v236
v_lshrrev_b32 v50, 4, v247
v_cmp_eq_u32 s[20:21], 4, v48
v_cmp_eq_u32 s[22:23], 8, v48
v_cmp_eq_u32 s[30:31], 12, v48
v_cmp_eq_u32 vcc, v49, v50
v_mul_f32 v240, 0xbfb8aa3b, v33
v_exp_f32 v240, v240
v_cndmask_b32 v40, v40, v41, s[20:21]
v_cndmask_b32 v40, v40, v42, s[22:23]
v_cndmask_b32 v40, v40, v43, s[30:31]
v_cndmask_b32 v40, 0, v40, vcc
v_add_f32 v240, 1.0, v240
v_rcp_f32 v240, v240
v_mov_b32 v48, v40
s_nop 1
v_permlane32_swap_b32 v48, v40
s_nop 1
v_add_f32 v40, v40, v48
v_mov_b32 v48, v40
s_nop 1
v_permlane16_swap_b32 v48, v40
s_nop 1
v_add_f32 v40, v40, v48
v_mul_f32 v32, 0xbb124925, v32
v_mul_f32 v40, 0x3b124925, v40
v_fma_f32 v40, -v32, v32, v40
v_max_f32 v40, 0, v40
v_add_f32 v40, 0x3727c5ac, v40
v_rsq_f32 v40, v40
s_nop 0
v_mul_f32 v33, 0xc0135761, v40
v_mul_f32 v34, v40, v40
v_mul_f32 v34, v34, v40
v_mul_f32 v34, 0xbdd2d3e8, v34
v_rcp_f32 v35, v40
s_nop 0
v_sub_f32 v40, 1.0, v240
buffer_store_dword v40, v247, s[36:39], s86 offen offset:0
v_mul_f32 v241, 0xbfb8aa3b, v37
v_exp_f32 v241, v241
v_cndmask_b32 v44, v44, v45, s[20:21]
v_cndmask_b32 v44, v44, v46, s[22:23]
v_cndmask_b32 v44, v44, v47, s[30:31]
v_cndmask_b32 v44, 0, v44, vcc
v_add_f32 v241, 1.0, v241
v_rcp_f32 v241, v241
v_mov_b32 v48, v44
s_nop 1
v_permlane32_swap_b32 v48, v44
s_nop 1
v_add_f32 v44, v44, v48
v_mov_b32 v48, v44
s_nop 1
v_permlane16_swap_b32 v48, v44
s_nop 1
v_add_f32 v44, v44, v48
v_mul_f32 v36, 0xbb124925, v36
v_mul_f32 v44, 0x3b124925, v44
v_fma_f32 v44, -v36, v36, v44
v_max_f32 v44, 0, v44
v_add_f32 v44, 0x3727c5ac, v44
v_rsq_f32 v44, v44
s_nop 0
v_mul_f32 v37, 0xc0135761, v44
v_mul_f32 v38, v44, v44
v_mul_f32 v38, v38, v44
v_mul_f32 v38, 0xbdd2d3e8, v38
v_rcp_f32 v39, v44
s_nop 0
v_sub_f32 v44, 1.0, v241
buffer_store_dword v44, v247, s[36:39], s86 offen offset:64
v_accvgpr_read_b32 v208, a16
v_accvgpr_read_b32 v209, a17
v_accvgpr_read_b32 v210, a18
v_accvgpr_read_b32 v211, a19
v_accvgpr_read_b32 v212, a20
v_accvgpr_read_b32 v213, a21
v_accvgpr_read_b32 v214, a22
v_accvgpr_read_b32 v215, a23
s_cmp_eq_u32 s75, 0
s_cbranch_scc1 .Lspecial
v_accvgpr_read_b32 v48, a0
v_accvgpr_read_b32 v49, a1
v_accvgpr_read_b32 v50, a2
v_accvgpr_read_b32 v51, a3
v_accvgpr_read_b32 v52, a4
v_accvgpr_read_b32 v53, a5
v_accvgpr_read_b32 v54, a6
v_accvgpr_read_b32 v55, a7
v_fmac_f32 v48, v208, v32
v_fmac_f32 v49, v209, v32
v_fmac_f32 v50, v210, v32
v_fmac_f32 v51, v211, v32
v_fmac_f32 v52, v212, v32
v_fmac_f32 v53, v213, v32
v_fmac_f32 v54, v214, v32
v_fmac_f32 v55, v215, v32
v_mul_f32 v56, v48, v48
v_mul_f32 v57, v49, v49
v_mul_f32 v58, v50, v50
v_mul_f32 v59, v51, v51
v_mul_f32 v60, v52, v52
v_mul_f32 v61, v53, v53
v_mul_f32 v62, v54, v54
v_mul_f32 v63, v55, v55
v_fma_f32 v56, v56, v34, v33
v_fma_f32 v57, v57, v34, v33
v_fma_f32 v58, v58, v34, v33
v_fma_f32 v59, v59, v34, v33
v_fma_f32 v60, v60, v34, v33
v_fma_f32 v61, v61, v34, v33
v_fma_f32 v62, v62, v34, v33
v_fma_f32 v63, v63, v34, v33
v_mul_f32 v56, v56, v48
)ASM"
R"ASM(
v_mul_f32 v57, v57, v49
v_mul_f32 v58, v58, v50
v_mul_f32 v59, v59, v51
v_mul_f32 v60, v60, v52
v_mul_f32 v61, v61, v53
v_mul_f32 v62, v62, v54
v_mul_f32 v63, v63, v55
v_exp_f32 v56, v56
v_exp_f32 v57, v57
v_exp_f32 v58, v58
v_exp_f32 v59, v59
v_exp_f32 v60, v60
v_exp_f32 v61, v61
v_exp_f32 v62, v62
v_exp_f32 v63, v63
v_fma_f32 v56, v56, v35, v35
v_fma_f32 v57, v57, v35, v35
v_fma_f32 v58, v58, v35, v35
v_fma_f32 v59, v59, v35, v35
v_fma_f32 v60, v60, v35, v35
v_fma_f32 v61, v61, v35, v35
v_fma_f32 v62, v62, v35, v35
v_fma_f32 v63, v63, v35, v35
v_rcp_f32 v56, v56
v_rcp_f32 v57, v57
v_rcp_f32 v58, v58
v_rcp_f32 v59, v59
v_rcp_f32 v60, v60
v_rcp_f32 v61, v61
v_rcp_f32 v62, v62
v_rcp_f32 v63, v63
v_mul_f32 v48, v48, v56
v_mul_f32 v49, v49, v57
v_mul_f32 v50, v50, v58
v_mul_f32 v51, v51, v59
v_mul_f32 v52, v52, v60
v_mul_f32 v53, v53, v61
v_mul_f32 v54, v54, v62
v_mul_f32 v55, v55, v63
v_cvt_pk_bf16_f32 v40, v48, v49
v_cvt_pk_bf16_f32 v41, v50, v51
v_cvt_pk_bf16_f32 v42, v52, v53
v_cvt_pk_bf16_f32 v43, v54, v55
v_ashrrev_i32 v56, 31, v217
v_and_b32 v217, 0x7fffffff, v217
v_bfi_b32 v40, v56, 0, v40
v_bfi_b32 v41, v56, 0, v41
buffer_store_dwordx2 v[40:41], v217, s[36:39], 0 offen offset:0 sc1
v_ashrrev_i32 v56, 31, v218
v_and_b32 v218, 0x7fffffff, v218
v_bfi_b32 v42, v56, 0, v42
v_bfi_b32 v43, v56, 0, v43
buffer_store_dwordx2 v[42:43], v218, s[36:39], 0 offen offset:0 sc1
v_accvgpr_read_b32 v48, a8
v_accvgpr_read_b32 v49, a9
v_accvgpr_read_b32 v50, a10
v_accvgpr_read_b32 v51, a11
v_accvgpr_read_b32 v52, a12
v_accvgpr_read_b32 v53, a13
v_accvgpr_read_b32 v54, a14
v_accvgpr_read_b32 v55, a15
v_fmac_f32 v48, v208, v36
v_fmac_f32 v49, v209, v36
v_fmac_f32 v50, v210, v36
v_fmac_f32 v51, v211, v36
v_fmac_f32 v52, v212, v36
v_fmac_f32 v53, v213, v36
v_fmac_f32 v54, v214, v36
v_fmac_f32 v55, v215, v36
v_mul_f32 v56, v48, v48
v_mul_f32 v57, v49, v49
v_mul_f32 v58, v50, v50
v_mul_f32 v59, v51, v51
v_mul_f32 v60, v52, v52
v_mul_f32 v61, v53, v53
v_mul_f32 v62, v54, v54
v_mul_f32 v63, v55, v55
v_fma_f32 v56, v56, v38, v37
v_fma_f32 v57, v57, v38, v37
v_fma_f32 v58, v58, v38, v37
v_fma_f32 v59, v59, v38, v37
v_fma_f32 v60, v60, v38, v37
v_fma_f32 v61, v61, v38, v37
v_fma_f32 v62, v62, v38, v37
v_fma_f32 v63, v63, v38, v37
v_mul_f32 v56, v56, v48
v_mul_f32 v57, v57, v49
v_mul_f32 v58, v58, v50
v_mul_f32 v59, v59, v51
v_mul_f32 v60, v60, v52
v_mul_f32 v61, v61, v53
v_mul_f32 v62, v62, v54
v_mul_f32 v63, v63, v55
v_exp_f32 v56, v56
v_exp_f32 v57, v57
v_exp_f32 v58, v58
v_exp_f32 v59, v59
v_exp_f32 v60, v60
v_exp_f32 v61, v61
v_exp_f32 v62, v62
v_exp_f32 v63, v63
v_fma_f32 v56, v56, v39, v39
v_fma_f32 v57, v57, v39, v39
v_fma_f32 v58, v58, v39, v39
v_fma_f32 v59, v59, v39, v39
v_fma_f32 v60, v60, v39, v39
v_fma_f32 v61, v61, v39, v39
v_fma_f32 v62, v62, v39, v39
v_fma_f32 v63, v63, v39, v39
v_rcp_f32 v56, v56
v_rcp_f32 v57, v57
v_rcp_f32 v58, v58
v_rcp_f32 v59, v59
v_rcp_f32 v60, v60
v_rcp_f32 v61, v61
v_rcp_f32 v62, v62
v_rcp_f32 v63, v63
v_mul_f32 v48, v48, v56
v_mul_f32 v49, v49, v57
v_mul_f32 v50, v50, v58
v_mul_f32 v51, v51, v59
v_mul_f32 v52, v52, v60
v_mul_f32 v53, v53, v61
v_mul_f32 v54, v54, v62
v_mul_f32 v55, v55, v63
v_cvt_pk_bf16_f32 v44, v48, v49
v_cvt_pk_bf16_f32 v45, v50, v51
v_cvt_pk_bf16_f32 v46, v52, v53
v_cvt_pk_bf16_f32 v47, v54, v55
v_ashrrev_i32 v56, 31, v219
v_and_b32 v219, 0x7fffffff, v219
v_bfi_b32 v44, v56, 0, v44
v_bfi_b32 v45, v56, 0, v45
buffer_store_dwordx2 v[44:45], v219, s[36:39], 0 offen offset:0 sc1
v_ashrrev_i32 v56, 31, v220
v_and_b32 v220, 0x7fffffff, v220
v_bfi_b32 v46, v56, 0, v46
v_bfi_b32 v47, v56, 0, v47
buffer_store_dwordx2 v[46:47], v220, s[36:39], 0 offen offset:0 sc1
s_branch .Lqpack
.Lspecial:
buffer_store_dwordx4 v[208:211], v217, s[36:39], 0 offen offset:0 sc1
buffer_store_dwordx4 v[212:215], v217, s[36:39], 0 offen offset:64 sc1
buffer_store_dwordx4 v[208:211], v217, s[36:39], 0 offen offset:0 sc1
buffer_store_dwordx4 v[212:215], v217, s[36:39], 0 offen offset:64 sc1
.Lqpack:
v_accvgpr_write_b32 a0, 0
v_accvgpr_write_b32 a1, 0
v_accvgpr_write_b32 a2, 0
v_accvgpr_write_b32 a3, 0
v_accvgpr_write_b32 a4, 0
v_accvgpr_write_b32 a5, 0
v_accvgpr_write_b32 a6, 0
v_accvgpr_write_b32 a7, 0
v_accvgpr_write_b32 a8, 0
v_accvgpr_write_b32 a9, 0
v_accvgpr_write_b32 a10, 0
v_accvgpr_write_b32 a11, 0
v_accvgpr_write_b32 a12, 0
v_accvgpr_write_b32 a13, 0
v_accvgpr_write_b32 a14, 0
v_accvgpr_write_b32 a15, 0
v_accvgpr_write_b32 a16, 0
v_accvgpr_write_b32 a17, 0
v_accvgpr_write_b32 a18, 0
v_accvgpr_write_b32 a19, 0
v_accvgpr_write_b32 a20, 0
v_accvgpr_write_b32 a21, 0
v_accvgpr_write_b32 a22, 0
v_accvgpr_write_b32 a23, 0
v_accvgpr_write_b32 a24, 0
v_accvgpr_write_b32 a25, 0
v_accvgpr_write_b32 a26, 0
v_accvgpr_write_b32 a27, 0
v_accvgpr_write_b32 a28, 0
v_accvgpr_write_b32 a29, 0
v_accvgpr_write_b32 a30, 0
v_accvgpr_write_b32 a31, 0
v_accvgpr_write_b32 a32, 0
v_accvgpr_write_b32 a33, 0
v_accvgpr_write_b32 a34, 0
v_accvgpr_write_b32 a35, 0
v_accvgpr_write_b32 a36, 0
v_accvgpr_write_b32 a37, 0
v_accvgpr_write_b32 a38, 0
v_accvgpr_write_b32 a39, 0
v_accvgpr_write_b32 a40, 0
v_accvgpr_write_b32 a41, 0
v_accvgpr_write_b32 a42, 0
v_accvgpr_write_b32 a43, 0
v_accvgpr_write_b32 a44, 0
v_accvgpr_write_b32 a45, 0
v_accvgpr_write_b32 a46, 0
v_accvgpr_write_b32 a47, 0
v_accvgpr_write_b32 a48, 0
v_accvgpr_write_b32 a49, 0
v_accvgpr_write_b32 a50, 0
v_accvgpr_write_b32 a51, 0
v_accvgpr_write_b32 a52, 0
v_accvgpr_write_b32 a53, 0
)ASM"
R"ASM(
v_accvgpr_write_b32 a54, 0
v_accvgpr_write_b32 a55, 0
v_accvgpr_write_b32 a56, 0
v_accvgpr_write_b32 a57, 0
v_accvgpr_write_b32 a58, 0
v_accvgpr_write_b32 a59, 0
v_accvgpr_write_b32 a60, 0
v_accvgpr_write_b32 a61, 0
v_accvgpr_write_b32 a62, 0
v_accvgpr_write_b32 a63, 0
v_accvgpr_write_b32 a64, 0
v_accvgpr_write_b32 a65, 0
v_accvgpr_write_b32 a66, 0
v_accvgpr_write_b32 a67, 0
v_accvgpr_write_b32 a68, 0
v_accvgpr_write_b32 a69, 0
v_accvgpr_write_b32 a70, 0
v_accvgpr_write_b32 a71, 0
v_accvgpr_write_b32 a72, 0
v_accvgpr_write_b32 a73, 0
v_accvgpr_write_b32 a74, 0
v_accvgpr_write_b32 a75, 0
v_accvgpr_write_b32 a76, 0
v_accvgpr_write_b32 a77, 0
v_accvgpr_write_b32 a78, 0
v_accvgpr_write_b32 a79, 0
v_accvgpr_write_b32 a80, 0
v_accvgpr_write_b32 a81, 0
v_accvgpr_write_b32 a82, 0
v_accvgpr_write_b32 a83, 0
v_accvgpr_write_b32 a84, 0
v_accvgpr_write_b32 a85, 0
v_accvgpr_write_b32 a86, 0
v_accvgpr_write_b32 a87, 0
v_accvgpr_write_b32 a88, 0
v_accvgpr_write_b32 a89, 0
v_accvgpr_write_b32 a90, 0
v_accvgpr_write_b32 a91, 0
v_accvgpr_write_b32 a92, 0
v_accvgpr_write_b32 a93, 0
v_accvgpr_write_b32 a94, 0
v_accvgpr_write_b32 a95, 0
v_accvgpr_write_b32 a96, 0
v_accvgpr_write_b32 a97, 0
v_accvgpr_write_b32 a98, 0
v_accvgpr_write_b32 a99, 0
s_mov_b32 s26, 0
s_getpc_b64 s[20:21]
.Lpc0:
s_add_u32 s20, s20, .Lev1-.Lpc0
s_addc_u32 s21, s21, 0
v_cmp_lt_u32 vcc, 127, v236
v_mov_b32 v217, 0xc0000000
s_add_u32 s73, s73, 524288
v_cndmask_b32 v217, v202, v217, vcc
s_waitcnt vmcnt(11)
s_barrier
ds_read_b64_tr_b16 v[64:65], v232 offset:0
ds_read_b64_tr_b16 v[66:67], v232 offset:2048
ds_read_b128 v[0:3], v224 offset:0
ds_read_b128 v[4:7], v224 offset:2048
ds_read_b128 v[8:11], v224 offset:4096
ds_read_b128 v[12:15], v224 offset:6144
ds_read_b64_tr_b16 v[68:69], v233 offset:0
ds_read_b64_tr_b16 v[70:71], v233 offset:2048
ds_read_b128 v[16:19], v224 offset:8192
ds_read_b128 v[20:23], v224 offset:10240
ds_read_b128 v[24:27], v224 offset:12288
ds_read_b128 v[28:31], v224 offset:14336
ds_read_b64_tr_b16 v[72:73], v234 offset:0
ds_read_b64_tr_b16 v[74:75], v234 offset:2048
s_cmp_eq_u32 s74, 0
s_cbranch_scc0 .LV
.p2align 3
.LK:
s_waitcnt lgkmcnt(11)
v_mfma_f32_16x16x32_bf16 a[0:3], v[64:67], v[0:3], a[0:3]
ds_read_b64_tr_b16 v[76:77], v235 offset:0
s_waitcnt lgkmcnt(11)
v_mfma_f32_16x16x32_bf16 a[32:35], v[64:67], v[4:7], a[32:35]
ds_read_b64_tr_b16 v[78:79], v235 offset:2048
s_add_u32 m0, s27, s68
s_nop 0
buffer_load_dwordx4 v221, s[40:43], s4 offen offset:0 lds
s_waitcnt lgkmcnt(11)
v_mfma_f32_16x16x32_bf16 a[64:67], v[64:67], v[8:11], a[64:67]
ds_read_b128 v[32:35], v225 offset:0
s_waitcnt lgkmcnt(11)
v_mfma_f32_16x16x32_bf16 a[96:99], v[64:67], v[12:15], a[96:99]
ds_read_b128 v[36:39], v225 offset:2048
s_waitcnt lgkmcnt(9)
v_mfma_f32_16x16x32_bf16 a[128:131], v[64:67], v[16:19], a[128:131]
s_waitcnt lgkmcnt(8)
v_mfma_f32_16x16x32_bf16 a[160:163], v[64:67], v[20:23], a[160:163]
buffer_load_dwordx4 v222, s[40:43], s5 offen offset:1024 lds
s_waitcnt lgkmcnt(7)
v_mfma_f32_16x16x32_bf16 a[192:195], v[64:67], v[24:27], a[192:195]
s_waitcnt lgkmcnt(6)
v_mfma_f32_16x16x32_bf16 a[224:227], v[64:67], v[28:31], a[224:227]
v_mfma_f32_16x16x32_bf16 a[4:7], v[68:71], v[0:3], a[4:7]
ds_read_b64_tr_b16 v[64:65], v232 offset:8192
v_mfma_f32_16x16x32_bf16 a[36:39], v[68:71], v[4:7], a[36:39]
ds_read_b64_tr_b16 v[66:67], v232 offset:10240
v_mfma_f32_16x16x32_bf16 a[68:71], v[68:71], v[8:11], a[68:71]
ds_read_b128 v[40:43], v225 offset:4096
buffer_load_dwordx4 v221, s[40:43], s6 offen offset:2048 lds
v_mfma_f32_16x16x32_bf16 a[100:103], v[68:71], v[12:15], a[100:103]
v_mfma_f32_16x16x32_bf16 a[132:135], v[68:71], v[16:19], a[132:135]
v_mfma_f32_16x16x32_bf16 a[164:167], v[68:71], v[20:23], a[164:167]
v_mfma_f32_16x16x32_bf16 a[196:199], v[68:71], v[24:27], a[196:199]
buffer_load_dwordx4 v222, s[40:43], s7 offen offset:3072 lds
v_mfma_f32_16x16x32_bf16 a[228:231], v[68:71], v[28:31], a[228:231]
s_waitcnt lgkmcnt(7)
v_mfma_f32_16x16x32_bf16 a[8:11], v[72:75], v[0:3], a[8:11]
ds_read_b64_tr_b16 v[68:69], v233 offset:8192
v_mfma_f32_16x16x32_bf16 a[40:43], v[72:75], v[4:7], a[40:43]
ds_read_b64_tr_b16 v[70:71], v233 offset:10240
v_mfma_f32_16x16x32_bf16 a[72:75], v[72:75], v[8:11], a[72:75]
v_mfma_f32_16x16x32_bf16 a[104:107], v[72:75], v[12:15], a[104:107]
ds_read_b128 v[44:47], v225 offset:6144
s_add_u32 m0, m0, 4096
s_nop 0
buffer_load_dwordx4 v221, s[40:43], s8 offen offset:0 lds
v_mfma_f32_16x16x32_bf16 a[136:139], v[72:75], v[16:19], a[136:139]
v_mfma_f32_16x16x32_bf16 a[168:171], v[72:75], v[20:23], a[168:171]
v_mfma_f32_16x16x32_bf16 a[200:203], v[72:75], v[24:27], a[200:203]
v_mfma_f32_16x16x32_bf16 a[232:235], v[72:75], v[28:31], a[232:235]
s_waitcnt lgkmcnt(8)
v_mfma_f32_16x16x32_bf16 a[12:15], v[76:79], v[0:3], a[12:15]
ds_read_b64_tr_b16 v[72:73], v234 offset:8192
buffer_load_dwordx4 v222, s[40:43], s9 offen offset:1024 lds
v_mfma_f32_16x16x32_bf16 a[44:47], v[76:79], v[4:7], a[44:47]
ds_read_b64_tr_b16 v[74:75], v234 offset:10240
v_mfma_f32_16x16x32_bf16 a[76:79], v[76:79], v[8:11], a[76:79]
ds_read_b128 v[48:51], v225 offset:8192
v_mfma_f32_16x16x32_bf16 a[108:111], v[76:79], v[12:15], a[108:111]
v_mfma_f32_16x16x32_bf16 a[140:143], v[76:79], v[16:19], a[140:143]
buffer_load_dwordx4 v221, s[40:43], s10 offen offset:2048 lds
v_mfma_f32_16x16x32_bf16 a[172:175], v[76:79], v[20:23], a[172:175]
v_mfma_f32_16x16x32_bf16 a[204:207], v[76:79], v[24:27], a[204:207]
v_mfma_f32_16x16x32_bf16 a[236:239], v[76:79], v[28:31], a[236:239]
s_waitcnt lgkmcnt(7)
v_mfma_f32_16x16x32_bf16 a[16:19], v[64:67], v[0:3], a[16:19]
ds_read_b64_tr_b16 v[76:77], v235 offset:8192
v_mfma_f32_16x16x32_bf16 a[48:51], v[64:67], v[4:7], a[48:51]
ds_read_b64_tr_b16 v[78:79], v235 offset:10240
buffer_load_dwordx4 v222, s[40:43], s11 offen offset:3072 lds
v_mfma_f32_16x16x32_bf16 a[80:83], v[64:67], v[8:11], a[80:83]
v_mfma_f32_16x16x32_bf16 a[112:115], v[64:67], v[12:15], a[112:115]
ds_read_b128 v[52:55], v225 offset:10240
v_mfma_f32_16x16x32_bf16 a[144:147], v[64:67], v[16:19], a[144:147]
v_mfma_f32_16x16x32_bf16 a[176:179], v[64:67], v[20:23], a[176:179]
v_mfma_f32_16x16x32_bf16 a[208:211], v[64:67], v[24:27], a[208:211]
v_add_u32 v221, 128, v221
v_add_u32 v222, 128, v222
v_mfma_f32_16x16x32_bf16 a[240:243], v[64:67], v[28:31], a[240:243]
s_waitcnt lgkmcnt(7)
v_mfma_f32_16x16x32_bf16 a[20:23], v[68:71], v[0:3], a[20:23]
ds_read_b64_tr_b16 v[64:65], v232 offset:4096
v_mfma_f32_16x16x32_bf16 a[52:55], v[68:71], v[4:7], a[52:55]
ds_read_b64_tr_b16 v[66:67], v232 offset:6144
v_mfma_f32_16x16x32_bf16 a[84:87], v[68:71], v[8:11], a[84:87]
ds_read_b128 v[56:59], v225 offset:12288
s_add_u32 m0, s27, s68
s_add_u32 m0, m0, 65536
s_nop 0
buffer_load_dwordx4 v223, s[44:47], s12 offen offset:0 lds
v_mfma_f32_16x16x32_bf16 a[116:119], v[68:71], v[12:15], a[116:119]
v_mfma_f32_16x16x32_bf16 a[148:151], v[68:71], v[16:19], a[148:151]
v_mfma_f32_16x16x32_bf16 a[180:183], v[68:71], v[20:23], a[180:183]
v_mfma_f32_16x16x32_bf16 a[212:215], v[68:71], v[24:27], a[212:215]
v_mfma_f32_16x16x32_bf16 a[244:247], v[68:71], v[28:31], a[244:247]
buffer_load_dwordx4 v223, s[44:47], s13 offen offset:1024 lds
s_waitcnt lgkmcnt(7)
v_mfma_f32_16x16x32_bf16 a[24:27], v[72:75], v[0:3], a[24:27]
ds_read_b64_tr_b16 v[68:69], v233 offset:4096
v_mfma_f32_16x16x32_bf16 a[56:59], v[72:75], v[4:7], a[56:59]
ds_read_b64_tr_b16 v[70:71], v233 offset:6144
v_mfma_f32_16x16x32_bf16 a[88:91], v[72:75], v[8:11], a[88:91]
v_mfma_f32_16x16x32_bf16 a[120:123], v[72:75], v[12:15], a[120:123]
ds_read_b128 v[60:63], v225 offset:14336
buffer_load_dwordx4 v223, s[44:47], s14 offen offset:2048 lds
v_mfma_f32_16x16x32_bf16 a[152:155], v[72:75], v[16:19], a[152:155]
v_mfma_f32_16x16x32_bf16 a[184:187], v[72:75], v[20:23], a[184:187]
v_mfma_f32_16x16x32_bf16 a[216:219], v[72:75], v[24:27], a[216:219]
v_mfma_f32_16x16x32_bf16 a[248:251], v[72:75], v[28:31], a[248:251]
s_waitcnt lgkmcnt(7)
v_mfma_f32_16x16x32_bf16 a[28:31], v[76:79], v[0:3], a[28:31]
ds_read_b64_tr_b16 v[72:73], v234 offset:4096
buffer_load_dwordx4 v223, s[44:47], s15 offen offset:3072 lds
v_mfma_f32_16x16x32_bf16 a[60:63], v[76:79], v[4:7], a[60:63]
ds_read_b64_tr_b16 v[74:75], v234 offset:6144
v_mfma_f32_16x16x32_bf16 a[92:95], v[76:79], v[8:11], a[92:95]
v_mfma_f32_16x16x32_bf16 a[124:127], v[76:79], v[12:15], a[124:127]
v_mfma_f32_16x16x32_bf16 a[156:159], v[76:79], v[16:19], a[156:159]
v_mfma_f32_16x16x32_bf16 a[188:191], v[76:79], v[20:23], a[188:191]
s_add_u32 m0, m0, 4096
s_nop 0
buffer_load_dwordx4 v223, s[44:47], s16 offen offset:0 lds
v_mfma_f32_16x16x32_bf16 a[220:223], v[76:79], v[24:27], a[220:223]
)ASM"
R"ASM(
v_mfma_f32_16x16x32_bf16 a[252:255], v[76:79], v[28:31], a[252:255]
s_waitcnt lgkmcnt(6)
v_mfma_f32_16x16x32_bf16 a[0:3], v[64:67], v[32:35], a[0:3]
ds_read_b64_tr_b16 v[76:77], v235 offset:4096
v_mfma_f32_16x16x32_bf16 a[32:35], v[64:67], v[36:39], a[32:35]
ds_read_b64_tr_b16 v[78:79], v235 offset:6144
buffer_load_dwordx4 v223, s[44:47], s17 offen offset:1024 lds
v_mfma_f32_16x16x32_bf16 a[64:67], v[64:67], v[40:43], a[64:67]
v_mfma_f32_16x16x32_bf16 a[96:99], v[64:67], v[44:47], a[96:99]
v_mfma_f32_16x16x32_bf16 a[128:131], v[64:67], v[48:51], a[128:131]
v_mfma_f32_16x16x32_bf16 a[160:163], v[64:67], v[52:55], a[160:163]
s_waitcnt lgkmcnt(7)
v_mfma_f32_16x16x32_bf16 a[192:195], v[64:67], v[56:59], a[192:195]
buffer_load_dwordx4 v223, s[44:47], s18 offen offset:2048 lds
s_waitcnt lgkmcnt(4)
v_mfma_f32_16x16x32_bf16 a[224:227], v[64:67], v[60:63], a[224:227]
v_mfma_f32_16x16x32_bf16 a[4:7], v[68:71], v[32:35], a[4:7]
ds_read_b64_tr_b16 v[64:65], v232 offset:12288
v_mfma_f32_16x16x32_bf16 a[36:39], v[68:71], v[36:39], a[36:39]
ds_read_b64_tr_b16 v[66:67], v232 offset:14336
v_mfma_f32_16x16x32_bf16 a[68:71], v[68:71], v[40:43], a[68:71]
v_mfma_f32_16x16x32_bf16 a[100:103], v[68:71], v[44:47], a[100:103]
buffer_load_dwordx4 v223, s[44:47], s19 offen offset:3072 lds
v_mfma_f32_16x16x32_bf16 a[132:135], v[68:71], v[48:51], a[132:135]
v_mfma_f32_16x16x32_bf16 a[164:167], v[68:71], v[52:55], a[164:167]
v_mfma_f32_16x16x32_bf16 a[196:199], v[68:71], v[56:59], a[196:199]
v_mfma_f32_16x16x32_bf16 a[228:231], v[68:71], v[60:63], a[228:231]
v_add_u32 v223, s96, v223
s_xor_b32 s27, s27, 32768
s_waitcnt lgkmcnt(4)
v_mfma_f32_16x16x32_bf16 a[8:11], v[72:75], v[32:35], a[8:11]
ds_read_b64_tr_b16 v[68:69], v233 offset:12288
v_mfma_f32_16x16x32_bf16 a[40:43], v[72:75], v[36:39], a[40:43]
ds_read_b64_tr_b16 v[70:71], v233 offset:14336
v_mfma_f32_16x16x32_bf16 a[72:75], v[72:75], v[40:43], a[72:75]
v_mfma_f32_16x16x32_bf16 a[104:107], v[72:75], v[44:47], a[104:107]
v_mfma_f32_16x16x32_bf16 a[136:139], v[72:75], v[48:51], a[136:139]
buffer_load_dword v220, v217, s[44:47], s73 offen offset:0
s_add_u32 s73, s73, 524288
v_mfma_f32_16x16x32_bf16 a[168:171], v[72:75], v[52:55], a[168:171]
v_mfma_f32_16x16x32_bf16 a[200:203], v[72:75], v[56:59], a[200:203]
v_mfma_f32_16x16x32_bf16 a[232:235], v[72:75], v[60:63], a[232:235]
s_waitcnt lgkmcnt(4)
v_mfma_f32_16x16x32_bf16 a[12:15], v[76:79], v[32:35], a[12:15]
ds_read_b64_tr_b16 v[72:73], v234 offset:12288
v_mfma_f32_16x16x32_bf16 a[44:47], v[76:79], v[36:39], a[44:47]
ds_read_b64_tr_b16 v[74:75], v234 offset:14336
v_mfma_f32_16x16x32_bf16 a[76:79], v[76:79], v[40:43], a[76:79]
v_mfma_f32_16x16x32_bf16 a[108:111], v[76:79], v[44:47], a[108:111]
v_mfma_f32_16x16x32_bf16 a[140:143], v[76:79], v[48:51], a[140:143]
v_mfma_f32_16x16x32_bf16 a[172:175], v[76:79], v[52:55], a[172:175]
v_mfma_f32_16x16x32_bf16 a[204:207], v[76:79], v[56:59], a[204:207]
v_mfma_f32_16x16x32_bf16 a[236:239], v[76:79], v[60:63], a[236:239]
s_waitcnt lgkmcnt(4)
v_mfma_f32_16x16x32_bf16 a[16:19], v[64:67], v[32:35], a[16:19]
ds_read_b64_tr_b16 v[76:77], v235 offset:12288
v_mfma_f32_16x16x32_bf16 a[48:51], v[64:67], v[36:39], a[48:51]
ds_read_b64_tr_b16 v[78:79], v235 offset:14336
v_mfma_f32_16x16x32_bf16 a[80:83], v[64:67], v[40:43], a[80:83]
v_xor_b32 v232, 0x8000, v232
v_mfma_f32_16x16x32_bf16 a[112:115], v[64:67], v[44:47], a[112:115]
v_mfma_f32_16x16x32_bf16 a[144:147], v[64:67], v[48:51], a[144:147]
v_xor_b32 v233, 0x8000, v233
v_mfma_f32_16x16x32_bf16 a[176:179], v[64:67], v[52:55], a[176:179]
v_mfma_f32_16x16x32_bf16 a[208:211], v[64:67], v[56:59], a[208:211]
v_xor_b32 v234, 0x8000, v234
v_mfma_f32_16x16x32_bf16 a[240:243], v[64:67], v[60:63], a[240:243]
s_waitcnt lgkmcnt(4)
v_mfma_f32_16x16x32_bf16 a[20:23], v[68:71], v[32:35], a[20:23]
v_mfma_f32_16x16x32_bf16 a[52:55], v[68:71], v[36:39], a[52:55]
v_xor_b32 v235, 0x8000, v235
v_mfma_f32_16x16x32_bf16 a[84:87], v[68:71], v[40:43], a[84:87]
v_mfma_f32_16x16x32_bf16 a[116:119], v[68:71], v[44:47], a[116:119]
v_xor_b32 v224, 0x8000, v224
v_mfma_f32_16x16x32_bf16 a[148:151], v[68:71], v[48:51], a[148:151]
v_mfma_f32_16x16x32_bf16 a[180:183], v[68:71], v[52:55], a[180:183]
v_xor_b32 v225, 0x8000, v225
v_mfma_f32_16x16x32_bf16 a[212:215], v[68:71], v[56:59], a[212:215]
v_mfma_f32_16x16x32_bf16 a[244:247], v[68:71], v[60:63], a[244:247]
s_waitcnt lgkmcnt(2)
v_mfma_f32_16x16x32_bf16 a[24:27], v[72:75], v[32:35], a[24:27]
s_waitcnt vmcnt(1) lgkmcnt(0)
s_barrier
ds_read_b64_tr_b16 v[64:65], v232 offset:0
v_mfma_f32_16x16x32_bf16 a[56:59], v[72:75], v[36:39], a[56:59]
ds_read_b64_tr_b16 v[66:67], v232 offset:2048
v_mfma_f32_16x16x32_bf16 a[88:91], v[72:75], v[40:43], a[88:91]
ds_read_b128 v[0:3], v224 offset:0
v_mfma_f32_16x16x32_bf16 a[120:123], v[72:75], v[44:47], a[120:123]
ds_read_b128 v[4:7], v224 offset:2048
v_mfma_f32_16x16x32_bf16 a[152:155], v[72:75], v[48:51], a[152:155]
ds_read_b128 v[8:11], v224 offset:4096
v_mfma_f32_16x16x32_bf16 a[184:187], v[72:75], v[52:55], a[184:187]
ds_read_b128 v[12:15], v224 offset:6144
v_mfma_f32_16x16x32_bf16 a[216:219], v[72:75], v[56:59], a[216:219]
v_mfma_f32_16x16x32_bf16 a[248:251], v[72:75], v[60:63], a[248:251]
ds_read_b64_tr_b16 v[68:69], v233 offset:0
v_mfma_f32_16x16x32_bf16 a[28:31], v[76:79], v[32:35], a[28:31]
ds_read_b64_tr_b16 v[70:71], v233 offset:2048
v_mfma_f32_16x16x32_bf16 a[60:63], v[76:79], v[36:39], a[60:63]
ds_read_b128 v[16:19], v224 offset:8192
v_mfma_f32_16x16x32_bf16 a[92:95], v[76:79], v[40:43], a[92:95]
ds_read_b128 v[20:23], v224 offset:10240
v_mfma_f32_16x16x32_bf16 a[124:127], v[76:79], v[44:47], a[124:127]
ds_read_b128 v[24:27], v224 offset:12288
v_mfma_f32_16x16x32_bf16 a[156:159], v[76:79], v[48:51], a[156:159]
ds_read_b128 v[28:31], v224 offset:14336
v_mfma_f32_16x16x32_bf16 a[188:191], v[76:79], v[52:55], a[188:191]
v_mfma_f32_16x16x32_bf16 a[220:223], v[76:79], v[56:59], a[220:223]
ds_read_b64_tr_b16 v[72:73], v234 offset:0
v_mfma_f32_16x16x32_bf16 a[252:255], v[76:79], v[60:63], a[252:255]
ds_read_b64_tr_b16 v[74:75], v234 offset:2048
s_swappc_b64 s[24:25], s[20:21]
s_add_u32 s26, s26, 1
s_bitcmp1_b32 7928, s26
s_cbranch_scc1 .LKx
s_branch .LK
.p2align 3
.LKx:
s_waitcnt lgkmcnt(11)
v_mfma_f32_16x16x32_bf16 a[0:3], v[64:67], v[0:3], a[0:3]
ds_read_b64_tr_b16 v[76:77], v235 offset:0
s_waitcnt lgkmcnt(11)
v_mfma_f32_16x16x32_bf16 a[32:35], v[64:67], v[4:7], a[32:35]
ds_read_b64_tr_b16 v[78:79], v235 offset:2048
s_add_u32 m0, s27, s68
s_nop 0
buffer_load_dwordx4 v221, s[40:43], s4 offen offset:0 lds
s_waitcnt lgkmcnt(11)
v_mfma_f32_16x16x32_bf16 a[64:67], v[64:67], v[8:11], a[64:67]
ds_read_b128 v[32:35], v225 offset:0
s_waitcnt lgkmcnt(11)
v_mfma_f32_16x16x32_bf16 a[96:99], v[64:67], v[12:15], a[96:99]
ds_read_b128 v[36:39], v225 offset:2048
s_waitcnt lgkmcnt(9)
v_mfma_f32_16x16x32_bf16 a[128:131], v[64:67], v[16:19], a[128:131]
buffer_load_dwordx4 v222, s[40:43], s5 offen offset:1024 lds
s_waitcnt lgkmcnt(8)
v_mfma_f32_16x16x32_bf16 a[160:163], v[64:67], v[20:23], a[160:163]
s_waitcnt lgkmcnt(7)
v_mfma_f32_16x16x32_bf16 a[192:195], v[64:67], v[24:27], a[192:195]
s_waitcnt lgkmcnt(6)
v_mfma_f32_16x16x32_bf16 a[224:227], v[64:67], v[28:31], a[224:227]
buffer_load_dwordx4 v221, s[40:43], s6 offen offset:2048 lds
v_mfma_f32_16x16x32_bf16 a[4:7], v[68:71], v[0:3], a[4:7]
ds_read_b64_tr_b16 v[64:65], v232 offset:8192
v_mfma_f32_16x16x32_bf16 a[36:39], v[68:71], v[4:7], a[36:39]
ds_read_b64_tr_b16 v[66:67], v232 offset:10240
v_mfma_f32_16x16x32_bf16 a[68:71], v[68:71], v[8:11], a[68:71]
ds_read_b128 v[40:43], v225 offset:4096
v_mfma_f32_16x16x32_bf16 a[100:103], v[68:71], v[12:15], a[100:103]
buffer_load_dwordx4 v222, s[40:43], s7 offen offset:3072 lds
v_mfma_f32_16x16x32_bf16 a[132:135], v[68:71], v[16:19], a[132:135]
v_mfma_f32_16x16x32_bf16 a[164:167], v[68:71], v[20:23], a[164:167]
v_mfma_f32_16x16x32_bf16 a[196:199], v[68:71], v[24:27], a[196:199]
s_add_u32 m0, m0, 4096
s_nop 0
buffer_load_dwordx4 v221, s[40:43], s8 offen offset:0 lds
v_mfma_f32_16x16x32_bf16 a[228:231], v[68:71], v[28:31], a[228:231]
s_waitcnt lgkmcnt(7)
v_mfma_f32_16x16x32_bf16 a[8:11], v[72:75], v[0:3], a[8:11]
ds_read_b64_tr_b16 v[68:69], v233 offset:8192
v_mfma_f32_16x16x32_bf16 a[40:43], v[72:75], v[4:7], a[40:43]
ds_read_b64_tr_b16 v[70:71], v233 offset:10240
v_mfma_f32_16x16x32_bf16 a[72:75], v[72:75], v[8:11], a[72:75]
buffer_load_dwordx4 v222, s[40:43], s9 offen offset:1024 lds
v_mfma_f32_16x16x32_bf16 a[104:107], v[72:75], v[12:15], a[104:107]
ds_read_b128 v[44:47], v225 offset:6144
v_mfma_f32_16x16x32_bf16 a[136:139], v[72:75], v[16:19], a[136:139]
v_mfma_f32_16x16x32_bf16 a[168:171], v[72:75], v[20:23], a[168:171]
buffer_load_dwordx4 v221, s[40:43], s10 offen offset:2048 lds
v_mfma_f32_16x16x32_bf16 a[200:203], v[72:75], v[24:27], a[200:203]
v_mfma_f32_16x16x32_bf16 a[232:235], v[72:75], v[28:31], a[232:235]
s_waitcnt lgkmcnt(8)
v_mfma_f32_16x16x32_bf16 a[12:15], v[76:79], v[0:3], a[12:15]
ds_read_b64_tr_b16 v[72:73], v234 offset:8192
buffer_load_dwordx4 v222, s[40:43], s11 offen offset:3072 lds
v_mfma_f32_16x16x32_bf16 a[44:47], v[76:79], v[4:7], a[44:47]
ds_read_b64_tr_b16 v[74:75], v234 offset:10240
v_mfma_f32_16x16x32_bf16 a[76:79], v[76:79], v[8:11], a[76:79]
ds_read_b128 v[48:51], v225 offset:8192
v_mfma_f32_16x16x32_bf16 a[108:111], v[76:79], v[12:15], a[108:111]
v_mfma_f32_16x16x32_bf16 a[140:143], v[76:79], v[16:19], a[140:143]
v_add_u32 v221, 128, v221
v_add_u32 v222, 128, v222
v_mfma_f32_16x16x32_bf16 a[172:175], v[76:79], v[20:23], a[172:175]
v_mfma_f32_16x16x32_bf16 a[204:207], v[76:79], v[24:27], a[204:207]
v_mfma_f32_16x16x32_bf16 a[236:239], v[76:79], v[28:31], a[236:239]
s_add_u32 m0, s27, s68
s_add_u32 m0, m0, 65536
s_nop 0
buffer_load_dwordx4 v223, s[44:47], s12 offen offset:0 lds
s_waitcnt lgkmcnt(7)
v_mfma_f32_16x16x32_bf16 a[16:19], v[64:67], v[0:3], a[16:19]
ds_read_b64_tr_b16 v[76:77], v235 offset:8192
v_mfma_f32_16x16x32_bf16 a[48:51], v[64:67], v[4:7], a[48:51]
ds_read_b64_tr_b16 v[78:79], v235 offset:10240
v_mfma_f32_16x16x32_bf16 a[80:83], v[64:67], v[8:11], a[80:83]
v_mfma_f32_16x16x32_bf16 a[112:115], v[64:67], v[12:15], a[112:115]
ds_read_b128 v[52:55], v225 offset:10240
)ASM"
R"ASM(
buffer_load_dwordx4 v223, s[44:47], s13 offen offset:1024 lds
v_mfma_f32_16x16x32_bf16 a[144:147], v[64:67], v[16:19], a[144:147]
v_mfma_f32_16x16x32_bf16 a[176:179], v[64:67], v[20:23], a[176:179]
v_mfma_f32_16x16x32_bf16 a[208:211], v[64:67], v[24:27], a[208:211]
buffer_load_dwordx4 v223, s[44:47], s14 offen offset:2048 lds
v_mfma_f32_16x16x32_bf16 a[240:243], v[64:67], v[28:31], a[240:243]
s_waitcnt lgkmcnt(7)
v_mfma_f32_16x16x32_bf16 a[20:23], v[68:71], v[0:3], a[20:23]
ds_read_b64_tr_b16 v[64:65], v232 offset:4096
v_mfma_f32_16x16x32_bf16 a[52:55], v[68:71], v[4:7], a[52:55]
ds_read_b64_tr_b16 v[66:67], v232 offset:6144
v_mfma_f32_16x16x32_bf16 a[84:87], v[68:71], v[8:11], a[84:87]
ds_read_b128 v[56:59], v225 offset:12288
buffer_load_dwordx4 v223, s[44:47], s15 offen offset:3072 lds
v_mfma_f32_16x16x32_bf16 a[116:119], v[68:71], v[12:15], a[116:119]
v_mfma_f32_16x16x32_bf16 a[148:151], v[68:71], v[16:19], a[148:151]
v_mfma_f32_16x16x32_bf16 a[180:183], v[68:71], v[20:23], a[180:183]
s_add_u32 m0, m0, 4096
s_nop 0
buffer_load_dwordx4 v223, s[44:47], s16 offen offset:0 lds
v_mfma_f32_16x16x32_bf16 a[212:215], v[68:71], v[24:27], a[212:215]
v_mfma_f32_16x16x32_bf16 a[244:247], v[68:71], v[28:31], a[244:247]
s_waitcnt lgkmcnt(7)
v_mfma_f32_16x16x32_bf16 a[24:27], v[72:75], v[0:3], a[24:27]
ds_read_b64_tr_b16 v[68:69], v233 offset:4096
buffer_load_dwordx4 v223, s[44:47], s17 offen offset:1024 lds
v_mfma_f32_16x16x32_bf16 a[56:59], v[72:75], v[4:7], a[56:59]
ds_read_b64_tr_b16 v[70:71], v233 offset:6144
v_mfma_f32_16x16x32_bf16 a[88:91], v[72:75], v[8:11], a[88:91]
v_mfma_f32_16x16x32_bf16 a[120:123], v[72:75], v[12:15], a[120:123]
ds_read_b128 v[60:63], v225 offset:14336
v_mfma_f32_16x16x32_bf16 a[152:155], v[72:75], v[16:19], a[152:155]
buffer_load_dwordx4 v223, s[44:47], s18 offen offset:2048 lds
v_mfma_f32_16x16x32_bf16 a[184:187], v[72:75], v[20:23], a[184:187]
v_mfma_f32_16x16x32_bf16 a[216:219], v[72:75], v[24:27], a[216:219]
v_mfma_f32_16x16x32_bf16 a[248:251], v[72:75], v[28:31], a[248:251]
buffer_load_dwordx4 v223, s[44:47], s19 offen offset:3072 lds
s_waitcnt lgkmcnt(7)
v_mfma_f32_16x16x32_bf16 a[28:31], v[76:79], v[0:3], a[28:31]
ds_read_b64_tr_b16 v[72:73], v234 offset:4096
v_mfma_f32_16x16x32_bf16 a[60:63], v[76:79], v[4:7], a[60:63]
ds_read_b64_tr_b16 v[74:75], v234 offset:6144
v_mfma_f32_16x16x32_bf16 a[92:95], v[76:79], v[8:11], a[92:95]
v_mfma_f32_16x16x32_bf16 a[124:127], v[76:79], v[12:15], a[124:127]
v_add_u32 v223, s96, v223
s_xor_b32 s27, s27, 32768
v_mfma_f32_16x16x32_bf16 a[156:159], v[76:79], v[16:19], a[156:159]
v_mfma_f32_16x16x32_bf16 a[188:191], v[76:79], v[20:23], a[188:191]
v_mfma_f32_16x16x32_bf16 a[220:223], v[76:79], v[24:27], a[220:223]
buffer_load_dword v220, v217, s[44:47], s73 offen offset:0
s_add_u32 s73, s73, 524288
v_mfma_f32_16x16x32_bf16 a[252:255], v[76:79], v[28:31], a[252:255]
s_waitcnt lgkmcnt(6)
v_mfma_f32_16x16x32_bf16 a[0:3], v[64:67], v[32:35], a[0:3]
ds_read_b64_tr_b16 v[76:77], v235 offset:4096
v_mfma_f32_16x16x32_bf16 a[32:35], v[64:67], v[36:39], a[32:35]
ds_read_b64_tr_b16 v[78:79], v235 offset:6144
v_mfma_f32_16x16x32_bf16 a[64:67], v[64:67], v[40:43], a[64:67]
v_mfma_f32_16x16x32_bf16 a[96:99], v[64:67], v[44:47], a[96:99]
v_mfma_f32_16x16x32_bf16 a[128:131], v[64:67], v[48:51], a[128:131]
s_swappc_b64 s[24:25], s[20:21]
v_mfma_f32_16x16x32_bf16 a[160:163], v[64:67], v[52:55], a[160:163]
s_waitcnt lgkmcnt(7)
v_mfma_f32_16x16x32_bf16 a[192:195], v[64:67], v[56:59], a[192:195]
s_waitcnt lgkmcnt(4)
v_mfma_f32_16x16x32_bf16 a[224:227], v[64:67], v[60:63], a[224:227]
v_mfma_f32_16x16x32_bf16 a[4:7], v[68:71], v[32:35], a[4:7]
ds_read_b64_tr_b16 v[64:65], v232 offset:12288
v_mfma_f32_16x16x32_bf16 a[36:39], v[68:71], v[36:39], a[36:39]
ds_read_b64_tr_b16 v[66:67], v232 offset:14336
v_mfma_f32_16x16x32_bf16 a[68:71], v[68:71], v[40:43], a[68:71]
v_mfma_f32_16x16x32_bf16 a[100:103], v[68:71], v[44:47], a[100:103]
v_mfma_f32_16x16x32_bf16 a[132:135], v[68:71], v[48:51], a[132:135]
v_mfma_f32_16x16x32_bf16 a[164:167], v[68:71], v[52:55], a[164:167]
v_mfma_f32_16x16x32_bf16 a[196:199], v[68:71], v[56:59], a[196:199]
v_mfma_f32_16x16x32_bf16 a[228:231], v[68:71], v[60:63], a[228:231]
s_waitcnt lgkmcnt(4)
v_mfma_f32_16x16x32_bf16 a[8:11], v[72:75], v[32:35], a[8:11]
ds_read_b64_tr_b16 v[68:69], v233 offset:12288
s_add_u32 m0, s68, 131072
s_add_u32 s22, s22, s23
buffer_load_dwordx4 v218, s[60:63], s22 offen offset:0 sc1 lds
v_mfma_f32_16x16x32_bf16 a[40:43], v[72:75], v[36:39], a[40:43]
ds_read_b64_tr_b16 v[70:71], v233 offset:14336
v_mfma_f32_16x16x32_bf16 a[72:75], v[72:75], v[40:43], a[72:75]
v_mfma_f32_16x16x32_bf16 a[104:107], v[72:75], v[44:47], a[104:107]
v_mfma_f32_16x16x32_bf16 a[136:139], v[72:75], v[48:51], a[136:139]
v_mfma_f32_16x16x32_bf16 a[168:171], v[72:75], v[52:55], a[168:171]
v_mfma_f32_16x16x32_bf16 a[200:203], v[72:75], v[56:59], a[200:203]
s_add_u32 m0, s68, 132096
s_add_u32 s22, s22, s23
buffer_load_dwordx4 v218, s[60:63], s22 offen offset:0 sc1 lds
v_mfma_f32_16x16x32_bf16 a[232:235], v[72:75], v[60:63], a[232:235]
s_waitcnt lgkmcnt(4)
v_mfma_f32_16x16x32_bf16 a[12:15], v[76:79], v[32:35], a[12:15]
ds_read_b64_tr_b16 v[72:73], v234 offset:12288
v_mfma_f32_16x16x32_bf16 a[44:47], v[76:79], v[36:39], a[44:47]
ds_read_b64_tr_b16 v[74:75], v234 offset:14336
v_mfma_f32_16x16x32_bf16 a[76:79], v[76:79], v[40:43], a[76:79]
v_mfma_f32_16x16x32_bf16 a[108:111], v[76:79], v[44:47], a[108:111]
v_mfma_f32_16x16x32_bf16 a[140:143], v[76:79], v[48:51], a[140:143]
s_add_u32 m0, s68, 133120
s_add_u32 s22, s22, s23
buffer_load_dwordx4 v218, s[60:63], s22 offen offset:0 sc1 lds
v_mfma_f32_16x16x32_bf16 a[172:175], v[76:79], v[52:55], a[172:175]
v_mfma_f32_16x16x32_bf16 a[204:207], v[76:79], v[56:59], a[204:207]
v_mfma_f32_16x16x32_bf16 a[236:239], v[76:79], v[60:63], a[236:239]
s_waitcnt lgkmcnt(4)
v_mfma_f32_16x16x32_bf16 a[16:19], v[64:67], v[32:35], a[16:19]
ds_read_b64_tr_b16 v[76:77], v235 offset:12288
v_mfma_f32_16x16x32_bf16 a[48:51], v[64:67], v[36:39], a[48:51]
ds_read_b64_tr_b16 v[78:79], v235 offset:14336
v_mfma_f32_16x16x32_bf16 a[80:83], v[64:67], v[40:43], a[80:83]
v_xor_b32 v232, 0x8000, v232
s_add_u32 m0, s68, 134144
s_add_u32 s22, s22, s23
buffer_load_dwordx4 v218, s[60:63], s22 offen offset:0 sc1 lds
v_mfma_f32_16x16x32_bf16 a[112:115], v[64:67], v[44:47], a[112:115]
v_mfma_f32_16x16x32_bf16 a[144:147], v[64:67], v[48:51], a[144:147]
v_xor_b32 v233, 0x8000, v233
v_mfma_f32_16x16x32_bf16 a[176:179], v[64:67], v[52:55], a[176:179]
v_mfma_f32_16x16x32_bf16 a[208:211], v[64:67], v[56:59], a[208:211]
v_xor_b32 v234, 0x8000, v234
v_mfma_f32_16x16x32_bf16 a[240:243], v[64:67], v[60:63], a[240:243]
s_waitcnt lgkmcnt(4)
v_mfma_f32_16x16x32_bf16 a[20:23], v[68:71], v[32:35], a[20:23]
s_add_u32 m0, s68, 135168
s_add_u32 s22, s22, s23
buffer_load_dwordx4 v218, s[60:63], s22 offen offset:0 sc1 lds
v_mfma_f32_16x16x32_bf16 a[52:55], v[68:71], v[36:39], a[52:55]
v_xor_b32 v235, 0x8000, v235
v_mfma_f32_16x16x32_bf16 a[84:87], v[68:71], v[40:43], a[84:87]
v_mfma_f32_16x16x32_bf16 a[116:119], v[68:71], v[44:47], a[116:119]
v_xor_b32 v224, 0x8000, v224
v_mfma_f32_16x16x32_bf16 a[148:151], v[68:71], v[48:51], a[148:151]
v_mfma_f32_16x16x32_bf16 a[180:183], v[68:71], v[52:55], a[180:183]
v_xor_b32 v225, 0x8000, v225
v_mfma_f32_16x16x32_bf16 a[212:215], v[68:71], v[56:59], a[212:215]
s_add_u32 m0, s68, 136192
s_add_u32 s22, s22, s23
buffer_load_dwordx4 v218, s[60:63], s22 offen offset:0 sc1 lds
v_mfma_f32_16x16x32_bf16 a[244:247], v[68:71], v[60:63], a[244:247]
s_waitcnt lgkmcnt(2)
v_mfma_f32_16x16x32_bf16 a[24:27], v[72:75], v[32:35], a[24:27]
s_waitcnt vmcnt(7) lgkmcnt(0)
s_barrier
ds_read_b64_tr_b16 v[64:65], v232 offset:0
v_mfma_f32_16x16x32_bf16 a[56:59], v[72:75], v[36:39], a[56:59]
ds_read_b64_tr_b16 v[66:67], v232 offset:2048
v_mfma_f32_16x16x32_bf16 a[88:91], v[72:75], v[40:43], a[88:91]
ds_read_b128 v[0:3], v224 offset:0
v_mfma_f32_16x16x32_bf16 a[120:123], v[72:75], v[44:47], a[120:123]
ds_read_b128 v[4:7], v224 offset:2048
v_mfma_f32_16x16x32_bf16 a[152:155], v[72:75], v[48:51], a[152:155]
ds_read_b128 v[8:11], v224 offset:4096
s_add_u32 m0, s68, 137216
s_add_u32 s22, s22, s23
buffer_load_dwordx4 v218, s[60:63], s22 offen offset:0 sc1 lds
v_mfma_f32_16x16x32_bf16 a[184:187], v[72:75], v[52:55], a[184:187]
ds_read_b128 v[12:15], v224 offset:6144
v_mfma_f32_16x16x32_bf16 a[216:219], v[72:75], v[56:59], a[216:219]
v_mfma_f32_16x16x32_bf16 a[248:251], v[72:75], v[60:63], a[248:251]
ds_read_b64_tr_b16 v[68:69], v233 offset:0
v_mfma_f32_16x16x32_bf16 a[28:31], v[76:79], v[32:35], a[28:31]
ds_read_b64_tr_b16 v[70:71], v233 offset:2048
v_mfma_f32_16x16x32_bf16 a[60:63], v[76:79], v[36:39], a[60:63]
ds_read_b128 v[16:19], v224 offset:8192
v_mfma_f32_16x16x32_bf16 a[92:95], v[76:79], v[40:43], a[92:95]
ds_read_b128 v[20:23], v224 offset:10240
s_add_u32 m0, s68, 138240
s_add_u32 s22, s22, s23
buffer_load_dwordx4 v218, s[60:63], s22 offen offset:0 sc1 lds
v_mfma_f32_16x16x32_bf16 a[124:127], v[76:79], v[44:47], a[124:127]
ds_read_b128 v[24:27], v224 offset:12288
v_mfma_f32_16x16x32_bf16 a[156:159], v[76:79], v[48:51], a[156:159]
ds_read_b128 v[28:31], v224 offset:14336
v_mfma_f32_16x16x32_bf16 a[188:191], v[76:79], v[52:55], a[188:191]
v_mfma_f32_16x16x32_bf16 a[220:223], v[76:79], v[56:59], a[220:223]
ds_read_b64_tr_b16 v[72:73], v234 offset:0
v_mfma_f32_16x16x32_bf16 a[252:255], v[76:79], v[60:63], a[252:255]
ds_read_b64_tr_b16 v[74:75], v234 offset:2048
s_swappc_b64 s[24:25], s[20:21]
s_add_u32 s26, s26, 1
s_bitcmp1_b32 7928, s26
s_cbranch_scc1 .LKx
s_branch .LK
.p2align 3
.LV:
s_waitcnt lgkmcnt(11)
v_mfma_f32_16x16x32_bf16 a[0:3], v[0:3], v[64:67], a[0:3]
ds_read_b64_tr_b16 v[76:77], v235 offset:0
s_waitcnt lgkmcnt(11)
v_mfma_f32_16x16x32_bf16 a[4:7], v[4:7], v[64:67], a[4:7]
ds_read_b64_tr_b16 v[78:79], v235 offset:2048
s_add_u32 m0, s27, s68
s_nop 0
buffer_load_dwordx4 v221, s[40:43], s4 offen offset:0 lds
s_waitcnt lgkmcnt(11)
v_mfma_f32_16x16x32_bf16 a[64:67], v[8:11], v[64:67], a[64:67]
ds_read_b128 v[32:35], v225 offset:0
)ASM"
R"ASM(
s_waitcnt lgkmcnt(11)
v_mfma_f32_16x16x32_bf16 a[68:71], v[12:15], v[64:67], a[68:71]
ds_read_b128 v[36:39], v225 offset:2048
s_waitcnt lgkmcnt(9)
v_mfma_f32_16x16x32_bf16 a[128:131], v[16:19], v[64:67], a[128:131]
s_waitcnt lgkmcnt(8)
v_mfma_f32_16x16x32_bf16 a[132:135], v[20:23], v[64:67], a[132:135]
buffer_load_dwordx4 v222, s[40:43], s5 offen offset:1024 lds
s_waitcnt lgkmcnt(7)
v_mfma_f32_16x16x32_bf16 a[192:195], v[24:27], v[64:67], a[192:195]
s_waitcnt lgkmcnt(6)
v_mfma_f32_16x16x32_bf16 a[196:199], v[28:31], v[64:67], a[196:199]
v_mfma_f32_16x16x32_bf16 a[8:11], v[0:3], v[68:71], a[8:11]
ds_read_b64_tr_b16 v[64:65], v232 offset:8192
v_mfma_f32_16x16x32_bf16 a[12:15], v[4:7], v[68:71], a[12:15]
ds_read_b64_tr_b16 v[66:67], v232 offset:10240
v_mfma_f32_16x16x32_bf16 a[72:75], v[8:11], v[68:71], a[72:75]
ds_read_b128 v[40:43], v225 offset:4096
buffer_load_dwordx4 v221, s[40:43], s6 offen offset:2048 lds
v_mfma_f32_16x16x32_bf16 a[76:79], v[12:15], v[68:71], a[76:79]
v_mfma_f32_16x16x32_bf16 a[136:139], v[16:19], v[68:71], a[136:139]
v_mfma_f32_16x16x32_bf16 a[140:143], v[20:23], v[68:71], a[140:143]
v_mfma_f32_16x16x32_bf16 a[200:203], v[24:27], v[68:71], a[200:203]
buffer_load_dwordx4 v222, s[40:43], s7 offen offset:3072 lds
v_mfma_f32_16x16x32_bf16 a[204:207], v[28:31], v[68:71], a[204:207]
s_waitcnt lgkmcnt(7)
v_mfma_f32_16x16x32_bf16 a[16:19], v[0:3], v[72:75], a[16:19]
ds_read_b64_tr_b16 v[68:69], v233 offset:8192
v_mfma_f32_16x16x32_bf16 a[20:23], v[4:7], v[72:75], a[20:23]
ds_read_b64_tr_b16 v[70:71], v233 offset:10240
v_mfma_f32_16x16x32_bf16 a[80:83], v[8:11], v[72:75], a[80:83]
v_mfma_f32_16x16x32_bf16 a[84:87], v[12:15], v[72:75], a[84:87]
ds_read_b128 v[44:47], v225 offset:6144
s_add_u32 m0, m0, 4096
s_nop 0
buffer_load_dwordx4 v221, s[40:43], s8 offen offset:0 lds
v_mfma_f32_16x16x32_bf16 a[144:147], v[16:19], v[72:75], a[144:147]
v_mfma_f32_16x16x32_bf16 a[148:151], v[20:23], v[72:75], a[148:151]
v_mfma_f32_16x16x32_bf16 a[208:211], v[24:27], v[72:75], a[208:211]
v_mfma_f32_16x16x32_bf16 a[212:215], v[28:31], v[72:75], a[212:215]
s_waitcnt lgkmcnt(8)
v_mfma_f32_16x16x32_bf16 a[24:27], v[0:3], v[76:79], a[24:27]
ds_read_b64_tr_b16 v[72:73], v234 offset:8192
buffer_load_dwordx4 v222, s[40:43], s9 offen offset:1024 lds
v_mfma_f32_16x16x32_bf16 a[28:31], v[4:7], v[76:79], a[28:31]
ds_read_b64_tr_b16 v[74:75], v234 offset:10240
v_mfma_f32_16x16x32_bf16 a[88:91], v[8:11], v[76:79], a[88:91]
ds_read_b128 v[48:51], v225 offset:8192
v_mfma_f32_16x16x32_bf16 a[92:95], v[12:15], v[76:79], a[92:95]
v_mfma_f32_16x16x32_bf16 a[152:155], v[16:19], v[76:79], a[152:155]
buffer_load_dwordx4 v221, s[40:43], s10 offen offset:2048 lds
v_mfma_f32_16x16x32_bf16 a[156:159], v[20:23], v[76:79], a[156:159]
v_mfma_f32_16x16x32_bf16 a[216:219], v[24:27], v[76:79], a[216:219]
v_mfma_f32_16x16x32_bf16 a[220:223], v[28:31], v[76:79], a[220:223]
s_waitcnt lgkmcnt(7)
v_mfma_f32_16x16x32_bf16 a[32:35], v[0:3], v[64:67], a[32:35]
ds_read_b64_tr_b16 v[76:77], v235 offset:8192
v_mfma_f32_16x16x32_bf16 a[36:39], v[4:7], v[64:67], a[36:39]
ds_read_b64_tr_b16 v[78:79], v235 offset:10240
buffer_load_dwordx4 v222, s[40:43], s11 offen offset:3072 lds
v_mfma_f32_16x16x32_bf16 a[96:99], v[8:11], v[64:67], a[96:99]
v_mfma_f32_16x16x32_bf16 a[100:103], v[12:15], v[64:67], a[100:103]
ds_read_b128 v[52:55], v225 offset:10240
v_mfma_f32_16x16x32_bf16 a[160:163], v[16:19], v[64:67], a[160:163]
v_mfma_f32_16x16x32_bf16 a[164:167], v[20:23], v[64:67], a[164:167]
v_mfma_f32_16x16x32_bf16 a[224:227], v[24:27], v[64:67], a[224:227]
v_add_u32 v221, 128, v221
v_add_u32 v222, 128, v222
v_mfma_f32_16x16x32_bf16 a[228:231], v[28:31], v[64:67], a[228:231]
s_waitcnt lgkmcnt(7)
v_mfma_f32_16x16x32_bf16 a[40:43], v[0:3], v[68:71], a[40:43]
ds_read_b64_tr_b16 v[64:65], v232 offset:4096
v_mfma_f32_16x16x32_bf16 a[44:47], v[4:7], v[68:71], a[44:47]
ds_read_b64_tr_b16 v[66:67], v232 offset:6144
v_mfma_f32_16x16x32_bf16 a[104:107], v[8:11], v[68:71], a[104:107]
ds_read_b128 v[56:59], v225 offset:12288
s_add_u32 m0, s27, s68
s_add_u32 m0, m0, 65536
s_nop 0
buffer_load_dwordx4 v223, s[44:47], s12 offen offset:0 lds
v_mfma_f32_16x16x32_bf16 a[108:111], v[12:15], v[68:71], a[108:111]
v_mfma_f32_16x16x32_bf16 a[168:171], v[16:19], v[68:71], a[168:171]
v_mfma_f32_16x16x32_bf16 a[172:175], v[20:23], v[68:71], a[172:175]
v_mfma_f32_16x16x32_bf16 a[232:235], v[24:27], v[68:71], a[232:235]
v_mfma_f32_16x16x32_bf16 a[236:239], v[28:31], v[68:71], a[236:239]
buffer_load_dwordx4 v223, s[44:47], s13 offen offset:1024 lds
s_waitcnt lgkmcnt(7)
v_mfma_f32_16x16x32_bf16 a[48:51], v[0:3], v[72:75], a[48:51]
ds_read_b64_tr_b16 v[68:69], v233 offset:4096
v_mfma_f32_16x16x32_bf16 a[52:55], v[4:7], v[72:75], a[52:55]
ds_read_b64_tr_b16 v[70:71], v233 offset:6144
v_mfma_f32_16x16x32_bf16 a[112:115], v[8:11], v[72:75], a[112:115]
v_mfma_f32_16x16x32_bf16 a[116:119], v[12:15], v[72:75], a[116:119]
ds_read_b128 v[60:63], v225 offset:14336
buffer_load_dwordx4 v223, s[44:47], s14 offen offset:2048 lds
v_mfma_f32_16x16x32_bf16 a[176:179], v[16:19], v[72:75], a[176:179]
v_mfma_f32_16x16x32_bf16 a[180:183], v[20:23], v[72:75], a[180:183]
v_mfma_f32_16x16x32_bf16 a[240:243], v[24:27], v[72:75], a[240:243]
v_mfma_f32_16x16x32_bf16 a[244:247], v[28:31], v[72:75], a[244:247]
s_waitcnt lgkmcnt(7)
v_mfma_f32_16x16x32_bf16 a[56:59], v[0:3], v[76:79], a[56:59]
ds_read_b64_tr_b16 v[72:73], v234 offset:4096
buffer_load_dwordx4 v223, s[44:47], s15 offen offset:3072 lds
v_mfma_f32_16x16x32_bf16 a[60:63], v[4:7], v[76:79], a[60:63]
ds_read_b64_tr_b16 v[74:75], v234 offset:6144
v_mfma_f32_16x16x32_bf16 a[120:123], v[8:11], v[76:79], a[120:123]
v_mfma_f32_16x16x32_bf16 a[124:127], v[12:15], v[76:79], a[124:127]
v_mfma_f32_16x16x32_bf16 a[184:187], v[16:19], v[76:79], a[184:187]
v_mfma_f32_16x16x32_bf16 a[188:191], v[20:23], v[76:79], a[188:191]
s_add_u32 m0, m0, 4096
s_nop 0
buffer_load_dwordx4 v223, s[44:47], s16 offen offset:0 lds
v_mfma_f32_16x16x32_bf16 a[248:251], v[24:27], v[76:79], a[248:251]
v_mfma_f32_16x16x32_bf16 a[252:255], v[28:31], v[76:79], a[252:255]
s_waitcnt lgkmcnt(6)
v_mfma_f32_16x16x32_bf16 a[0:3], v[32:35], v[64:67], a[0:3]
ds_read_b64_tr_b16 v[76:77], v235 offset:4096
v_mfma_f32_16x16x32_bf16 a[4:7], v[36:39], v[64:67], a[4:7]
ds_read_b64_tr_b16 v[78:79], v235 offset:6144
buffer_load_dwordx4 v223, s[44:47], s17 offen offset:1024 lds
v_mfma_f32_16x16x32_bf16 a[64:67], v[40:43], v[64:67], a[64:67]
v_mfma_f32_16x16x32_bf16 a[68:71], v[44:47], v[64:67], a[68:71]
v_mfma_f32_16x16x32_bf16 a[128:131], v[48:51], v[64:67], a[128:131]
v_mfma_f32_16x16x32_bf16 a[132:135], v[52:55], v[64:67], a[132:135]
s_waitcnt lgkmcnt(7)
v_mfma_f32_16x16x32_bf16 a[192:195], v[56:59], v[64:67], a[192:195]
buffer_load_dwordx4 v223, s[44:47], s18 offen offset:2048 lds
s_waitcnt lgkmcnt(4)
v_mfma_f32_16x16x32_bf16 a[196:199], v[60:63], v[64:67], a[196:199]
v_mfma_f32_16x16x32_bf16 a[8:11], v[32:35], v[68:71], a[8:11]
ds_read_b64_tr_b16 v[64:65], v232 offset:12288
v_mfma_f32_16x16x32_bf16 a[12:15], v[36:39], v[68:71], a[12:15]
ds_read_b64_tr_b16 v[66:67], v232 offset:14336
v_mfma_f32_16x16x32_bf16 a[72:75], v[40:43], v[68:71], a[72:75]
v_mfma_f32_16x16x32_bf16 a[76:79], v[44:47], v[68:71], a[76:79]
buffer_load_dwordx4 v223, s[44:47], s19 offen offset:3072 lds
v_mfma_f32_16x16x32_bf16 a[136:139], v[48:51], v[68:71], a[136:139]
v_mfma_f32_16x16x32_bf16 a[140:143], v[52:55], v[68:71], a[140:143]
v_mfma_f32_16x16x32_bf16 a[200:203], v[56:59], v[68:71], a[200:203]
v_mfma_f32_16x16x32_bf16 a[204:207], v[60:63], v[68:71], a[204:207]
v_add_u32 v223, s96, v223
s_xor_b32 s27, s27, 32768
s_waitcnt lgkmcnt(4)
v_mfma_f32_16x16x32_bf16 a[16:19], v[32:35], v[72:75], a[16:19]
ds_read_b64_tr_b16 v[68:69], v233 offset:12288
v_mfma_f32_16x16x32_bf16 a[20:23], v[36:39], v[72:75], a[20:23]
ds_read_b64_tr_b16 v[70:71], v233 offset:14336
v_mfma_f32_16x16x32_bf16 a[80:83], v[40:43], v[72:75], a[80:83]
v_mfma_f32_16x16x32_bf16 a[84:87], v[44:47], v[72:75], a[84:87]
v_mfma_f32_16x16x32_bf16 a[144:147], v[48:51], v[72:75], a[144:147]
buffer_load_dword v220, v217, s[44:47], s73 offen offset:0
s_add_u32 s73, s73, 524288
v_mfma_f32_16x16x32_bf16 a[148:151], v[52:55], v[72:75], a[148:151]
v_mfma_f32_16x16x32_bf16 a[208:211], v[56:59], v[72:75], a[208:211]
v_mfma_f32_16x16x32_bf16 a[212:215], v[60:63], v[72:75], a[212:215]
s_waitcnt lgkmcnt(4)
v_mfma_f32_16x16x32_bf16 a[24:27], v[32:35], v[76:79], a[24:27]
ds_read_b64_tr_b16 v[72:73], v234 offset:12288
v_mfma_f32_16x16x32_bf16 a[28:31], v[36:39], v[76:79], a[28:31]
ds_read_b64_tr_b16 v[74:75], v234 offset:14336
v_mfma_f32_16x16x32_bf16 a[88:91], v[40:43], v[76:79], a[88:91]
v_mfma_f32_16x16x32_bf16 a[92:95], v[44:47], v[76:79], a[92:95]
v_mfma_f32_16x16x32_bf16 a[152:155], v[48:51], v[76:79], a[152:155]
v_mfma_f32_16x16x32_bf16 a[156:159], v[52:55], v[76:79], a[156:159]
v_mfma_f32_16x16x32_bf16 a[216:219], v[56:59], v[76:79], a[216:219]
v_mfma_f32_16x16x32_bf16 a[220:223], v[60:63], v[76:79], a[220:223]
s_waitcnt lgkmcnt(4)
v_mfma_f32_16x16x32_bf16 a[32:35], v[32:35], v[64:67], a[32:35]
ds_read_b64_tr_b16 v[76:77], v235 offset:12288
v_mfma_f32_16x16x32_bf16 a[36:39], v[36:39], v[64:67], a[36:39]
ds_read_b64_tr_b16 v[78:79], v235 offset:14336
v_mfma_f32_16x16x32_bf16 a[96:99], v[40:43], v[64:67], a[96:99]
v_xor_b32 v232, 0x8000, v232
v_mfma_f32_16x16x32_bf16 a[100:103], v[44:47], v[64:67], a[100:103]
v_mfma_f32_16x16x32_bf16 a[160:163], v[48:51], v[64:67], a[160:163]
v_xor_b32 v233, 0x8000, v233
v_mfma_f32_16x16x32_bf16 a[164:167], v[52:55], v[64:67], a[164:167]
v_mfma_f32_16x16x32_bf16 a[224:227], v[56:59], v[64:67], a[224:227]
v_xor_b32 v234, 0x8000, v234
v_mfma_f32_16x16x32_bf16 a[228:231], v[60:63], v[64:67], a[228:231]
s_waitcnt lgkmcnt(4)
v_mfma_f32_16x16x32_bf16 a[40:43], v[32:35], v[68:71], a[40:43]
v_mfma_f32_16x16x32_bf16 a[44:47], v[36:39], v[68:71], a[44:47]
v_xor_b32 v235, 0x8000, v235
v_mfma_f32_16x16x32_bf16 a[104:107], v[40:43], v[68:71], a[104:107]
v_mfma_f32_16x16x32_bf16 a[108:111], v[44:47], v[68:71], a[108:111]
v_xor_b32 v224, 0x8000, v224
v_mfma_f32_16x16x32_bf16 a[168:171], v[48:51], v[68:71], a[168:171]
v_mfma_f32_16x16x32_bf16 a[172:175], v[52:55], v[68:71], a[172:175]
v_xor_b32 v225, 0x8000, v225
v_mfma_f32_16x16x32_bf16 a[232:235], v[56:59], v[68:71], a[232:235]
v_mfma_f32_16x16x32_bf16 a[236:239], v[60:63], v[68:71], a[236:239]
s_waitcnt lgkmcnt(2)
v_mfma_f32_16x16x32_bf16 a[48:51], v[32:35], v[72:75], a[48:51]
s_waitcnt vmcnt(1) lgkmcnt(0)
s_barrier
ds_read_b64_tr_b16 v[64:65], v232 offset:0
v_mfma_f32_16x16x32_bf16 a[52:55], v[36:39], v[72:75], a[52:55]
ds_read_b64_tr_b16 v[66:67], v232 offset:2048
v_mfma_f32_16x16x32_bf16 a[112:115], v[40:43], v[72:75], a[112:115]
)ASM"
R"ASM(
ds_read_b128 v[0:3], v224 offset:0
v_mfma_f32_16x16x32_bf16 a[116:119], v[44:47], v[72:75], a[116:119]
ds_read_b128 v[4:7], v224 offset:2048
v_mfma_f32_16x16x32_bf16 a[176:179], v[48:51], v[72:75], a[176:179]
ds_read_b128 v[8:11], v224 offset:4096
v_mfma_f32_16x16x32_bf16 a[180:183], v[52:55], v[72:75], a[180:183]
ds_read_b128 v[12:15], v224 offset:6144
v_mfma_f32_16x16x32_bf16 a[240:243], v[56:59], v[72:75], a[240:243]
v_mfma_f32_16x16x32_bf16 a[244:247], v[60:63], v[72:75], a[244:247]
ds_read_b64_tr_b16 v[68:69], v233 offset:0
v_mfma_f32_16x16x32_bf16 a[56:59], v[32:35], v[76:79], a[56:59]
ds_read_b64_tr_b16 v[70:71], v233 offset:2048
v_mfma_f32_16x16x32_bf16 a[60:63], v[36:39], v[76:79], a[60:63]
ds_read_b128 v[16:19], v224 offset:8192
v_mfma_f32_16x16x32_bf16 a[120:123], v[40:43], v[76:79], a[120:123]
ds_read_b128 v[20:23], v224 offset:10240
v_mfma_f32_16x16x32_bf16 a[124:127], v[44:47], v[76:79], a[124:127]
ds_read_b128 v[24:27], v224 offset:12288
v_mfma_f32_16x16x32_bf16 a[184:187], v[48:51], v[76:79], a[184:187]
ds_read_b128 v[28:31], v224 offset:14336
v_mfma_f32_16x16x32_bf16 a[188:191], v[52:55], v[76:79], a[188:191]
v_mfma_f32_16x16x32_bf16 a[248:251], v[56:59], v[76:79], a[248:251]
ds_read_b64_tr_b16 v[72:73], v234 offset:0
v_mfma_f32_16x16x32_bf16 a[252:255], v[60:63], v[76:79], a[252:255]
ds_read_b64_tr_b16 v[74:75], v234 offset:2048
s_swappc_b64 s[24:25], s[20:21]
s_add_u32 s26, s26, 1
s_bitcmp1_b32 7928, s26
s_cbranch_scc1 .LVx
s_branch .LV
.p2align 3
.LVx:
s_waitcnt lgkmcnt(11)
v_mfma_f32_16x16x32_bf16 a[0:3], v[0:3], v[64:67], a[0:3]
ds_read_b64_tr_b16 v[76:77], v235 offset:0
s_waitcnt lgkmcnt(11)
v_mfma_f32_16x16x32_bf16 a[4:7], v[4:7], v[64:67], a[4:7]
ds_read_b64_tr_b16 v[78:79], v235 offset:2048
s_add_u32 m0, s27, s68
s_nop 0
buffer_load_dwordx4 v221, s[40:43], s4 offen offset:0 lds
s_waitcnt lgkmcnt(11)
v_mfma_f32_16x16x32_bf16 a[64:67], v[8:11], v[64:67], a[64:67]
ds_read_b128 v[32:35], v225 offset:0
s_waitcnt lgkmcnt(11)
v_mfma_f32_16x16x32_bf16 a[68:71], v[12:15], v[64:67], a[68:71]
ds_read_b128 v[36:39], v225 offset:2048
s_waitcnt lgkmcnt(9)
v_mfma_f32_16x16x32_bf16 a[128:131], v[16:19], v[64:67], a[128:131]
buffer_load_dwordx4 v222, s[40:43], s5 offen offset:1024 lds
s_waitcnt lgkmcnt(8)
v_mfma_f32_16x16x32_bf16 a[132:135], v[20:23], v[64:67], a[132:135]
s_waitcnt lgkmcnt(7)
v_mfma_f32_16x16x32_bf16 a[192:195], v[24:27], v[64:67], a[192:195]
s_waitcnt lgkmcnt(6)
v_mfma_f32_16x16x32_bf16 a[196:199], v[28:31], v[64:67], a[196:199]
buffer_load_dwordx4 v221, s[40:43], s6 offen offset:2048 lds
v_mfma_f32_16x16x32_bf16 a[8:11], v[0:3], v[68:71], a[8:11]
ds_read_b64_tr_b16 v[64:65], v232 offset:8192
v_mfma_f32_16x16x32_bf16 a[12:15], v[4:7], v[68:71], a[12:15]
ds_read_b64_tr_b16 v[66:67], v232 offset:10240
v_mfma_f32_16x16x32_bf16 a[72:75], v[8:11], v[68:71], a[72:75]
ds_read_b128 v[40:43], v225 offset:4096
v_mfma_f32_16x16x32_bf16 a[76:79], v[12:15], v[68:71], a[76:79]
buffer_load_dwordx4 v222, s[40:43], s7 offen offset:3072 lds
v_mfma_f32_16x16x32_bf16 a[136:139], v[16:19], v[68:71], a[136:139]
v_mfma_f32_16x16x32_bf16 a[140:143], v[20:23], v[68:71], a[140:143]
v_mfma_f32_16x16x32_bf16 a[200:203], v[24:27], v[68:71], a[200:203]
s_add_u32 m0, m0, 4096
s_nop 0
buffer_load_dwordx4 v221, s[40:43], s8 offen offset:0 lds
v_mfma_f32_16x16x32_bf16 a[204:207], v[28:31], v[68:71], a[204:207]
s_waitcnt lgkmcnt(7)
v_mfma_f32_16x16x32_bf16 a[16:19], v[0:3], v[72:75], a[16:19]
ds_read_b64_tr_b16 v[68:69], v233 offset:8192
v_mfma_f32_16x16x32_bf16 a[20:23], v[4:7], v[72:75], a[20:23]
ds_read_b64_tr_b16 v[70:71], v233 offset:10240
v_mfma_f32_16x16x32_bf16 a[80:83], v[8:11], v[72:75], a[80:83]
buffer_load_dwordx4 v222, s[40:43], s9 offen offset:1024 lds
v_mfma_f32_16x16x32_bf16 a[84:87], v[12:15], v[72:75], a[84:87]
ds_read_b128 v[44:47], v225 offset:6144
v_mfma_f32_16x16x32_bf16 a[144:147], v[16:19], v[72:75], a[144:147]
v_mfma_f32_16x16x32_bf16 a[148:151], v[20:23], v[72:75], a[148:151]
buffer_load_dwordx4 v221, s[40:43], s10 offen offset:2048 lds
v_mfma_f32_16x16x32_bf16 a[208:211], v[24:27], v[72:75], a[208:211]
v_mfma_f32_16x16x32_bf16 a[212:215], v[28:31], v[72:75], a[212:215]
s_waitcnt lgkmcnt(8)
v_mfma_f32_16x16x32_bf16 a[24:27], v[0:3], v[76:79], a[24:27]
ds_read_b64_tr_b16 v[72:73], v234 offset:8192
buffer_load_dwordx4 v222, s[40:43], s11 offen offset:3072 lds
v_mfma_f32_16x16x32_bf16 a[28:31], v[4:7], v[76:79], a[28:31]
ds_read_b64_tr_b16 v[74:75], v234 offset:10240
v_mfma_f32_16x16x32_bf16 a[88:91], v[8:11], v[76:79], a[88:91]
ds_read_b128 v[48:51], v225 offset:8192
v_mfma_f32_16x16x32_bf16 a[92:95], v[12:15], v[76:79], a[92:95]
v_mfma_f32_16x16x32_bf16 a[152:155], v[16:19], v[76:79], a[152:155]
v_add_u32 v221, 128, v221
v_add_u32 v222, 128, v222
v_mfma_f32_16x16x32_bf16 a[156:159], v[20:23], v[76:79], a[156:159]
v_mfma_f32_16x16x32_bf16 a[216:219], v[24:27], v[76:79], a[216:219]
v_mfma_f32_16x16x32_bf16 a[220:223], v[28:31], v[76:79], a[220:223]
s_add_u32 m0, s27, s68
s_add_u32 m0, m0, 65536
s_nop 0
buffer_load_dwordx4 v223, s[44:47], s12 offen offset:0 lds
s_waitcnt lgkmcnt(7)
v_mfma_f32_16x16x32_bf16 a[32:35], v[0:3], v[64:67], a[32:35]
ds_read_b64_tr_b16 v[76:77], v235 offset:8192
v_mfma_f32_16x16x32_bf16 a[36:39], v[4:7], v[64:67], a[36:39]
ds_read_b64_tr_b16 v[78:79], v235 offset:10240
v_mfma_f32_16x16x32_bf16 a[96:99], v[8:11], v[64:67], a[96:99]
v_mfma_f32_16x16x32_bf16 a[100:103], v[12:15], v[64:67], a[100:103]
ds_read_b128 v[52:55], v225 offset:10240
buffer_load_dwordx4 v223, s[44:47], s13 offen offset:1024 lds
v_mfma_f32_16x16x32_bf16 a[160:163], v[16:19], v[64:67], a[160:163]
v_mfma_f32_16x16x32_bf16 a[164:167], v[20:23], v[64:67], a[164:167]
v_mfma_f32_16x16x32_bf16 a[224:227], v[24:27], v[64:67], a[224:227]
buffer_load_dwordx4 v223, s[44:47], s14 offen offset:2048 lds
v_mfma_f32_16x16x32_bf16 a[228:231], v[28:31], v[64:67], a[228:231]
s_waitcnt lgkmcnt(7)
v_mfma_f32_16x16x32_bf16 a[40:43], v[0:3], v[68:71], a[40:43]
ds_read_b64_tr_b16 v[64:65], v232 offset:4096
v_mfma_f32_16x16x32_bf16 a[44:47], v[4:7], v[68:71], a[44:47]
ds_read_b64_tr_b16 v[66:67], v232 offset:6144
v_mfma_f32_16x16x32_bf16 a[104:107], v[8:11], v[68:71], a[104:107]
ds_read_b128 v[56:59], v225 offset:12288
buffer_load_dwordx4 v223, s[44:47], s15 offen offset:3072 lds
v_mfma_f32_16x16x32_bf16 a[108:111], v[12:15], v[68:71], a[108:111]
v_mfma_f32_16x16x32_bf16 a[168:171], v[16:19], v[68:71], a[168:171]
v_mfma_f32_16x16x32_bf16 a[172:175], v[20:23], v[68:71], a[172:175]
s_add_u32 m0, m0, 4096
s_nop 0
buffer_load_dwordx4 v223, s[44:47], s16 offen offset:0 lds
v_mfma_f32_16x16x32_bf16 a[232:235], v[24:27], v[68:71], a[232:235]
v_mfma_f32_16x16x32_bf16 a[236:239], v[28:31], v[68:71], a[236:239]
s_waitcnt lgkmcnt(7)
v_mfma_f32_16x16x32_bf16 a[48:51], v[0:3], v[72:75], a[48:51]
ds_read_b64_tr_b16 v[68:69], v233 offset:4096
buffer_load_dwordx4 v223, s[44:47], s17 offen offset:1024 lds
v_mfma_f32_16x16x32_bf16 a[52:55], v[4:7], v[72:75], a[52:55]
ds_read_b64_tr_b16 v[70:71], v233 offset:6144
v_mfma_f32_16x16x32_bf16 a[112:115], v[8:11], v[72:75], a[112:115]
v_mfma_f32_16x16x32_bf16 a[116:119], v[12:15], v[72:75], a[116:119]
ds_read_b128 v[60:63], v225 offset:14336
v_mfma_f32_16x16x32_bf16 a[176:179], v[16:19], v[72:75], a[176:179]
buffer_load_dwordx4 v223, s[44:47], s18 offen offset:2048 lds
v_mfma_f32_16x16x32_bf16 a[180:183], v[20:23], v[72:75], a[180:183]
v_mfma_f32_16x16x32_bf16 a[240:243], v[24:27], v[72:75], a[240:243]
v_mfma_f32_16x16x32_bf16 a[244:247], v[28:31], v[72:75], a[244:247]
buffer_load_dwordx4 v223, s[44:47], s19 offen offset:3072 lds
s_waitcnt lgkmcnt(7)
v_mfma_f32_16x16x32_bf16 a[56:59], v[0:3], v[76:79], a[56:59]
ds_read_b64_tr_b16 v[72:73], v234 offset:4096
v_mfma_f32_16x16x32_bf16 a[60:63], v[4:7], v[76:79], a[60:63]
ds_read_b64_tr_b16 v[74:75], v234 offset:6144
v_mfma_f32_16x16x32_bf16 a[120:123], v[8:11], v[76:79], a[120:123]
v_mfma_f32_16x16x32_bf16 a[124:127], v[12:15], v[76:79], a[124:127]
v_add_u32 v223, s96, v223
s_xor_b32 s27, s27, 32768
v_mfma_f32_16x16x32_bf16 a[184:187], v[16:19], v[76:79], a[184:187]
v_mfma_f32_16x16x32_bf16 a[188:191], v[20:23], v[76:79], a[188:191]
v_mfma_f32_16x16x32_bf16 a[248:251], v[24:27], v[76:79], a[248:251]
buffer_load_dword v220, v217, s[44:47], s73 offen offset:0
s_add_u32 s73, s73, 524288
v_mfma_f32_16x16x32_bf16 a[252:255], v[28:31], v[76:79], a[252:255]
s_waitcnt lgkmcnt(6)
v_mfma_f32_16x16x32_bf16 a[0:3], v[32:35], v[64:67], a[0:3]
ds_read_b64_tr_b16 v[76:77], v235 offset:4096
v_mfma_f32_16x16x32_bf16 a[4:7], v[36:39], v[64:67], a[4:7]
ds_read_b64_tr_b16 v[78:79], v235 offset:6144
v_mfma_f32_16x16x32_bf16 a[64:67], v[40:43], v[64:67], a[64:67]
v_mfma_f32_16x16x32_bf16 a[68:71], v[44:47], v[64:67], a[68:71]
v_mfma_f32_16x16x32_bf16 a[128:131], v[48:51], v[64:67], a[128:131]
s_swappc_b64 s[24:25], s[20:21]
v_mfma_f32_16x16x32_bf16 a[132:135], v[52:55], v[64:67], a[132:135]
s_waitcnt lgkmcnt(7)
v_mfma_f32_16x16x32_bf16 a[192:195], v[56:59], v[64:67], a[192:195]
s_waitcnt lgkmcnt(4)
v_mfma_f32_16x16x32_bf16 a[196:199], v[60:63], v[64:67], a[196:199]
v_mfma_f32_16x16x32_bf16 a[8:11], v[32:35], v[68:71], a[8:11]
ds_read_b64_tr_b16 v[64:65], v232 offset:12288
v_mfma_f32_16x16x32_bf16 a[12:15], v[36:39], v[68:71], a[12:15]
ds_read_b64_tr_b16 v[66:67], v232 offset:14336
v_mfma_f32_16x16x32_bf16 a[72:75], v[40:43], v[68:71], a[72:75]
v_mfma_f32_16x16x32_bf16 a[76:79], v[44:47], v[68:71], a[76:79]
v_mfma_f32_16x16x32_bf16 a[136:139], v[48:51], v[68:71], a[136:139]
v_mfma_f32_16x16x32_bf16 a[140:143], v[52:55], v[68:71], a[140:143]
v_mfma_f32_16x16x32_bf16 a[200:203], v[56:59], v[68:71], a[200:203]
v_mfma_f32_16x16x32_bf16 a[204:207], v[60:63], v[68:71], a[204:207]
s_waitcnt lgkmcnt(4)
v_mfma_f32_16x16x32_bf16 a[16:19], v[32:35], v[72:75], a[16:19]
ds_read_b64_tr_b16 v[68:69], v233 offset:12288
s_add_u32 m0, s68, 131072
s_add_u32 s22, s22, s23
buffer_load_dwordx4 v218, s[60:63], s22 offen offset:0 sc1 lds
v_mfma_f32_16x16x32_bf16 a[20:23], v[36:39], v[72:75], a[20:23]
ds_read_b64_tr_b16 v[70:71], v233 offset:14336
v_mfma_f32_16x16x32_bf16 a[80:83], v[40:43], v[72:75], a[80:83]
v_mfma_f32_16x16x32_bf16 a[84:87], v[44:47], v[72:75], a[84:87]
v_mfma_f32_16x16x32_bf16 a[144:147], v[48:51], v[72:75], a[144:147]
)ASM"
R"ASM(
v_mfma_f32_16x16x32_bf16 a[148:151], v[52:55], v[72:75], a[148:151]
v_mfma_f32_16x16x32_bf16 a[208:211], v[56:59], v[72:75], a[208:211]
s_add_u32 m0, s68, 132096
s_add_u32 s22, s22, s23
buffer_load_dwordx4 v218, s[60:63], s22 offen offset:0 sc1 lds
v_mfma_f32_16x16x32_bf16 a[212:215], v[60:63], v[72:75], a[212:215]
s_waitcnt lgkmcnt(4)
v_mfma_f32_16x16x32_bf16 a[24:27], v[32:35], v[76:79], a[24:27]
ds_read_b64_tr_b16 v[72:73], v234 offset:12288
v_mfma_f32_16x16x32_bf16 a[28:31], v[36:39], v[76:79], a[28:31]
ds_read_b64_tr_b16 v[74:75], v234 offset:14336
v_mfma_f32_16x16x32_bf16 a[88:91], v[40:43], v[76:79], a[88:91]
v_mfma_f32_16x16x32_bf16 a[92:95], v[44:47], v[76:79], a[92:95]
v_mfma_f32_16x16x32_bf16 a[152:155], v[48:51], v[76:79], a[152:155]
s_add_u32 m0, s68, 133120
s_add_u32 s22, s22, s23
buffer_load_dwordx4 v218, s[60:63], s22 offen offset:0 sc1 lds
v_mfma_f32_16x16x32_bf16 a[156:159], v[52:55], v[76:79], a[156:159]
v_mfma_f32_16x16x32_bf16 a[216:219], v[56:59], v[76:79], a[216:219]
v_mfma_f32_16x16x32_bf16 a[220:223], v[60:63], v[76:79], a[220:223]
s_waitcnt lgkmcnt(4)
v_mfma_f32_16x16x32_bf16 a[32:35], v[32:35], v[64:67], a[32:35]
ds_read_b64_tr_b16 v[76:77], v235 offset:12288
v_mfma_f32_16x16x32_bf16 a[36:39], v[36:39], v[64:67], a[36:39]
ds_read_b64_tr_b16 v[78:79], v235 offset:14336
v_mfma_f32_16x16x32_bf16 a[96:99], v[40:43], v[64:67], a[96:99]
v_xor_b32 v232, 0x8000, v232
s_add_u32 m0, s68, 134144
s_add_u32 s22, s22, s23
buffer_load_dwordx4 v218, s[60:63], s22 offen offset:0 sc1 lds
v_mfma_f32_16x16x32_bf16 a[100:103], v[44:47], v[64:67], a[100:103]
v_mfma_f32_16x16x32_bf16 a[160:163], v[48:51], v[64:67], a[160:163]
v_xor_b32 v233, 0x8000, v233
v_mfma_f32_16x16x32_bf16 a[164:167], v[52:55], v[64:67], a[164:167]
v_mfma_f32_16x16x32_bf16 a[224:227], v[56:59], v[64:67], a[224:227]
v_xor_b32 v234, 0x8000, v234
v_mfma_f32_16x16x32_bf16 a[228:231], v[60:63], v[64:67], a[228:231]
s_waitcnt lgkmcnt(4)
v_mfma_f32_16x16x32_bf16 a[40:43], v[32:35], v[68:71], a[40:43]
s_add_u32 m0, s68, 135168
s_add_u32 s22, s22, s23
buffer_load_dwordx4 v218, s[60:63], s22 offen offset:0 sc1 lds
v_mfma_f32_16x16x32_bf16 a[44:47], v[36:39], v[68:71], a[44:47]
v_xor_b32 v235, 0x8000, v235
v_mfma_f32_16x16x32_bf16 a[104:107], v[40:43], v[68:71], a[104:107]
v_mfma_f32_16x16x32_bf16 a[108:111], v[44:47], v[68:71], a[108:111]
v_xor_b32 v224, 0x8000, v224
v_mfma_f32_16x16x32_bf16 a[168:171], v[48:51], v[68:71], a[168:171]
v_mfma_f32_16x16x32_bf16 a[172:175], v[52:55], v[68:71], a[172:175]
v_xor_b32 v225, 0x8000, v225
v_mfma_f32_16x16x32_bf16 a[232:235], v[56:59], v[68:71], a[232:235]
s_add_u32 m0, s68, 136192
s_add_u32 s22, s22, s23
buffer_load_dwordx4 v218, s[60:63], s22 offen offset:0 sc1 lds
v_mfma_f32_16x16x32_bf16 a[236:239], v[60:63], v[68:71], a[236:239]
s_waitcnt lgkmcnt(2)
v_mfma_f32_16x16x32_bf16 a[48:51], v[32:35], v[72:75], a[48:51]
s_waitcnt vmcnt(7) lgkmcnt(0)
s_barrier
ds_read_b64_tr_b16 v[64:65], v232 offset:0
v_mfma_f32_16x16x32_bf16 a[52:55], v[36:39], v[72:75], a[52:55]
ds_read_b64_tr_b16 v[66:67], v232 offset:2048
v_mfma_f32_16x16x32_bf16 a[112:115], v[40:43], v[72:75], a[112:115]
ds_read_b128 v[0:3], v224 offset:0
v_mfma_f32_16x16x32_bf16 a[116:119], v[44:47], v[72:75], a[116:119]
ds_read_b128 v[4:7], v224 offset:2048
v_mfma_f32_16x16x32_bf16 a[176:179], v[48:51], v[72:75], a[176:179]
ds_read_b128 v[8:11], v224 offset:4096
s_add_u32 m0, s68, 137216
s_add_u32 s22, s22, s23
buffer_load_dwordx4 v218, s[60:63], s22 offen offset:0 sc1 lds
v_mfma_f32_16x16x32_bf16 a[180:183], v[52:55], v[72:75], a[180:183]
ds_read_b128 v[12:15], v224 offset:6144
v_mfma_f32_16x16x32_bf16 a[240:243], v[56:59], v[72:75], a[240:243]
v_mfma_f32_16x16x32_bf16 a[244:247], v[60:63], v[72:75], a[244:247]
ds_read_b64_tr_b16 v[68:69], v233 offset:0
v_mfma_f32_16x16x32_bf16 a[56:59], v[32:35], v[76:79], a[56:59]
ds_read_b64_tr_b16 v[70:71], v233 offset:2048
v_mfma_f32_16x16x32_bf16 a[60:63], v[36:39], v[76:79], a[60:63]
ds_read_b128 v[16:19], v224 offset:8192
v_mfma_f32_16x16x32_bf16 a[120:123], v[40:43], v[76:79], a[120:123]
ds_read_b128 v[20:23], v224 offset:10240
s_add_u32 m0, s68, 138240
s_add_u32 s22, s22, s23
buffer_load_dwordx4 v218, s[60:63], s22 offen offset:0 sc1 lds
v_mfma_f32_16x16x32_bf16 a[124:127], v[44:47], v[76:79], a[124:127]
ds_read_b128 v[24:27], v224 offset:12288
v_mfma_f32_16x16x32_bf16 a[184:187], v[48:51], v[76:79], a[184:187]
ds_read_b128 v[28:31], v224 offset:14336
v_mfma_f32_16x16x32_bf16 a[188:191], v[52:55], v[76:79], a[188:191]
v_mfma_f32_16x16x32_bf16 a[248:251], v[56:59], v[76:79], a[248:251]
ds_read_b64_tr_b16 v[72:73], v234 offset:0
v_mfma_f32_16x16x32_bf16 a[252:255], v[60:63], v[76:79], a[252:255]
ds_read_b64_tr_b16 v[74:75], v234 offset:2048
s_swappc_b64 s[24:25], s[20:21]
s_add_u32 s26, s26, 1
s_bitcmp1_b32 7928, s26
s_cbranch_scc1 .LVx
s_branch .LV
.Lev1:
v_mov_b32 v239, s29
s_mov_b64 exec, 1
buffer_store_dword v239, off, s[36:39], s80 offset:0 sc1
s_mov_b64 exec, -1
s_add_u32 s28, s68, 131072
v_add_u32 v219, s28, v236
s_swappc_b64 s[20:21], s[24:25]
buffer_load_dword v239, v231, s[36:39], 0 offen offset:0 sc1
s_swappc_b64 s[20:21], s[24:25]
s_mov_b32 s97, 0
.Lp1:
v_cmp_ne_u32 vcc, s29, v239
s_add_u32 s97, s97, 1
s_nop 2
s_cbranch_vccz .Lq1
s_cmp_ge_u32 s97, 400000
s_cbranch_scc1 .Lq1
s_sleep 1
buffer_load_dword v239, v231, s[36:39], 0 offen offset:0 sc1
s_waitcnt vmcnt(0)
s_branch .Lp1
.Lq1:
s_sub_u32 s28, s83, 50331648
s_cmp_eq_u32 s85, 0
s_cselect_b32 s28, 0xc0000000, s28
buffer_load_dwordx4 v[208:211], v236, s[36:39], s28 offen offset:0 sc1
buffer_load_dwordx4 v[212:215], v236, s[36:39], s28 offen offset:1024 sc1
v_mov_b32 v218, v236
s_mov_b32 s23, 1024
s_mov_b32 s60, s36
s_mov_b32 s61, s37
s_mov_b32 s62, s38
s_sub_u32 s22, s82, 1024
s_swappc_b64 s[20:21], s[24:25]
s_swappc_b64 s[20:21], s[24:25]
s_swappc_b64 s[20:21], s[24:25]
s_waitcnt vmcnt(17)
ds_read_b128 v[80:83], v219 offset:0
ds_read_b128 v[84:87], v219 offset:1024
ds_read_b128 v[88:91], v219 offset:2048
ds_read_b128 v[92:95], v219 offset:3072
ds_read_b128 v[96:99], v219 offset:4096
ds_read_b128 v[100:103], v219 offset:5120
ds_read_b128 v[104:107], v219 offset:6144
ds_read_b128 v[108:111], v219 offset:7168
s_swappc_b64 s[20:21], s[24:25]
s_swappc_b64 s[20:21], s[24:25]
s_waitcnt vmcnt(17)
ds_read_b128 v[112:115], v219 offset:0
ds_read_b128 v[116:119], v219 offset:1024
ds_read_b128 v[120:123], v219 offset:2048
ds_read_b128 v[124:127], v219 offset:3072
ds_read_b128 v[128:131], v219 offset:4096
ds_read_b128 v[132:135], v219 offset:5120
ds_read_b128 v[136:139], v219 offset:6144
ds_read_b128 v[140:143], v219 offset:7168
s_swappc_b64 s[20:21], s[24:25]
s_swappc_b64 s[20:21], s[24:25]
s_waitcnt vmcnt(17)
ds_read_b128 v[144:147], v219 offset:0
ds_read_b128 v[148:151], v219 offset:1024
ds_read_b128 v[152:155], v219 offset:2048
ds_read_b128 v[156:159], v219 offset:3072
ds_read_b128 v[160:163], v219 offset:4096
ds_read_b128 v[164:167], v219 offset:5120
ds_read_b128 v[168:171], v219 offset:6144
ds_read_b128 v[172:175], v219 offset:7168
s_sub_u32 s22, s22, 4096
s_swappc_b64 s[20:21], s[24:25]
s_swappc_b64 s[20:21], s[24:25]
s_waitcnt vmcnt(17)
ds_read_b128 v[176:179], v219 offset:4096
ds_read_b128 v[180:183], v219 offset:5120
ds_read_b128 v[184:187], v219 offset:6144
ds_read_b128 v[188:191], v219 offset:7168
v_mov_b32 v218, v226
s_mov_b32 s23, 14336
s_mov_b32 s60, s64
s_mov_b32 s61, s65
s_mov_b32 s62, s66
s_mov_b32 s22, -14336
s_swappc_b64 s[20:21], s[24:25]
s_swappc_b64 s[20:21], s[24:25]
s_cmp_eq_u32 s85, 0
s_cbranch_scc1 .Lnob2
s_waitcnt lgkmcnt(0)
ds_read_b64_tr_b16 v[32:33], v227 offset:0
ds_read_b64_tr_b16 v[34:35], v227 offset:1024
ds_read_b64_tr_b16 v[36:37], v228 offset:0
ds_read_b64_tr_b16 v[38:39], v228 offset:1024
ds_read_b64_tr_b16 v[40:41], v227 offset:2048
ds_read_b64_tr_b16 v[42:43], v227 offset:3072
ds_read_b64_tr_b16 v[44:45], v228 offset:2048
ds_read_b64_tr_b16 v[46:47], v228 offset:3072
ds_read_b64_tr_b16 v[48:49], v227 offset:4096
ds_read_b64_tr_b16 v[50:51], v227 offset:5120
ds_read_b64_tr_b16 v[52:53], v228 offset:4096
ds_read_b64_tr_b16 v[54:55], v228 offset:5120
ds_read_b64_tr_b16 v[56:57], v227 offset:6144
ds_read_b64_tr_b16 v[58:59], v227 offset:7168
)ASM"
R"ASM(
v_mov_b32 v192, 0x3f80
v_mov_b32 v193, 0x3f800000
v_cmp_eq_u32 vcc, 0, v229
v_cmp_eq_u32 s[30:31], 1, v229
s_nop 0
v_cndmask_b32 v60, 0, v192, vcc
v_cndmask_b32 v60, v60, v193, s[30:31]
v_cmp_eq_u32 vcc, 2, v229
v_cmp_eq_u32 s[30:31], 3, v229
s_nop 0
v_cndmask_b32 v61, 0, v192, vcc
v_cndmask_b32 v61, v61, v193, s[30:31]
v_cmp_eq_u32 vcc, 4, v229
v_cmp_eq_u32 s[30:31], 5, v229
s_nop 0
v_cndmask_b32 v62, 0, v192, vcc
v_cndmask_b32 v62, v62, v193, s[30:31]
v_cmp_eq_u32 vcc, 6, v229
v_cmp_eq_u32 s[30:31], 7, v229
s_nop 0
v_cndmask_b32 v63, 0, v192, vcc
v_cndmask_b32 v63, v63, v193, s[30:31]
v_cmp_eq_u32 vcc, -16, v229
v_cmp_eq_u32 s[30:31], -15, v229
s_nop 0
v_cndmask_b32 v76, 0, v192, vcc
v_cndmask_b32 v76, v76, v193, s[30:31]
v_cmp_eq_u32 vcc, -14, v229
v_cmp_eq_u32 s[30:31], -13, v229
s_nop 0
v_cndmask_b32 v77, 0, v192, vcc
v_cndmask_b32 v77, v77, v193, s[30:31]
v_cmp_eq_u32 vcc, -12, v229
v_cmp_eq_u32 s[30:31], -11, v229
s_nop 0
v_cndmask_b32 v78, 0, v192, vcc
v_cndmask_b32 v78, v78, v193, s[30:31]
v_cmp_eq_u32 vcc, -10, v229
v_cmp_eq_u32 s[30:31], -9, v229
s_nop 0
v_cndmask_b32 v79, 0, v192, vcc
v_cndmask_b32 v79, v79, v193, s[30:31]
s_nop 1
v_mfma_f32_16x16x32_bf16 v[192:195], v[60:63], v[208:211], 0
v_mfma_f32_16x16x32_bf16 v[200:203], v[60:63], v[212:215], 0
v_mfma_f32_16x16x32_bf16 v[196:199], v[76:79], v[208:211], 0
v_mfma_f32_16x16x32_bf16 v[204:207], v[76:79], v[212:215], 0
s_waitcnt lgkmcnt(12)
v_mfma_f32_16x16x32_bf16 v[192:195], v[32:35], v[80:83], v[192:195]
ds_read_b64_tr_b16 v[60:61], v228 offset:6144
v_mfma_f32_16x16x32_bf16 v[200:203], v[32:35], v[84:87], v[200:203]
ds_read_b64_tr_b16 v[62:63], v228 offset:7168
s_waitcnt lgkmcnt(12)
v_mfma_f32_16x16x32_bf16 v[196:199], v[36:39], v[80:83], v[196:199]
ds_read_b64_tr_b16 v[32:33], v227 offset:8192
v_mfma_f32_16x16x32_bf16 v[204:207], v[36:39], v[84:87], v[204:207]
ds_read_b64_tr_b16 v[34:35], v227 offset:9216
s_waitcnt lgkmcnt(12)
v_mfma_f32_16x16x32_bf16 v[192:195], v[40:43], v[88:91], v[192:195]
ds_read_b64_tr_b16 v[36:37], v228 offset:8192
v_mfma_f32_16x16x32_bf16 v[200:203], v[40:43], v[92:95], v[200:203]
ds_read_b64_tr_b16 v[38:39], v228 offset:9216
s_waitcnt lgkmcnt(12)
v_mfma_f32_16x16x32_bf16 v[196:199], v[44:47], v[88:91], v[196:199]
ds_read_b64_tr_b16 v[40:41], v227 offset:10240
v_mfma_f32_16x16x32_bf16 v[204:207], v[44:47], v[92:95], v[204:207]
ds_read_b64_tr_b16 v[42:43], v227 offset:11264
s_waitcnt lgkmcnt(12)
v_mfma_f32_16x16x32_bf16 v[192:195], v[48:51], v[96:99], v[192:195]
ds_read_b64_tr_b16 v[44:45], v228 offset:10240
v_mfma_f32_16x16x32_bf16 v[200:203], v[48:51], v[100:103], v[200:203]
ds_read_b64_tr_b16 v[46:47], v228 offset:11264
s_waitcnt lgkmcnt(12)
v_mfma_f32_16x16x32_bf16 v[196:199], v[52:55], v[96:99], v[196:199]
ds_read_b64_tr_b16 v[48:49], v227 offset:12288
v_mfma_f32_16x16x32_bf16 v[204:207], v[52:55], v[100:103], v[204:207]
ds_read_b64_tr_b16 v[50:51], v227 offset:13312
s_waitcnt lgkmcnt(12)
v_mfma_f32_16x16x32_bf16 v[192:195], v[56:59], v[104:107], v[192:195]
ds_read_b64_tr_b16 v[52:53], v228 offset:12288
v_mfma_f32_16x16x32_bf16 v[200:203], v[56:59], v[108:111], v[200:203]
ds_read_b64_tr_b16 v[54:55], v228 offset:13312
s_waitcnt lgkmcnt(12)
v_mfma_f32_16x16x32_bf16 v[196:199], v[60:63], v[104:107], v[196:199]
ds_read_b64_tr_b16 v[56:57], v227 offset:14336
v_mfma_f32_16x16x32_bf16 v[204:207], v[60:63], v[108:111], v[204:207]
ds_read_b64_tr_b16 v[58:59], v227 offset:15360
s_waitcnt lgkmcnt(12)
v_mfma_f32_16x16x32_bf16 v[192:195], v[32:35], v[112:115], v[192:195]
ds_read_b64_tr_b16 v[60:61], v228 offset:14336
v_mfma_f32_16x16x32_bf16 v[200:203], v[32:35], v[116:119], v[200:203]
ds_read_b64_tr_b16 v[62:63], v228 offset:15360
s_waitcnt lgkmcnt(12)
v_mfma_f32_16x16x32_bf16 v[196:199], v[36:39], v[112:115], v[196:199]
ds_read_b64_tr_b16 v[32:33], v227 offset:16384
v_mfma_f32_16x16x32_bf16 v[204:207], v[36:39], v[116:119], v[204:207]
ds_read_b64_tr_b16 v[34:35], v227 offset:17408
s_waitcnt lgkmcnt(12)
v_mfma_f32_16x16x32_bf16 v[192:195], v[40:43], v[120:123], v[192:195]
ds_read_b64_tr_b16 v[36:37], v228 offset:16384
v_mfma_f32_16x16x32_bf16 v[200:203], v[40:43], v[124:127], v[200:203]
ds_read_b64_tr_b16 v[38:39], v228 offset:17408
s_waitcnt lgkmcnt(12)
v_mfma_f32_16x16x32_bf16 v[196:199], v[44:47], v[120:123], v[196:199]
ds_read_b64_tr_b16 v[40:41], v227 offset:18432
v_mfma_f32_16x16x32_bf16 v[204:207], v[44:47], v[124:127], v[204:207]
ds_read_b64_tr_b16 v[42:43], v227 offset:19456
s_waitcnt lgkmcnt(12)
v_mfma_f32_16x16x32_bf16 v[192:195], v[48:51], v[128:131], v[192:195]
ds_read_b64_tr_b16 v[44:45], v228 offset:18432
v_mfma_f32_16x16x32_bf16 v[200:203], v[48:51], v[132:135], v[200:203]
ds_read_b64_tr_b16 v[46:47], v228 offset:19456
s_waitcnt lgkmcnt(12)
v_mfma_f32_16x16x32_bf16 v[196:199], v[52:55], v[128:131], v[196:199]
ds_read_b64_tr_b16 v[48:49], v227 offset:20480
v_mfma_f32_16x16x32_bf16 v[204:207], v[52:55], v[132:135], v[204:207]
ds_read_b64_tr_b16 v[50:51], v227 offset:21504
s_waitcnt lgkmcnt(12)
v_mfma_f32_16x16x32_bf16 v[192:195], v[56:59], v[136:139], v[192:195]
ds_read_b64_tr_b16 v[52:53], v228 offset:20480
v_mfma_f32_16x16x32_bf16 v[200:203], v[56:59], v[140:143], v[200:203]
ds_read_b64_tr_b16 v[54:55], v228 offset:21504
s_waitcnt lgkmcnt(12)
v_mfma_f32_16x16x32_bf16 v[196:199], v[60:63], v[136:139], v[196:199]
ds_read_b64_tr_b16 v[56:57], v227 offset:22528
v_mfma_f32_16x16x32_bf16 v[204:207], v[60:63], v[140:143], v[204:207]
ds_read_b64_tr_b16 v[58:59], v227 offset:23552
s_waitcnt lgkmcnt(12)
v_mfma_f32_16x16x32_bf16 v[192:195], v[32:35], v[144:147], v[192:195]
ds_read_b64_tr_b16 v[60:61], v228 offset:22528
v_mfma_f32_16x16x32_bf16 v[200:203], v[32:35], v[148:151], v[200:203]
ds_read_b64_tr_b16 v[62:63], v228 offset:23552
s_waitcnt lgkmcnt(12)
v_mfma_f32_16x16x32_bf16 v[196:199], v[36:39], v[144:147], v[196:199]
ds_read_b64_tr_b16 v[32:33], v227 offset:24576
v_mfma_f32_16x16x32_bf16 v[204:207], v[36:39], v[148:151], v[204:207]
ds_read_b64_tr_b16 v[34:35], v227 offset:25600
s_waitcnt lgkmcnt(12)
v_mfma_f32_16x16x32_bf16 v[192:195], v[40:43], v[152:155], v[192:195]
ds_read_b64_tr_b16 v[36:37], v228 offset:24576
v_mfma_f32_16x16x32_bf16 v[200:203], v[40:43], v[156:159], v[200:203]
ds_read_b64_tr_b16 v[38:39], v228 offset:25600
s_waitcnt lgkmcnt(12)
v_mfma_f32_16x16x32_bf16 v[196:199], v[44:47], v[152:155], v[196:199]
ds_read_b64_tr_b16 v[40:41], v227 offset:26624
v_mfma_f32_16x16x32_bf16 v[204:207], v[44:47], v[156:159], v[204:207]
ds_read_b64_tr_b16 v[42:43], v227 offset:27648
s_waitcnt lgkmcnt(12)
v_mfma_f32_16x16x32_bf16 v[192:195], v[48:51], v[160:163], v[192:195]
ds_read_b64_tr_b16 v[44:45], v228 offset:26624
v_mfma_f32_16x16x32_bf16 v[200:203], v[48:51], v[164:167], v[200:203]
ds_read_b64_tr_b16 v[46:47], v228 offset:27648
s_waitcnt lgkmcnt(12)
v_mfma_f32_16x16x32_bf16 v[196:199], v[52:55], v[160:163], v[196:199]
v_mfma_f32_16x16x32_bf16 v[204:207], v[52:55], v[164:167], v[204:207]
s_waitcnt lgkmcnt(10)
v_mfma_f32_16x16x32_bf16 v[192:195], v[56:59], v[168:171], v[192:195]
v_mfma_f32_16x16x32_bf16 v[200:203], v[56:59], v[172:175], v[200:203]
s_waitcnt lgkmcnt(8)
v_mfma_f32_16x16x32_bf16 v[196:199], v[60:63], v[168:171], v[196:199]
v_mfma_f32_16x16x32_bf16 v[204:207], v[60:63], v[172:175], v[204:207]
s_waitcnt lgkmcnt(6)
v_mfma_f32_16x16x32_bf16 v[192:195], v[32:35], v[176:179], v[192:195]
v_mfma_f32_16x16x32_bf16 v[200:203], v[32:35], v[180:183], v[200:203]
s_waitcnt lgkmcnt(4)
v_mfma_f32_16x16x32_bf16 v[196:199], v[36:39], v[176:179], v[196:199]
v_mfma_f32_16x16x32_bf16 v[204:207], v[36:39], v[180:183], v[204:207]
s_waitcnt lgkmcnt(2)
v_mfma_f32_16x16x32_bf16 v[192:195], v[40:43], v[184:187], v[192:195]
v_mfma_f32_16x16x32_bf16 v[200:203], v[40:43], v[188:191], v[200:203]
s_waitcnt lgkmcnt(0)
v_mfma_f32_16x16x32_bf16 v[196:199], v[44:47], v[184:187], v[196:199]
v_mfma_f32_16x16x32_bf16 v[204:207], v[44:47], v[188:191], v[204:207]
s_nop 7
v_cvt_pk_bf16_f32 v80, v192, v193
v_cvt_pk_bf16_f32 v81, v194, v195
v_cvt_pk_bf16_f32 v82, v196, v197
v_cvt_pk_bf16_f32 v83, v198, v199
v_cvt_pk_bf16_f32 v84, v200, v201
v_cvt_pk_bf16_f32 v85, v202, v203
v_cvt_pk_bf16_f32 v86, v204, v205
v_cvt_pk_bf16_f32 v87, v206, v207
buffer_store_dwordx4 v[80:83], v236, s[36:39], s83 offen offset:0 sc1
buffer_store_dwordx4 v[84:87], v236, s[36:39], s83 offen offset:1024 sc1
v_add_f32 v96, v192, v193
v_mul_f32 v97, v192, v192
v_fmac_f32 v97, v193, v193
v_add_f32 v96, v96, v194
v_fmac_f32 v97, v194, v194
v_add_f32 v96, v96, v195
v_fmac_f32 v97, v195, v195
v_add_f32 v96, v96, v196
v_fmac_f32 v97, v196, v196
v_add_f32 v96, v96, v197
v_fmac_f32 v97, v197, v197
v_add_f32 v96, v96, v198
v_fmac_f32 v97, v198, v198
v_add_f32 v96, v96, v199
v_fmac_f32 v97, v199, v199
v_mov_b32 v100, v96
)ASM"
R"ASM(
s_nop 1
v_permlane32_swap_b32 v100, v96
s_nop 1
v_add_f32 v96, v96, v100
v_mov_b32 v100, v96
s_nop 1
v_permlane16_swap_b32 v100, v96
s_nop 1
v_add_f32 v96, v96, v100
v_mov_b32 v100, v97
s_nop 1
v_permlane32_swap_b32 v100, v97
s_nop 1
v_add_f32 v97, v97, v100
v_mov_b32 v100, v97
s_nop 1
v_permlane16_swap_b32 v100, v97
s_nop 1
v_add_f32 v97, v97, v100
buffer_store_dwordx2 v[96:97], v230, s[36:39], 0 offen offset:0 sc1
v_add_f32 v98, v200, v201
v_mul_f32 v99, v200, v200
v_fmac_f32 v99, v201, v201
v_add_f32 v98, v98, v202
v_fmac_f32 v99, v202, v202
v_add_f32 v98, v98, v203
v_fmac_f32 v99, v203, v203
v_add_f32 v98, v98, v204
v_fmac_f32 v99, v204, v204
v_add_f32 v98, v98, v205
v_fmac_f32 v99, v205, v205
v_add_f32 v98, v98, v206
v_fmac_f32 v99, v206, v206
v_add_f32 v98, v98, v207
v_fmac_f32 v99, v207, v207
v_mov_b32 v100, v98
s_nop 1
v_permlane32_swap_b32 v100, v98
s_nop 1
v_add_f32 v98, v98, v100
v_mov_b32 v100, v98
s_nop 1
v_permlane16_swap_b32 v100, v98
s_nop 1
v_add_f32 v98, v98, v100
v_mov_b32 v100, v99
s_nop 1
v_permlane32_swap_b32 v100, v99
s_nop 1
v_add_f32 v99, v99, v100
v_mov_b32 v100, v99
s_nop 1
v_permlane16_swap_b32 v100, v99
s_nop 1
v_add_f32 v99, v99, v100
buffer_store_dwordx2 v[98:99], v230, s[36:39], 0 offen offset:128 sc1
.Lnob2:
v_mov_b32 v218, v236
s_mov_b32 s23, 1024
s_mov_b32 s60, s36
s_mov_b32 s61, s37
s_mov_b32 s62, s38
s_sub_u32 s22, s84, 50332672
s_barrier
s_swappc_b64 s[20:21], s[24:25]
s_swappc_b64 s[20:21], s[24:25]
v_mov_b32 v239, s29
s_mov_b64 exec, 1
buffer_store_dword v239, off, s[36:39], s81 offset:0 sc1
s_mov_b64 exec, -1
s_swappc_b64 s[20:21], s[24:25]
s_waitcnt vmcnt(17)
ds_read_b128 v[80:83], v219 offset:0
ds_read_b128 v[84:87], v219 offset:1024
ds_read_b128 v[88:91], v219 offset:2048
ds_read_b128 v[92:95], v219 offset:3072
ds_read_b128 v[96:99], v219 offset:4096
ds_read_b128 v[100:103], v219 offset:5120
ds_read_b128 v[104:107], v219 offset:6144
ds_read_b128 v[108:111], v219 offset:7168
s_swappc_b64 s[20:21], s[24:25]
s_swappc_b64 s[20:21], s[24:25]
s_waitcnt vmcnt(17)
ds_read_b128 v[112:115], v219 offset:0
ds_read_b128 v[116:119], v219 offset:1024
ds_read_b128 v[120:123], v219 offset:2048
ds_read_b128 v[124:127], v219 offset:3072
ds_read_b128 v[128:131], v219 offset:4096
ds_read_b128 v[132:135], v219 offset:5120
ds_read_b128 v[136:139], v219 offset:6144
ds_read_b128 v[140:143], v219 offset:7168
s_swappc_b64 s[20:21], s[24:25]
s_swappc_b64 s[20:21], s[24:25]
s_waitcnt vmcnt(17)
ds_read_b128 v[144:147], v219 offset:0
ds_read_b128 v[148:151], v219 offset:1024
ds_read_b128 v[152:155], v219 offset:2048
ds_read_b128 v[156:159], v219 offset:3072
ds_read_b128 v[160:163], v219 offset:4096
ds_read_b128 v[164:167], v219 offset:5120
ds_read_b128 v[168:171], v219 offset:6144
ds_read_b128 v[172:175], v219 offset:7168
s_sub_u32 s22, s22, 4096
s_swappc_b64 s[20:21], s[24:25]
buffer_load_dword v239, v243, s[36:39], 0 offen offset:0 sc1
v_lshrrev_b32 v200, 4, v236
v_add_u32 v200, s87, v200
v_and_b32 v201, 3, v200
v_lshrrev_b32 v200, 2, v200
v_mul_u32_u24 v201, 128, v201
v_mul_u32_u24 v200, 8192, v200
v_add_u32 v200, v200, v201
s_bfm_b64 exec, 28, 0
buffer_load_dword v201, v200, s[52:55], 0 offen offset:0 sc1
s_mov_b64 exec, -1
v_lshrrev_b32 v202, 4, v236
v_add_u32 v202, s87, v202
v_and_b32 v203, 3, v202
v_lshrrev_b32 v202, 2, v202
v_mul_u32_u24 v203, 72, v203
v_mul_u32_u24 v202, 3584, v202
v_add_u32 v202, v202, v203
s_bfm_b64 exec, 28, 0
buffer_load_dword v203, v202, s[88:91], 0 offen offset:0 sc1
s_mov_b64 exec, -1
s_getpc_b64 s[92:93]
.Lpc192:
s_sub_u32 s92, s92, .Lpc192-.Lbeg
s_subb_u32 s93, s93, 0
s_mov_b32 s94, .Lend-.Lbeg
s_mov_b32 s95, 0x20000
buffer_load_dwordx4 v[192:195], v236, s[92:95], s79 offen offset:0
s_swappc_b64 s[20:21], s[24:25]
ds_read_b128 v[176:179], v219 offset:4096
ds_read_b128 v[180:183], v219 offset:5120
ds_read_b128 v[184:187], v219 offset:6144
ds_read_b128 v[188:191], v219 offset:7168
s_mov_b32 s97, 0
.Lp2:
v_cmp_ne_u32 vcc, s29, v239
s_add_u32 s97, s97, 1
s_nop 2
s_cbranch_vccz .Lq2
s_cmp_ge_u32 s97, 400000
s_cbranch_scc1 .Lq2
s_sleep 1
buffer_load_dword v239, v243, s[36:39], 0 offen offset:0 sc1
s_waitcnt vmcnt(0)
s_branch .Lp2
.Lq2:
s_swappc_b64 s[20:21], s[24:25]
s_mov_b32 s40, s52
s_mov_b32 s41, s53
s_mov_b32 s42, s54
s_mov_b32 s44, s52
s_mov_b32 s45, s53
s_mov_b32 s46, s54
s_mov_b32 s4, s12
s_mov_b32 s5, s13
s_mov_b32 s6, s14
s_mov_b32 s7, s15
s_mov_b32 s8, s16
s_mov_b32 s9, s17
s_mov_b32 s10, s18
s_mov_b32 s11, s19
v_mov_b32 v221, v251
v_mov_b32 v222, v251
v_add_u32 v223, 2097152, v251
s_swappc_b64 s[20:21], s[24:25]
v_mov_b32 v221, v219
v_readlane_b32 s44, v237, 0
v_readlane_b32 s45, v237, 1
v_readlane_b32 s46, v237, 2
v_readlane_b32 s56, v237, 3
v_readlane_b32 s57, v237, 4
v_readlane_b32 s58, v237, 5
v_readlane_b32 s48, v237, 6
v_readlane_b32 s49, v237, 7
v_readlane_b32 s50, v237, 8
v_readlane_b32 s60, v237, 9
v_readlane_b32 s61, v237, 10
v_readlane_b32 s62, v237, 11
v_readlane_b32 s64, v237, 12
v_readlane_b32 s65, v237, 13
v_readlane_b32 s66, v237, 14
v_readlane_b32 s67, v237, 15
v_readlane_b32 s69, v237, 16
v_readlane_b32 s70, v237, 17
v_readlane_b32 s72, v237, 18
v_readlane_b32 s73, v237, 19
v_readlane_b32 s74, v237, 20
v_readlane_b32 s75, v237, 21
v_readlane_b32 s78, v237, 22
v_readlane_b32 s79, v237, 23
v_readlane_b32 s80, v237, 24
v_readlane_b32 s81, v237, 25
v_readlane_b32 s82, v237, 26
v_readlane_b32 s83, v237, 27
v_readlane_b32 s85, v237, 28
v_readlane_b32 s86, v237, 29
)ASM"
R"ASM(
v_readlane_b32 s40, v237, 30
v_readlane_b32 s41, v237, 31
v_and_b32 v232, 0x1fff, v232
v_and_b32 v233, 0x1fff, v233
v_and_b32 v234, 0x1fff, v234
v_and_b32 v235, 0x1fff, v235
s_waitcnt lgkmcnt(0)
v_add_u32 v216, 0, v232
v_add_u32 v217, 0, v233
v_add_u32 v218, 0, v234
v_add_u32 v219, 0, v235
ds_read_b64_tr_b16 v[64:65], v216 offset:0
ds_read_b64_tr_b16 v[66:67], v216 offset:2048
ds_read_b64_tr_b16 v[68:69], v217 offset:0
ds_read_b64_tr_b16 v[70:71], v217 offset:2048
ds_read_b64_tr_b16 v[72:73], v218 offset:0
ds_read_b64_tr_b16 v[74:75], v218 offset:2048
ds_read_b64_tr_b16 v[76:77], v219 offset:0
ds_read_b64_tr_b16 v[78:79], v219 offset:2048
ds_read_b64_tr_b16 v[192:193], v216 offset:4096
ds_read_b64_tr_b16 v[194:195], v216 offset:6144
ds_read_b64_tr_b16 v[196:197], v217 offset:4096
ds_read_b64_tr_b16 v[198:199], v217 offset:6144
ds_read_b64_tr_b16 v[200:201], v218 offset:4096
ds_read_b64_tr_b16 v[202:203], v218 offset:6144
s_waitcnt lgkmcnt(6)
v_mfma_f32_16x16x32_bf16 v[0:3], v[64:67], v[80:83], 0
ds_read_b64_tr_b16 v[204:205], v219 offset:4096
s_add_u32 m0, s68, 131072
s_add_u32 s28, s84, 0
buffer_load_dwordx4 v236, s[36:39], s28 offen offset:0 sc1 lds
v_mfma_f32_16x16x32_bf16 v[8:11], v[64:67], v[84:87], 0
ds_read_b64_tr_b16 v[206:207], v219 offset:6144
v_mfma_f32_16x16x32_bf16 v[4:7], v[68:71], v[80:83], 0
ds_read_b64_tr_b16 v[64:65], v216 offset:8192
v_mfma_f32_16x16x32_bf16 v[12:15], v[68:71], v[84:87], 0
ds_read_b64_tr_b16 v[66:67], v216 offset:10240
buffer_load_dwordx4 v236, s[36:39], s28 offen offset:1024 sc1 lds
v_mfma_f32_16x16x32_bf16 v[16:19], v[72:75], v[80:83], 0
ds_read_b64_tr_b16 v[68:69], v217 offset:8192
s_add_u32 s98, s76, 65536
v_accvgpr_read_b32 v208, a128
v_mfma_f32_16x16x32_bf16 v[24:27], v[72:75], v[84:87], 0
ds_read_b64_tr_b16 v[70:71], v217 offset:10240
v_accvgpr_read_b32 v209, a129
v_accvgpr_read_b32 v210, a130
v_mfma_f32_16x16x32_bf16 v[20:23], v[76:79], v[80:83], 0
ds_read_b64_tr_b16 v[72:73], v218 offset:8192
v_accvgpr_read_b32 v211, a131
v_accvgpr_read_b32 v212, a132
v_mfma_f32_16x16x32_bf16 v[28:31], v[76:79], v[84:87], 0
ds_read_b64_tr_b16 v[74:75], v218 offset:10240
buffer_load_dwordx4 v236, s[36:39], s28 offen offset:2048 sc1 lds
s_waitcnt lgkmcnt(6)
v_mfma_f32_16x16x32_bf16 v[0:3], v[192:195], v[88:91], v[0:3]
ds_read_b64_tr_b16 v[76:77], v219 offset:8192
v_accvgpr_read_b32 v213, a133
v_accvgpr_read_b32 v214, a134
v_mfma_f32_16x16x32_bf16 v[8:11], v[192:195], v[92:95], v[8:11]
ds_read_b64_tr_b16 v[78:79], v219 offset:10240
v_accvgpr_read_b32 v215, a135
v_mfma_f32_16x16x32_bf16 v[4:7], v[196:199], v[88:91], v[4:7]
ds_read_b64_tr_b16 v[192:193], v216 offset:12288
buffer_load_dwordx4 v236, s[36:39], s28 offen offset:3072 sc1 lds
v_mfma_f32_16x16x32_bf16 v[12:15], v[196:199], v[92:95], v[12:15]
ds_read_b64_tr_b16 v[194:195], v216 offset:14336
v_cvt_pk_bf16_f32 v208, v208, v209
v_cvt_pk_bf16_f32 v209, v210, v211
v_mfma_f32_16x16x32_bf16 v[16:19], v[200:203], v[88:91], v[16:19]
ds_read_b64_tr_b16 v[196:197], v217 offset:12288
v_cvt_pk_bf16_f32 v210, v212, v213
v_cvt_pk_bf16_f32 v211, v214, v215
v_mfma_f32_16x16x32_bf16 v[24:27], v[200:203], v[92:95], v[24:27]
ds_read_b64_tr_b16 v[198:199], v217 offset:14336
buffer_store_dwordx4 v[208:211], v236, s[36:39], s98 offen offset:0 sc1
v_mfma_f32_16x16x32_bf16 v[20:23], v[204:207], v[88:91], v[20:23]
ds_read_b64_tr_b16 v[200:201], v218 offset:12288
s_add_u32 m0, s68, 135168
s_add_u32 s28, s84, 4096
buffer_load_dwordx4 v236, s[36:39], s28 offen offset:0 sc1 lds
v_mfma_f32_16x16x32_bf16 v[28:31], v[204:207], v[92:95], v[28:31]
ds_read_b64_tr_b16 v[202:203], v218 offset:14336
s_waitcnt lgkmcnt(6)
v_mfma_f32_16x16x32_bf16 v[0:3], v[64:67], v[96:99], v[0:3]
ds_read_b64_tr_b16 v[204:205], v219 offset:12288
v_accvgpr_read_b32 v224, a136
v_mfma_f32_16x16x32_bf16 v[8:11], v[64:67], v[100:103], v[8:11]
ds_read_b64_tr_b16 v[206:207], v219 offset:14336
buffer_load_dwordx4 v236, s[36:39], s28 offen offset:1024 sc1 lds
v_mfma_f32_16x16x32_bf16 v[4:7], v[68:71], v[96:99], v[4:7]
ds_read_b64_tr_b16 v[64:65], v216 offset:16384
v_accvgpr_read_b32 v225, a137
v_accvgpr_read_b32 v226, a138
v_mfma_f32_16x16x32_bf16 v[12:15], v[68:71], v[100:103], v[12:15]
ds_read_b64_tr_b16 v[66:67], v216 offset:18432
v_accvgpr_read_b32 v227, a139
v_accvgpr_read_b32 v228, a140
v_mfma_f32_16x16x32_bf16 v[16:19], v[72:75], v[96:99], v[16:19]
ds_read_b64_tr_b16 v[68:69], v217 offset:16384
v_accvgpr_read_b32 v229, a141
v_accvgpr_read_b32 v230, a142
v_mfma_f32_16x16x32_bf16 v[24:27], v[72:75], v[100:103], v[24:27]
ds_read_b64_tr_b16 v[70:71], v217 offset:18432
buffer_load_dwordx4 v236, s[36:39], s28 offen offset:2048 sc1 lds
v_mfma_f32_16x16x32_bf16 v[20:23], v[76:79], v[96:99], v[20:23]
ds_read_b64_tr_b16 v[72:73], v218 offset:16384
v_accvgpr_read_b32 v231, a143
v_cvt_pk_bf16_f32 v224, v224, v225
v_mfma_f32_16x16x32_bf16 v[28:31], v[76:79], v[100:103], v[28:31]
ds_read_b64_tr_b16 v[74:75], v218 offset:18432
v_cvt_pk_bf16_f32 v225, v226, v227
s_waitcnt lgkmcnt(6)
v_mfma_f32_16x16x32_bf16 v[0:3], v[192:195], v[104:107], v[0:3]
ds_read_b64_tr_b16 v[76:77], v219 offset:16384
buffer_load_dwordx4 v236, s[36:39], s28 offen offset:3072 sc1 lds
v_mfma_f32_16x16x32_bf16 v[8:11], v[192:195], v[108:111], v[8:11]
ds_read_b64_tr_b16 v[78:79], v219 offset:18432
v_cvt_pk_bf16_f32 v226, v228, v229
v_cvt_pk_bf16_f32 v227, v230, v231
v_mfma_f32_16x16x32_bf16 v[4:7], v[196:199], v[104:107], v[4:7]
ds_read_b64_tr_b16 v[192:193], v216 offset:20480
buffer_store_dwordx4 v[224:227], v236, s[36:39], s98 offen offset:1024 sc1
v_mfma_f32_16x16x32_bf16 v[12:15], v[196:199], v[108:111], v[12:15]
ds_read_b64_tr_b16 v[194:195], v216 offset:22528
v_accvgpr_read_b32 v208, a144
v_accvgpr_read_b32 v209, a145
v_mfma_f32_16x16x32_bf16 v[16:19], v[200:203], v[104:107], v[16:19]
ds_read_b64_tr_b16 v[196:197], v217 offset:20480
s_add_u32 m0, s68, 32768
s_mov_b32 s99, 128
buffer_load_dwordx4 v251, s[52:55], s99 offen offset:0 lds
v_mfma_f32_16x16x32_bf16 v[24:27], v[200:203], v[108:111], v[24:27]
ds_read_b64_tr_b16 v[198:199], v217 offset:22528
v_accvgpr_read_b32 v210, a146
v_accvgpr_read_b32 v211, a147
v_mfma_f32_16x16x32_bf16 v[20:23], v[204:207], v[104:107], v[20:23]
ds_read_b64_tr_b16 v[200:201], v218 offset:20480
v_accvgpr_read_b32 v212, a148
v_accvgpr_read_b32 v213, a149
v_mfma_f32_16x16x32_bf16 v[28:31], v[204:207], v[108:111], v[28:31]
ds_read_b64_tr_b16 v[202:203], v218 offset:22528
s_mov_b32 s99, 130176
buffer_load_dwordx4 v251, s[52:55], s99 offen offset:1024 lds
s_waitcnt lgkmcnt(6)
v_mfma_f32_16x16x32_bf16 v[0:3], v[64:67], v[112:115], v[0:3]
ds_read_b64_tr_b16 v[204:205], v219 offset:20480
v_accvgpr_read_b32 v214, a150
v_accvgpr_read_b32 v215, a151
v_mfma_f32_16x16x32_bf16 v[8:11], v[64:67], v[116:119], v[8:11]
ds_read_b64_tr_b16 v[206:207], v219 offset:22528
v_cvt_pk_bf16_f32 v208, v208, v209
v_cvt_pk_bf16_f32 v209, v210, v211
v_mfma_f32_16x16x32_bf16 v[4:7], v[68:71], v[112:115], v[4:7]
ds_read_b64_tr_b16 v[64:65], v216 offset:24576
v_cvt_pk_bf16_f32 v210, v212, v213
v_cvt_pk_bf16_f32 v211, v214, v215
v_mfma_f32_16x16x32_bf16 v[12:15], v[68:71], v[116:119], v[12:15]
ds_read_b64_tr_b16 v[66:67], v216 offset:26624
s_mov_b32 s99, 30848
buffer_load_dwordx4 v251, s[52:55], s99 offen offset:2048 lds
v_mfma_f32_16x16x32_bf16 v[16:19], v[72:75], v[112:115], v[16:19]
ds_read_b64_tr_b16 v[68:69], v217 offset:24576
buffer_store_dwordx4 v[208:211], v236, s[36:39], s98 offen offset:2048 sc1
v_mfma_f32_16x16x32_bf16 v[24:27], v[72:75], v[116:119], v[24:27]
ds_read_b64_tr_b16 v[70:71], v217 offset:26624
v_accvgpr_read_b32 v224, a152
v_mfma_f32_16x16x32_bf16 v[20:23], v[76:79], v[112:115], v[20:23]
ds_read_b64_tr_b16 v[72:73], v218 offset:24576
s_mov_b32 s99, 160896
buffer_load_dwordx4 v251, s[52:55], s99 offen offset:3072 lds
v_mfma_f32_16x16x32_bf16 v[28:31], v[76:79], v[116:119], v[28:31]
ds_read_b64_tr_b16 v[74:75], v218 offset:26624
v_accvgpr_read_b32 v225, a153
s_waitcnt lgkmcnt(6)
v_mfma_f32_16x16x32_bf16 v[0:3], v[192:195], v[120:123], v[0:3]
ds_read_b64_tr_b16 v[76:77], v219 offset:24576
v_accvgpr_read_b32 v226, a154
v_accvgpr_read_b32 v227, a155
v_mfma_f32_16x16x32_bf16 v[8:11], v[192:195], v[124:127], v[8:11]
ds_read_b64_tr_b16 v[78:79], v219 offset:26624
v_accvgpr_read_b32 v228, a156
v_accvgpr_read_b32 v229, a157
v_mfma_f32_16x16x32_bf16 v[4:7], v[196:199], v[120:123], v[4:7]
ds_read_b64_tr_b16 v[192:193], v216 offset:28672
s_add_u32 m0, m0, 4096
s_mov_b32 s99, 262272
buffer_load_dwordx4 v251, s[52:55], s99 offen offset:0 lds
v_mfma_f32_16x16x32_bf16 v[12:15], v[196:199], v[124:127], v[12:15]
ds_read_b64_tr_b16 v[194:195], v216 offset:30720
v_accvgpr_read_b32 v230, a158
v_accvgpr_read_b32 v231, a159
v_mfma_f32_16x16x32_bf16 v[16:19], v[200:203], v[120:123], v[16:19]
ds_read_b64_tr_b16 v[196:197], v217 offset:28672
v_cvt_pk_bf16_f32 v224, v224, v225
v_mfma_f32_16x16x32_bf16 v[24:27], v[200:203], v[124:127], v[24:27]
ds_read_b64_tr_b16 v[198:199], v217 offset:30720
s_mov_b32 s99, 392320
buffer_load_dwordx4 v251, s[52:55], s99 offen offset:1024 lds
v_mfma_f32_16x16x32_bf16 v[20:23], v[204:207], v[120:123], v[20:23]
ds_read_b64_tr_b16 v[200:201], v218 offset:28672
)ASM"
R"ASM(
v_cvt_pk_bf16_f32 v225, v226, v227
v_mfma_f32_16x16x32_bf16 v[28:31], v[204:207], v[124:127], v[28:31]
ds_read_b64_tr_b16 v[202:203], v218 offset:30720
s_waitcnt lgkmcnt(6)
v_mfma_f32_16x16x32_bf16 v[0:3], v[64:67], v[128:131], v[0:3]
ds_read_b64_tr_b16 v[204:205], v219 offset:28672
v_cvt_pk_bf16_f32 v226, v228, v229
v_mfma_f32_16x16x32_bf16 v[8:11], v[64:67], v[132:135], v[8:11]
ds_read_b64_tr_b16 v[206:207], v219 offset:30720
v_add_u32 v216, 65536, v232
v_add_u32 v217, 65536, v233
v_add_u32 v218, 65536, v234
v_add_u32 v219, 65536, v235
s_mov_b32 s99, 292992
buffer_load_dwordx4 v251, s[52:55], s99 offen offset:2048 lds
v_mfma_f32_16x16x32_bf16 v[4:7], v[68:71], v[128:131], v[4:7]
ds_read_b64_tr_b16 v[64:65], v216 offset:0
v_cvt_pk_bf16_f32 v227, v230, v231
v_mfma_f32_16x16x32_bf16 v[12:15], v[68:71], v[132:135], v[12:15]
ds_read_b64_tr_b16 v[66:67], v216 offset:2048
buffer_store_dwordx4 v[224:227], v236, s[36:39], s98 offen offset:3072 sc1
v_mfma_f32_16x16x32_bf16 v[16:19], v[72:75], v[128:131], v[16:19]
ds_read_b64_tr_b16 v[68:69], v217 offset:0
s_mov_b32 s99, 423040
buffer_load_dwordx4 v251, s[52:55], s99 offen offset:3072 lds
v_mfma_f32_16x16x32_bf16 v[24:27], v[72:75], v[132:135], v[24:27]
ds_read_b64_tr_b16 v[70:71], v217 offset:2048
s_add_u32 s98, s76, 0
v_mfma_f32_16x16x32_bf16 v[20:23], v[76:79], v[128:131], v[20:23]
ds_read_b64_tr_b16 v[72:73], v218 offset:0
v_accvgpr_read_b32 v208, a0
v_mfma_f32_16x16x32_bf16 v[28:31], v[76:79], v[132:135], v[28:31]
ds_read_b64_tr_b16 v[74:75], v218 offset:2048
s_waitcnt lgkmcnt(6)
v_mfma_f32_16x16x32_bf16 v[0:3], v[192:195], v[136:139], v[0:3]
ds_read_b64_tr_b16 v[76:77], v219 offset:0
v_accvgpr_read_b32 v209, a1
v_mfma_f32_16x16x32_bf16 v[8:11], v[192:195], v[140:143], v[8:11]
ds_read_b64_tr_b16 v[78:79], v219 offset:2048
v_accvgpr_read_b32 v210, a2
v_mfma_f32_16x16x32_bf16 v[4:7], v[196:199], v[136:139], v[4:7]
ds_read_b64_tr_b16 v[192:193], v216 offset:4096
v_accvgpr_read_b32 v211, a3
v_mfma_f32_16x16x32_bf16 v[12:15], v[196:199], v[140:143], v[12:15]
ds_read_b64_tr_b16 v[194:195], v216 offset:6144
v_accvgpr_read_b32 v212, a4
v_accvgpr_read_b32 v213, a5
v_mfma_f32_16x16x32_bf16 v[16:19], v[200:203], v[136:139], v[16:19]
ds_read_b64_tr_b16 v[196:197], v217 offset:4096
v_accvgpr_read_b32 v214, a6
v_accvgpr_read_b32 v215, a7
v_mfma_f32_16x16x32_bf16 v[24:27], v[200:203], v[140:143], v[24:27]
ds_read_b64_tr_b16 v[198:199], v217 offset:6144
v_cvt_pk_bf16_f32 v208, v208, v209
v_cvt_pk_bf16_f32 v209, v210, v211
v_mfma_f32_16x16x32_bf16 v[20:23], v[204:207], v[136:139], v[20:23]
ds_read_b64_tr_b16 v[200:201], v218 offset:4096
v_cvt_pk_bf16_f32 v210, v212, v213
v_cvt_pk_bf16_f32 v211, v214, v215
v_mfma_f32_16x16x32_bf16 v[28:31], v[204:207], v[140:143], v[28:31]
ds_read_b64_tr_b16 v[202:203], v218 offset:6144
buffer_store_dwordx4 v[208:211], v236, s[36:39], s98 offen offset:0 sc1
s_waitcnt lgkmcnt(6)
v_mfma_f32_16x16x32_bf16 v[0:3], v[64:67], v[144:147], v[0:3]
ds_read_b64_tr_b16 v[204:205], v219 offset:4096
s_add_u32 m0, s68, 98304
s_mov_b32 s99, 2097280
buffer_load_dwordx4 v251, s[52:55], s99 offen offset:0 lds
v_mfma_f32_16x16x32_bf16 v[8:11], v[64:67], v[148:151], v[8:11]
ds_read_b64_tr_b16 v[206:207], v219 offset:6144
v_mfma_f32_16x16x32_bf16 v[4:7], v[68:71], v[144:147], v[4:7]
ds_read_b64_tr_b16 v[64:65], v216 offset:8192
v_mfma_f32_16x16x32_bf16 v[12:15], v[68:71], v[148:151], v[12:15]
ds_read_b64_tr_b16 v[66:67], v216 offset:10240
v_mfma_f32_16x16x32_bf16 v[16:19], v[72:75], v[144:147], v[16:19]
ds_read_b64_tr_b16 v[68:69], v217 offset:8192
v_accvgpr_read_b32 v224, a8
v_mfma_f32_16x16x32_bf16 v[24:27], v[72:75], v[148:151], v[24:27]
ds_read_b64_tr_b16 v[70:71], v217 offset:10240
s_mov_b32 s99, 2227328
buffer_load_dwordx4 v251, s[52:55], s99 offen offset:1024 lds
v_mfma_f32_16x16x32_bf16 v[20:23], v[76:79], v[144:147], v[20:23]
ds_read_b64_tr_b16 v[72:73], v218 offset:8192
v_accvgpr_read_b32 v225, a9
v_mfma_f32_16x16x32_bf16 v[28:31], v[76:79], v[148:151], v[28:31]
ds_read_b64_tr_b16 v[74:75], v218 offset:10240
v_accvgpr_read_b32 v226, a10
s_waitcnt lgkmcnt(6)
v_mfma_f32_16x16x32_bf16 v[0:3], v[192:195], v[152:155], v[0:3]
ds_read_b64_tr_b16 v[76:77], v219 offset:8192
v_accvgpr_read_b32 v227, a11
v_accvgpr_read_b32 v228, a12
v_mfma_f32_16x16x32_bf16 v[8:11], v[192:195], v[156:159], v[8:11]
ds_read_b64_tr_b16 v[78:79], v219 offset:10240
v_accvgpr_read_b32 v229, a13
v_mfma_f32_16x16x32_bf16 v[4:7], v[196:199], v[152:155], v[4:7]
ds_read_b64_tr_b16 v[192:193], v216 offset:12288
s_mov_b32 s99, 2128000
buffer_load_dwordx4 v251, s[52:55], s99 offen offset:2048 lds
v_mfma_f32_16x16x32_bf16 v[12:15], v[196:199], v[156:159], v[12:15]
ds_read_b64_tr_b16 v[194:195], v216 offset:14336
v_accvgpr_read_b32 v230, a14
v_mfma_f32_16x16x32_bf16 v[16:19], v[200:203], v[152:155], v[16:19]
ds_read_b64_tr_b16 v[196:197], v217 offset:12288
v_accvgpr_read_b32 v231, a15
v_mfma_f32_16x16x32_bf16 v[24:27], v[200:203], v[156:159], v[24:27]
ds_read_b64_tr_b16 v[198:199], v217 offset:14336
v_cvt_pk_bf16_f32 v224, v224, v225
v_mfma_f32_16x16x32_bf16 v[20:23], v[204:207], v[152:155], v[20:23]
ds_read_b64_tr_b16 v[200:201], v218 offset:12288
v_cvt_pk_bf16_f32 v225, v226, v227
v_mfma_f32_16x16x32_bf16 v[28:31], v[204:207], v[156:159], v[28:31]
ds_read_b64_tr_b16 v[202:203], v218 offset:14336
s_mov_b32 s99, 2258048
buffer_load_dwordx4 v251, s[52:55], s99 offen offset:3072 lds
s_waitcnt lgkmcnt(6)
v_mfma_f32_16x16x32_bf16 v[0:3], v[64:67], v[160:163], v[0:3]
ds_read_b64_tr_b16 v[204:205], v219 offset:12288
v_cvt_pk_bf16_f32 v226, v228, v229
v_mfma_f32_16x16x32_bf16 v[8:11], v[64:67], v[164:167], v[8:11]
ds_read_b64_tr_b16 v[206:207], v219 offset:14336
v_cvt_pk_bf16_f32 v227, v230, v231
v_mfma_f32_16x16x32_bf16 v[4:7], v[68:71], v[160:163], v[4:7]
ds_read_b64_tr_b16 v[64:65], v216 offset:16384
buffer_store_dwordx4 v[224:227], v236, s[36:39], s98 offen offset:1024 sc1
v_mfma_f32_16x16x32_bf16 v[12:15], v[68:71], v[164:167], v[12:15]
ds_read_b64_tr_b16 v[66:67], v216 offset:18432
s_add_u32 s98, s76, 65536
s_add_u32 s98, s98, s77
v_mfma_f32_16x16x32_bf16 v[16:19], v[72:75], v[160:163], v[16:19]
ds_read_b64_tr_b16 v[68:69], v217 offset:16384
s_add_u32 m0, m0, 4096
s_mov_b32 s99, 2359424
buffer_load_dwordx4 v251, s[52:55], s99 offen offset:0 lds
s_waitcnt vmcnt(18)
v_mfma_f32_16x16x32_bf16 v[24:27], v[72:75], v[164:167], v[24:27]
ds_read_b64_tr_b16 v[70:71], v217 offset:18432
v_accvgpr_read_b32 v208, a160
v_mfma_f32_16x16x32_bf16 v[20:23], v[76:79], v[160:163], v[20:23]
ds_read_b64_tr_b16 v[72:73], v218 offset:16384
ds_read_b128 a[128:131], v221 offset:0
v_mfma_f32_16x16x32_bf16 v[28:31], v[76:79], v[164:167], v[28:31]
ds_read_b64_tr_b16 v[74:75], v218 offset:18432
s_waitcnt lgkmcnt(7)
v_mfma_f32_16x16x32_bf16 v[0:3], v[192:195], v[168:171], v[0:3]
ds_read_b64_tr_b16 v[76:77], v219 offset:16384
ds_read_b128 a[132:135], v221 offset:1024
v_mfma_f32_16x16x32_bf16 v[8:11], v[192:195], v[172:175], v[8:11]
ds_read_b64_tr_b16 v[78:79], v219 offset:18432
s_mov_b32 s99, 2489472
buffer_load_dwordx4 v251, s[52:55], s99 offen offset:1024 lds
v_mfma_f32_16x16x32_bf16 v[4:7], v[196:199], v[168:171], v[4:7]
ds_read_b64_tr_b16 v[192:193], v216 offset:20480
ds_read_b128 a[136:139], v221 offset:2048
v_mfma_f32_16x16x32_bf16 v[12:15], v[196:199], v[172:175], v[12:15]
ds_read_b64_tr_b16 v[194:195], v216 offset:22528
v_accvgpr_read_b32 v209, a161
v_mfma_f32_16x16x32_bf16 v[16:19], v[200:203], v[168:171], v[16:19]
ds_read_b64_tr_b16 v[196:197], v217 offset:20480
ds_read_b128 a[140:143], v221 offset:3072
v_mfma_f32_16x16x32_bf16 v[24:27], v[200:203], v[172:175], v[24:27]
ds_read_b64_tr_b16 v[198:199], v217 offset:22528
v_accvgpr_read_b32 v210, a162
v_accvgpr_read_b32 v211, a163
v_mfma_f32_16x16x32_bf16 v[20:23], v[204:207], v[168:171], v[20:23]
ds_read_b64_tr_b16 v[200:201], v218 offset:20480
s_mov_b32 s99, 2390144
buffer_load_dwordx4 v251, s[52:55], s99 offen offset:2048 lds
ds_read_b128 a[144:147], v221 offset:4096
v_mfma_f32_16x16x32_bf16 v[28:31], v[204:207], v[172:175], v[28:31]
ds_read_b64_tr_b16 v[202:203], v218 offset:22528
v_accvgpr_read_b32 v212, a164
s_waitcnt lgkmcnt(9)
v_mfma_f32_16x16x32_bf16 v[0:3], v[64:67], v[176:179], v[0:3]
ds_read_b64_tr_b16 v[204:205], v219 offset:20480
ds_read_b128 a[148:151], v221 offset:5120
v_accvgpr_read_b32 v213, a165
v_mfma_f32_16x16x32_bf16 v[8:11], v[64:67], v[180:183], v[8:11]
ds_read_b64_tr_b16 v[206:207], v219 offset:22528
v_add_u32 v216, 32768, v232
v_add_u32 v217, 32768, v233
v_add_u32 v218, 32768, v234
v_add_u32 v219, 32768, v235
v_mfma_f32_16x16x32_bf16 v[4:7], v[68:71], v[176:179], v[4:7]
s_waitcnt vmcnt(9)
s_barrier
ds_read_b64_tr_b16 v[64:65], v216 offset:0
ds_read_b128 a[152:155], v221 offset:6144
v_mfma_f32_16x16x32_bf16 v[12:15], v[68:71], v[180:183], v[12:15]
ds_read_b64_tr_b16 v[66:67], v216 offset:2048
s_mov_b32 s99, 2520192
buffer_load_dwordx4 v251, s[52:55], s99 offen offset:3072 lds
v_mfma_f32_16x16x32_bf16 v[16:19], v[72:75], v[176:179], v[16:19]
ds_read_b64_tr_b16 v[68:69], v217 offset:0
ds_read_b128 a[156:159], v221 offset:7168
v_accvgpr_read_b32 v214, a166
v_mfma_f32_16x16x32_bf16 v[24:27], v[72:75], v[180:183], v[24:27]
ds_read_b64_tr_b16 v[70:71], v217 offset:2048
v_accvgpr_read_b32 v215, a167
v_cvt_pk_bf16_f32 v208, v208, v209
)ASM"
R"ASM(
v_mfma_f32_16x16x32_bf16 v[20:23], v[76:79], v[176:179], v[20:23]
ds_read_b64_tr_b16 v[72:73], v218 offset:0
v_cvt_pk_bf16_f32 v209, v210, v211
v_cvt_pk_bf16_f32 v210, v212, v213
v_mfma_f32_16x16x32_bf16 v[28:31], v[76:79], v[180:183], v[28:31]
ds_read_b64_tr_b16 v[74:75], v218 offset:2048
v_cvt_pk_bf16_f32 v211, v214, v215
s_waitcnt lgkmcnt(8)
v_mfma_f32_16x16x32_bf16 v[0:3], v[192:195], v[184:187], v[0:3]
ds_read_b64_tr_b16 v[76:77], v219 offset:0
buffer_store_dwordx4 v[208:211], v236, s[36:39], s98 offen offset:0 sc1
v_mfma_f32_16x16x32_bf16 v[8:11], v[192:195], v[188:191], v[8:11]
ds_read_b64_tr_b16 v[78:79], v219 offset:2048
v_accvgpr_read_b32 v224, a168
v_accvgpr_read_b32 v225, a169
v_mfma_f32_16x16x32_bf16 v[4:7], v[196:199], v[184:187], v[4:7]
ds_read_b64_tr_b16 v[192:193], v216 offset:4096
v_accvgpr_read_b32 v226, a170
v_accvgpr_read_b32 v227, a171
v_mfma_f32_16x16x32_bf16 v[12:15], v[196:199], v[188:191], v[12:15]
ds_read_b64_tr_b16 v[194:195], v216 offset:6144
v_accvgpr_read_b32 v228, a172
v_accvgpr_read_b32 v229, a173
v_mfma_f32_16x16x32_bf16 v[16:19], v[200:203], v[184:187], v[16:19]
ds_read_b64_tr_b16 v[196:197], v217 offset:4096
v_accvgpr_read_b32 v230, a174
v_accvgpr_read_b32 v231, a175
v_mfma_f32_16x16x32_bf16 v[24:27], v[200:203], v[188:191], v[24:27]
ds_read_b64_tr_b16 v[198:199], v217 offset:6144
v_cvt_pk_bf16_f32 v224, v224, v225
v_cvt_pk_bf16_f32 v225, v226, v227
v_mfma_f32_16x16x32_bf16 v[20:23], v[204:207], v[184:187], v[20:23]
ds_read_b64_tr_b16 v[200:201], v218 offset:4096
v_cvt_pk_bf16_f32 v226, v228, v229
v_cvt_pk_bf16_f32 v227, v230, v231
v_mfma_f32_16x16x32_bf16 v[28:31], v[204:207], v[188:191], v[28:31]
ds_read_b64_tr_b16 v[202:203], v218 offset:6144
buffer_store_dwordx4 v[224:227], v236, s[36:39], s98 offen offset:1024 sc1
s_waitcnt lgkmcnt(6)
v_mfma_f32_16x16x32_bf16 v[32:35], v[64:67], v[80:83], 0
ds_read_b64_tr_b16 v[204:205], v219 offset:4096
v_mfma_f32_16x16x32_bf16 v[40:43], v[64:67], v[84:87], 0
ds_read_b64_tr_b16 v[206:207], v219 offset:6144
v_mfma_f32_16x16x32_bf16 v[36:39], v[68:71], v[80:83], 0
ds_read_b64_tr_b16 v[64:65], v216 offset:8192
v_mfma_f32_16x16x32_bf16 v[44:47], v[68:71], v[84:87], 0
ds_read_b64_tr_b16 v[66:67], v216 offset:10240
v_mfma_f32_16x16x32_bf16 v[48:51], v[72:75], v[80:83], 0
ds_read_b64_tr_b16 v[68:69], v217 offset:8192
s_add_u32 s96, s70, 0
v_mul_f32 v0, 0x3db8aa3b, v0
v_mfma_f32_16x16x32_bf16 v[56:59], v[72:75], v[84:87], 0
ds_read_b64_tr_b16 v[70:71], v217 offset:10240
v_mul_f32 v1, 0x3db8aa3b, v1
v_mul_f32 v2, 0x3db8aa3b, v2
v_mfma_f32_16x16x32_bf16 v[52:55], v[76:79], v[80:83], 0
ds_read_b64_tr_b16 v[72:73], v218 offset:8192
s_add_u32 m0, s68, 131072
s_add_u32 s28, s84, 8192
buffer_load_dwordx4 v236, s[36:39], s28 offen offset:0 sc1 lds
v_mfma_f32_16x16x32_bf16 v[60:63], v[76:79], v[84:87], 0
ds_read_b64_tr_b16 v[74:75], v218 offset:10240
v_mul_f32 v3, 0x3db8aa3b, v3
s_waitcnt lgkmcnt(6)
v_mfma_f32_16x16x32_bf16 v[32:35], v[192:195], v[88:91], v[32:35]
ds_read_b64_tr_b16 v[76:77], v219 offset:8192
v_mul_f32 v4, 0x3db8aa3b, v4
v_mul_f32 v5, 0x3db8aa3b, v5
v_mfma_f32_16x16x32_bf16 v[40:43], v[192:195], v[92:95], v[40:43]
ds_read_b64_tr_b16 v[78:79], v219 offset:10240
buffer_load_dwordx4 v236, s[36:39], s28 offen offset:1024 sc1 lds
v_mfma_f32_16x16x32_bf16 v[36:39], v[196:199], v[88:91], v[36:39]
ds_read_b64_tr_b16 v[192:193], v216 offset:12288
v_mul_f32 v6, 0x3db8aa3b, v6
v_mul_f32 v7, 0x3db8aa3b, v7
v_mfma_f32_16x16x32_bf16 v[44:47], v[196:199], v[92:95], v[44:47]
ds_read_b64_tr_b16 v[194:195], v216 offset:14336
v_cvt_pk_bf16_f32 v0, v0, v1
v_cvt_pk_bf16_f32 v1, v2, v3
v_mfma_f32_16x16x32_bf16 v[48:51], v[200:203], v[88:91], v[48:51]
ds_read_b64_tr_b16 v[196:197], v217 offset:12288
buffer_load_dwordx4 v236, s[36:39], s28 offen offset:2048 sc1 lds
v_mfma_f32_16x16x32_bf16 v[56:59], v[200:203], v[92:95], v[56:59]
ds_read_b64_tr_b16 v[198:199], v217 offset:14336
v_cvt_pk_bf16_f32 v2, v4, v5
v_cvt_pk_bf16_f32 v3, v6, v7
v_mfma_f32_16x16x32_bf16 v[52:55], v[204:207], v[88:91], v[52:55]
ds_read_b64_tr_b16 v[200:201], v218 offset:12288
v_accvgpr_write_b32 a0, v0
v_accvgpr_write_b32 a1, v1
v_mfma_f32_16x16x32_bf16 v[60:63], v[204:207], v[92:95], v[60:63]
ds_read_b64_tr_b16 v[202:203], v218 offset:14336
buffer_load_dwordx4 v236, s[36:39], s28 offen offset:3072 sc1 lds
s_waitcnt lgkmcnt(6)
v_mfma_f32_16x16x32_bf16 v[32:35], v[64:67], v[96:99], v[32:35]
ds_read_b64_tr_b16 v[204:205], v219 offset:12288
v_accvgpr_write_b32 a2, v2
v_accvgpr_write_b32 a3, v3
v_mfma_f32_16x16x32_bf16 v[40:43], v[64:67], v[100:103], v[40:43]
ds_read_b64_tr_b16 v[206:207], v219 offset:14336
buffer_store_dwordx4 v[0:3], v236, s[60:63], s96 offen offset:0 sc1
v_mfma_f32_16x16x32_bf16 v[36:39], v[68:71], v[96:99], v[36:39]
ds_read_b64_tr_b16 v[64:65], v216 offset:16384
s_add_u32 m0, s68, 135168
s_add_u32 s28, s84, 12288
buffer_load_dwordx4 v236, s[36:39], s28 offen offset:0 sc1 lds
v_mfma_f32_16x16x32_bf16 v[44:47], v[68:71], v[100:103], v[44:47]
ds_read_b64_tr_b16 v[66:67], v216 offset:18432
v_mul_f32 v8, 0x3db8aa3b, v8
v_mul_f32 v9, 0x3db8aa3b, v9
v_mfma_f32_16x16x32_bf16 v[48:51], v[72:75], v[96:99], v[48:51]
ds_read_b64_tr_b16 v[68:69], v217 offset:16384
v_mul_f32 v10, 0x3db8aa3b, v10
v_mul_f32 v11, 0x3db8aa3b, v11
v_mul_f32 v12, 0x3db8aa3b, v12
v_mfma_f32_16x16x32_bf16 v[56:59], v[72:75], v[100:103], v[56:59]
ds_read_b64_tr_b16 v[70:71], v217 offset:18432
buffer_load_dwordx4 v236, s[36:39], s28 offen offset:1024 sc1 lds
v_mul_f32 v13, 0x3db8aa3b, v13
v_mfma_f32_16x16x32_bf16 v[52:55], v[76:79], v[96:99], v[52:55]
ds_read_b64_tr_b16 v[72:73], v218 offset:16384
v_mul_f32 v14, 0x3db8aa3b, v14
v_mul_f32 v15, 0x3db8aa3b, v15
v_cvt_pk_bf16_f32 v8, v8, v9
v_mfma_f32_16x16x32_bf16 v[60:63], v[76:79], v[100:103], v[60:63]
ds_read_b64_tr_b16 v[74:75], v218 offset:18432
v_cvt_pk_bf16_f32 v9, v10, v11
v_cvt_pk_bf16_f32 v10, v12, v13
s_waitcnt lgkmcnt(6)
v_mfma_f32_16x16x32_bf16 v[32:35], v[192:195], v[104:107], v[32:35]
ds_read_b64_tr_b16 v[76:77], v219 offset:16384
buffer_load_dwordx4 v236, s[36:39], s28 offen offset:2048 sc1 lds
v_cvt_pk_bf16_f32 v11, v14, v15
v_mfma_f32_16x16x32_bf16 v[40:43], v[192:195], v[108:111], v[40:43]
ds_read_b64_tr_b16 v[78:79], v219 offset:18432
v_accvgpr_write_b32 a4, v8
v_accvgpr_write_b32 a5, v9
v_accvgpr_write_b32 a6, v10
v_mfma_f32_16x16x32_bf16 v[36:39], v[196:199], v[104:107], v[36:39]
ds_read_b64_tr_b16 v[192:193], v216 offset:20480
v_accvgpr_write_b32 a7, v11
buffer_store_dwordx4 v[8:11], v236, s[60:63], s96 offen offset:1024 sc1
v_mfma_f32_16x16x32_bf16 v[44:47], v[196:199], v[108:111], v[44:47]
ds_read_b64_tr_b16 v[194:195], v216 offset:22528
buffer_load_dwordx4 v236, s[36:39], s28 offen offset:3072 sc1 lds
v_mul_f32 v16, 0x3db8aa3b, v16
v_mfma_f32_16x16x32_bf16 v[48:51], v[200:203], v[104:107], v[48:51]
ds_read_b64_tr_b16 v[196:197], v217 offset:20480
v_mul_f32 v17, 0x3db8aa3b, v17
v_mul_f32 v18, 0x3db8aa3b, v18
v_mul_f32 v19, 0x3db8aa3b, v19
v_mfma_f32_16x16x32_bf16 v[56:59], v[200:203], v[108:111], v[56:59]
ds_read_b64_tr_b16 v[198:199], v217 offset:22528
v_mul_f32 v20, 0x3db8aa3b, v20
v_mul_f32 v21, 0x3db8aa3b, v21
v_mul_f32 v22, 0x3db8aa3b, v22
v_mfma_f32_16x16x32_bf16 v[52:55], v[204:207], v[104:107], v[52:55]
ds_read_b64_tr_b16 v[200:201], v218 offset:20480
v_mul_f32 v23, 0x3db8aa3b, v23
v_cvt_pk_bf16_f32 v16, v16, v17
v_cvt_pk_bf16_f32 v17, v18, v19
v_mfma_f32_16x16x32_bf16 v[60:63], v[204:207], v[108:111], v[60:63]
ds_read_b64_tr_b16 v[202:203], v218 offset:22528
s_add_u32 m0, s68, 0
s_mov_b32 s99, 256
buffer_load_dwordx4 v251, s[52:55], s99 offen offset:0 lds
s_waitcnt lgkmcnt(6)
v_mfma_f32_16x16x32_bf16 v[32:35], v[64:67], v[112:115], v[32:35]
ds_read_b64_tr_b16 v[204:205], v219 offset:20480
v_cvt_pk_bf16_f32 v18, v20, v21
v_cvt_pk_bf16_f32 v19, v22, v23
v_accvgpr_write_b32 a8, v16
v_mfma_f32_16x16x32_bf16 v[40:43], v[64:67], v[116:119], v[40:43]
ds_read_b64_tr_b16 v[206:207], v219 offset:22528
v_accvgpr_write_b32 a9, v17
v_accvgpr_write_b32 a10, v18
v_accvgpr_write_b32 a11, v19
v_mfma_f32_16x16x32_bf16 v[36:39], v[68:71], v[112:115], v[36:39]
ds_read_b64_tr_b16 v[64:65], v216 offset:24576
s_mov_b32 s99, 130304
buffer_load_dwordx4 v251, s[52:55], s99 offen offset:1024 lds
v_mfma_f32_16x16x32_bf16 v[44:47], v[68:71], v[116:119], v[44:47]
ds_read_b64_tr_b16 v[66:67], v216 offset:26624
buffer_store_dwordx4 v[16:19], v236, s[60:63], s96 offen offset:2048 sc1
v_mul_f32 v24, 0x3db8aa3b, v24
v_mfma_f32_16x16x32_bf16 v[48:51], v[72:75], v[112:115], v[48:51]
ds_read_b64_tr_b16 v[68:69], v217 offset:24576
v_mul_f32 v25, 0x3db8aa3b, v25
v_mul_f32 v26, 0x3db8aa3b, v26
v_mul_f32 v27, 0x3db8aa3b, v27
v_mfma_f32_16x16x32_bf16 v[56:59], v[72:75], v[116:119], v[56:59]
ds_read_b64_tr_b16 v[70:71], v217 offset:26624
s_mov_b32 s99, 30976
buffer_load_dwordx4 v251, s[52:55], s99 offen offset:2048 lds
v_mfma_f32_16x16x32_bf16 v[52:55], v[76:79], v[112:115], v[52:55]
ds_read_b64_tr_b16 v[72:73], v218 offset:24576
v_mul_f32 v28, 0x3db8aa3b, v28
v_mul_f32 v29, 0x3db8aa3b, v29
v_mul_f32 v30, 0x3db8aa3b, v30
v_mfma_f32_16x16x32_bf16 v[60:63], v[76:79], v[116:119], v[60:63]
)ASM"
R"ASM(
ds_read_b64_tr_b16 v[74:75], v218 offset:26624
v_mul_f32 v31, 0x3db8aa3b, v31
v_cvt_pk_bf16_f32 v24, v24, v25
s_waitcnt lgkmcnt(6)
v_mfma_f32_16x16x32_bf16 v[32:35], v[192:195], v[120:123], v[32:35]
ds_read_b64_tr_b16 v[76:77], v219 offset:24576
s_mov_b32 s99, 161024
buffer_load_dwordx4 v251, s[52:55], s99 offen offset:3072 lds
v_mfma_f32_16x16x32_bf16 v[40:43], v[192:195], v[124:127], v[40:43]
ds_read_b64_tr_b16 v[78:79], v219 offset:26624
v_cvt_pk_bf16_f32 v25, v26, v27
v_cvt_pk_bf16_f32 v26, v28, v29
v_cvt_pk_bf16_f32 v27, v30, v31
v_mfma_f32_16x16x32_bf16 v[36:39], v[196:199], v[120:123], v[36:39]
ds_read_b64_tr_b16 v[192:193], v216 offset:28672
v_accvgpr_write_b32 a12, v24
v_accvgpr_write_b32 a13, v25
v_accvgpr_write_b32 a14, v26
v_mfma_f32_16x16x32_bf16 v[44:47], v[196:199], v[124:127], v[44:47]
ds_read_b64_tr_b16 v[194:195], v216 offset:30720
s_add_u32 m0, m0, 4096
s_mov_b32 s99, 262400
buffer_load_dwordx4 v251, s[52:55], s99 offen offset:0 lds
v_mfma_f32_16x16x32_bf16 v[48:51], v[200:203], v[120:123], v[48:51]
ds_read_b64_tr_b16 v[196:197], v217 offset:28672
v_accvgpr_write_b32 a15, v27
buffer_store_dwordx4 v[24:27], v236, s[60:63], s96 offen offset:3072 sc1
v_mfma_f32_16x16x32_bf16 v[56:59], v[200:203], v[124:127], v[56:59]
ds_read_b64_tr_b16 v[198:199], v217 offset:30720
v_accvgpr_read_b32 v208, a176
v_accvgpr_read_b32 v209, a177
v_mfma_f32_16x16x32_bf16 v[52:55], v[204:207], v[120:123], v[52:55]
ds_read_b64_tr_b16 v[200:201], v218 offset:28672
s_mov_b32 s99, 392448
buffer_load_dwordx4 v251, s[52:55], s99 offen offset:1024 lds
v_mfma_f32_16x16x32_bf16 v[60:63], v[204:207], v[124:127], v[60:63]
ds_read_b64_tr_b16 v[202:203], v218 offset:30720
v_accvgpr_read_b32 v210, a178
v_accvgpr_read_b32 v211, a179
s_waitcnt lgkmcnt(6)
v_mfma_f32_16x16x32_bf16 v[32:35], v[64:67], v[128:131], v[32:35]
ds_read_b64_tr_b16 v[204:205], v219 offset:28672
v_accvgpr_read_b32 v212, a180
v_accvgpr_read_b32 v213, a181
v_accvgpr_read_b32 v214, a182
v_mfma_f32_16x16x32_bf16 v[40:43], v[64:67], v[132:135], v[40:43]
ds_read_b64_tr_b16 v[206:207], v219 offset:30720
v_add_u32 v216, 98304, v232
v_add_u32 v217, 98304, v233
v_add_u32 v218, 98304, v234
v_add_u32 v219, 98304, v235
s_mov_b32 s99, 293120
buffer_load_dwordx4 v251, s[52:55], s99 offen offset:2048 lds
v_mfma_f32_16x16x32_bf16 v[36:39], v[68:71], v[128:131], v[36:39]
s_waitcnt vmcnt(21)
s_barrier
ds_read_b64_tr_b16 v[64:65], v216 offset:0
v_accvgpr_read_b32 v215, a183
v_mfma_f32_16x16x32_bf16 v[44:47], v[68:71], v[132:135], v[44:47]
ds_read_b64_tr_b16 v[66:67], v216 offset:2048
v_cvt_pk_bf16_f32 v208, v208, v209
v_cvt_pk_bf16_f32 v209, v210, v211
v_cvt_pk_bf16_f32 v210, v212, v213
v_mfma_f32_16x16x32_bf16 v[48:51], v[72:75], v[128:131], v[48:51]
ds_read_b64_tr_b16 v[68:69], v217 offset:0
s_mov_b32 s99, 423168
buffer_load_dwordx4 v251, s[52:55], s99 offen offset:3072 lds
v_mfma_f32_16x16x32_bf16 v[56:59], v[72:75], v[132:135], v[56:59]
ds_read_b64_tr_b16 v[70:71], v217 offset:2048
v_cvt_pk_bf16_f32 v211, v214, v215
buffer_store_dwordx4 v[208:211], v236, s[36:39], s98 offen offset:2048 sc1
v_mfma_f32_16x16x32_bf16 v[52:55], v[76:79], v[128:131], v[52:55]
ds_read_b64_tr_b16 v[72:73], v218 offset:0
v_accvgpr_read_b32 v224, a184
v_accvgpr_read_b32 v225, a185
v_accvgpr_read_b32 v226, a186
v_mfma_f32_16x16x32_bf16 v[60:63], v[76:79], v[132:135], v[60:63]
ds_read_b64_tr_b16 v[74:75], v218 offset:2048
v_accvgpr_read_b32 v227, a187
v_accvgpr_read_b32 v228, a188
s_waitcnt lgkmcnt(6)
v_mfma_f32_16x16x32_bf16 v[32:35], v[192:195], v[136:139], v[32:35]
ds_read_b64_tr_b16 v[76:77], v219 offset:0
v_accvgpr_read_b32 v229, a189
v_accvgpr_read_b32 v230, a190
v_accvgpr_read_b32 v231, a191
v_mfma_f32_16x16x32_bf16 v[40:43], v[192:195], v[140:143], v[40:43]
ds_read_b64_tr_b16 v[78:79], v219 offset:2048
v_cvt_pk_bf16_f32 v224, v224, v225
v_cvt_pk_bf16_f32 v225, v226, v227
v_cvt_pk_bf16_f32 v226, v228, v229
v_mfma_f32_16x16x32_bf16 v[36:39], v[196:199], v[136:139], v[36:39]
ds_read_b64_tr_b16 v[192:193], v216 offset:4096
v_cvt_pk_bf16_f32 v227, v230, v231
buffer_store_dwordx4 v[224:227], v236, s[36:39], s98 offen offset:3072 sc1
v_mfma_f32_16x16x32_bf16 v[44:47], v[196:199], v[140:143], v[44:47]
ds_read_b64_tr_b16 v[194:195], v216 offset:6144
s_add_u32 s98, s76, 81920
v_accvgpr_read_b32 v208, a192
v_accvgpr_read_b32 v209, a193
v_mfma_f32_16x16x32_bf16 v[48:51], v[200:203], v[136:139], v[48:51]
ds_read_b64_tr_b16 v[196:197], v217 offset:4096
v_accvgpr_read_b32 v210, a194
v_accvgpr_read_b32 v211, a195
v_accvgpr_read_b32 v212, a196
v_mfma_f32_16x16x32_bf16 v[56:59], v[200:203], v[140:143], v[56:59]
ds_read_b64_tr_b16 v[198:199], v217 offset:6144
v_accvgpr_read_b32 v213, a197
v_accvgpr_read_b32 v214, a198
v_accvgpr_read_b32 v215, a199
v_mfma_f32_16x16x32_bf16 v[52:55], v[204:207], v[136:139], v[52:55]
ds_read_b64_tr_b16 v[200:201], v218 offset:4096
v_cvt_pk_bf16_f32 v208, v208, v209
v_cvt_pk_bf16_f32 v209, v210, v211
v_cvt_pk_bf16_f32 v210, v212, v213
v_mfma_f32_16x16x32_bf16 v[60:63], v[204:207], v[140:143], v[60:63]
ds_read_b64_tr_b16 v[202:203], v218 offset:6144
v_cvt_pk_bf16_f32 v211, v214, v215
buffer_store_dwordx4 v[208:211], v236, s[36:39], s98 offen offset:0 sc1
s_waitcnt lgkmcnt(6)
v_mfma_f32_16x16x32_bf16 v[32:35], v[64:67], v[144:147], v[32:35]
ds_read_b64_tr_b16 v[204:205], v219 offset:4096
s_add_u32 m0, s68, 65536
s_mov_b32 s99, 2097408
buffer_load_dwordx4 v251, s[52:55], s99 offen offset:0 lds
v_mfma_f32_16x16x32_bf16 v[40:43], v[64:67], v[148:151], v[40:43]
ds_read_b64_tr_b16 v[206:207], v219 offset:6144
v_mfma_f32_16x16x32_bf16 v[36:39], v[68:71], v[144:147], v[36:39]
ds_read_b64_tr_b16 v[64:65], v216 offset:8192
v_mfma_f32_16x16x32_bf16 v[44:47], v[68:71], v[148:151], v[44:47]
ds_read_b64_tr_b16 v[66:67], v216 offset:10240
v_mfma_f32_16x16x32_bf16 v[48:51], v[72:75], v[144:147], v[48:51]
ds_read_b64_tr_b16 v[68:69], v217 offset:8192
v_accvgpr_read_b32 v224, a200
v_mfma_f32_16x16x32_bf16 v[56:59], v[72:75], v[148:151], v[56:59]
ds_read_b64_tr_b16 v[70:71], v217 offset:10240
s_mov_b32 s99, 2227456
buffer_load_dwordx4 v251, s[52:55], s99 offen offset:1024 lds
v_mfma_f32_16x16x32_bf16 v[52:55], v[76:79], v[144:147], v[52:55]
ds_read_b64_tr_b16 v[72:73], v218 offset:8192
v_accvgpr_read_b32 v225, a201
v_mfma_f32_16x16x32_bf16 v[60:63], v[76:79], v[148:151], v[60:63]
ds_read_b64_tr_b16 v[74:75], v218 offset:10240
s_waitcnt lgkmcnt(6)
v_mfma_f32_16x16x32_bf16 v[32:35], v[192:195], v[152:155], v[32:35]
ds_read_b64_tr_b16 v[76:77], v219 offset:8192
v_accvgpr_read_b32 v226, a202
v_mfma_f32_16x16x32_bf16 v[40:43], v[192:195], v[156:159], v[40:43]
ds_read_b64_tr_b16 v[78:79], v219 offset:10240
v_accvgpr_read_b32 v227, a203
v_mfma_f32_16x16x32_bf16 v[36:39], v[196:199], v[152:155], v[36:39]
ds_read_b64_tr_b16 v[192:193], v216 offset:12288
s_mov_b32 s99, 2128128
buffer_load_dwordx4 v251, s[52:55], s99 offen offset:2048 lds
v_mfma_f32_16x16x32_bf16 v[44:47], v[196:199], v[156:159], v[44:47]
ds_read_b64_tr_b16 v[194:195], v216 offset:14336
v_accvgpr_read_b32 v228, a204
v_mfma_f32_16x16x32_bf16 v[48:51], v[200:203], v[152:155], v[48:51]
ds_read_b64_tr_b16 v[196:197], v217 offset:12288
v_accvgpr_read_b32 v229, a205
v_mfma_f32_16x16x32_bf16 v[56:59], v[200:203], v[156:159], v[56:59]
ds_read_b64_tr_b16 v[198:199], v217 offset:14336
v_accvgpr_read_b32 v230, a206
v_mfma_f32_16x16x32_bf16 v[52:55], v[204:207], v[152:155], v[52:55]
ds_read_b64_tr_b16 v[200:201], v218 offset:12288
v_accvgpr_read_b32 v231, a207
v_mfma_f32_16x16x32_bf16 v[60:63], v[204:207], v[156:159], v[60:63]
ds_read_b64_tr_b16 v[202:203], v218 offset:14336
s_mov_b32 s99, 2258176
buffer_load_dwordx4 v251, s[52:55], s99 offen offset:3072 lds
s_waitcnt lgkmcnt(6)
v_mfma_f32_16x16x32_bf16 v[32:35], v[64:67], v[160:163], v[32:35]
ds_read_b64_tr_b16 v[204:205], v219 offset:12288
v_cvt_pk_bf16_f32 v224, v224, v225
v_mfma_f32_16x16x32_bf16 v[40:43], v[64:67], v[164:167], v[40:43]
ds_read_b64_tr_b16 v[206:207], v219 offset:14336
v_cvt_pk_bf16_f32 v225, v226, v227
v_mfma_f32_16x16x32_bf16 v[36:39], v[68:71], v[160:163], v[36:39]
ds_read_b64_tr_b16 v[64:65], v216 offset:16384
v_cvt_pk_bf16_f32 v226, v228, v229
v_mfma_f32_16x16x32_bf16 v[44:47], v[68:71], v[164:167], v[44:47]
ds_read_b64_tr_b16 v[66:67], v216 offset:18432
v_cvt_pk_bf16_f32 v227, v230, v231
v_mfma_f32_16x16x32_bf16 v[48:51], v[72:75], v[160:163], v[48:51]
ds_read_b64_tr_b16 v[68:69], v217 offset:16384
s_add_u32 m0, m0, 4096
s_mov_b32 s99, 2359552
buffer_load_dwordx4 v251, s[52:55], s99 offen offset:0 lds
s_waitcnt vmcnt(18)
v_mfma_f32_16x16x32_bf16 v[56:59], v[72:75], v[164:167], v[56:59]
ds_read_b64_tr_b16 v[70:71], v217 offset:18432
buffer_store_dwordx4 v[224:227], v236, s[36:39], s98 offen offset:1024 sc1
v_mfma_f32_16x16x32_bf16 v[52:55], v[76:79], v[160:163], v[52:55]
ds_read_b64_tr_b16 v[72:73], v218 offset:16384
ds_read_b128 a[160:163], v221 offset:0
v_mfma_f32_16x16x32_bf16 v[60:63], v[76:79], v[164:167], v[60:63]
ds_read_b64_tr_b16 v[74:75], v218 offset:18432
s_waitcnt lgkmcnt(7)
v_mfma_f32_16x16x32_bf16 v[32:35], v[192:195], v[168:171], v[32:35]
ds_read_b64_tr_b16 v[76:77], v219 offset:16384
)ASM"
R"ASM(
ds_read_b128 a[164:167], v221 offset:1024
v_mfma_f32_16x16x32_bf16 v[40:43], v[192:195], v[172:175], v[40:43]
ds_read_b64_tr_b16 v[78:79], v219 offset:18432
s_mov_b32 s99, 2489600
buffer_load_dwordx4 v251, s[52:55], s99 offen offset:1024 lds
v_mfma_f32_16x16x32_bf16 v[36:39], v[196:199], v[168:171], v[36:39]
ds_read_b64_tr_b16 v[192:193], v216 offset:20480
ds_read_b128 a[168:171], v221 offset:2048
v_mfma_f32_16x16x32_bf16 v[44:47], v[196:199], v[172:175], v[44:47]
ds_read_b64_tr_b16 v[194:195], v216 offset:22528
s_add_u32 s98, s76, 0
v_accvgpr_read_b32 v208, a16
v_mfma_f32_16x16x32_bf16 v[48:51], v[200:203], v[168:171], v[48:51]
ds_read_b64_tr_b16 v[196:197], v217 offset:20480
ds_read_b128 a[172:175], v221 offset:3072
v_accvgpr_read_b32 v209, a17
v_mfma_f32_16x16x32_bf16 v[56:59], v[200:203], v[172:175], v[56:59]
ds_read_b64_tr_b16 v[198:199], v217 offset:22528
v_accvgpr_read_b32 v210, a18
v_accvgpr_read_b32 v211, a19
v_mfma_f32_16x16x32_bf16 v[52:55], v[204:207], v[168:171], v[52:55]
ds_read_b64_tr_b16 v[200:201], v218 offset:20480
s_mov_b32 s99, 2390272
buffer_load_dwordx4 v251, s[52:55], s99 offen offset:2048 lds
ds_read_b128 a[176:179], v221 offset:4096
v_mfma_f32_16x16x32_bf16 v[60:63], v[204:207], v[172:175], v[60:63]
ds_read_b64_tr_b16 v[202:203], v218 offset:22528
v_accvgpr_read_b32 v212, a20
s_waitcnt lgkmcnt(9)
v_mfma_f32_16x16x32_bf16 v[32:35], v[64:67], v[176:179], v[32:35]
ds_read_b64_tr_b16 v[204:205], v219 offset:20480
ds_read_b128 a[180:183], v221 offset:5120
v_accvgpr_read_b32 v213, a21
v_mfma_f32_16x16x32_bf16 v[40:43], v[64:67], v[180:183], v[40:43]
ds_read_b64_tr_b16 v[206:207], v219 offset:22528
v_add_u32 v216, 0, v232
v_add_u32 v217, 0, v233
v_add_u32 v218, 0, v234
v_add_u32 v219, 0, v235
v_mfma_f32_16x16x32_bf16 v[36:39], v[68:71], v[176:179], v[36:39]
s_waitcnt vmcnt(11)
s_barrier
ds_read_b64_tr_b16 v[64:65], v216 offset:0
ds_read_b128 a[184:187], v221 offset:6144
v_mfma_f32_16x16x32_bf16 v[44:47], v[68:71], v[180:183], v[44:47]
ds_read_b64_tr_b16 v[66:67], v216 offset:2048
s_mov_b32 s99, 2520320
buffer_load_dwordx4 v251, s[52:55], s99 offen offset:3072 lds
v_mfma_f32_16x16x32_bf16 v[48:51], v[72:75], v[176:179], v[48:51]
ds_read_b64_tr_b16 v[68:69], v217 offset:0
ds_read_b128 a[188:191], v221 offset:7168
v_accvgpr_read_b32 v214, a22
v_mfma_f32_16x16x32_bf16 v[56:59], v[72:75], v[180:183], v[56:59]
ds_read_b64_tr_b16 v[70:71], v217 offset:2048
v_accvgpr_read_b32 v215, a23
v_cvt_pk_bf16_f32 v208, v208, v209
v_mfma_f32_16x16x32_bf16 v[52:55], v[76:79], v[176:179], v[52:55]
ds_read_b64_tr_b16 v[72:73], v218 offset:0
v_cvt_pk_bf16_f32 v209, v210, v211
v_cvt_pk_bf16_f32 v210, v212, v213
v_mfma_f32_16x16x32_bf16 v[60:63], v[76:79], v[180:183], v[60:63]
ds_read_b64_tr_b16 v[74:75], v218 offset:2048
v_cvt_pk_bf16_f32 v211, v214, v215
s_waitcnt lgkmcnt(8)
v_mfma_f32_16x16x32_bf16 v[32:35], v[192:195], v[184:187], v[32:35]
ds_read_b64_tr_b16 v[76:77], v219 offset:0
buffer_store_dwordx4 v[208:211], v236, s[36:39], s98 offen offset:2048 sc1
v_mfma_f32_16x16x32_bf16 v[40:43], v[192:195], v[188:191], v[40:43]
ds_read_b64_tr_b16 v[78:79], v219 offset:2048
v_accvgpr_read_b32 v224, a24
v_accvgpr_read_b32 v225, a25
v_mfma_f32_16x16x32_bf16 v[36:39], v[196:199], v[184:187], v[36:39]
ds_read_b64_tr_b16 v[192:193], v216 offset:4096
v_accvgpr_read_b32 v226, a26
v_accvgpr_read_b32 v227, a27
v_mfma_f32_16x16x32_bf16 v[44:47], v[196:199], v[188:191], v[44:47]
ds_read_b64_tr_b16 v[194:195], v216 offset:6144
v_accvgpr_read_b32 v228, a28
v_accvgpr_read_b32 v229, a29
v_mfma_f32_16x16x32_bf16 v[48:51], v[200:203], v[184:187], v[48:51]
ds_read_b64_tr_b16 v[196:197], v217 offset:4096
v_accvgpr_read_b32 v230, a30
v_accvgpr_read_b32 v231, a31
v_mfma_f32_16x16x32_bf16 v[56:59], v[200:203], v[188:191], v[56:59]
ds_read_b64_tr_b16 v[198:199], v217 offset:6144
v_cvt_pk_bf16_f32 v224, v224, v225
v_cvt_pk_bf16_f32 v225, v226, v227
v_mfma_f32_16x16x32_bf16 v[52:55], v[204:207], v[184:187], v[52:55]
ds_read_b64_tr_b16 v[200:201], v218 offset:4096
v_cvt_pk_bf16_f32 v226, v228, v229
v_cvt_pk_bf16_f32 v227, v230, v231
v_mfma_f32_16x16x32_bf16 v[60:63], v[204:207], v[188:191], v[60:63]
ds_read_b64_tr_b16 v[202:203], v218 offset:6144
buffer_store_dwordx4 v[224:227], v236, s[36:39], s98 offen offset:3072 sc1
s_waitcnt lgkmcnt(6)
v_mfma_f32_16x16x32_bf16 v[0:3], v[64:67], v[80:83], 0
ds_read_b64_tr_b16 v[204:205], v219 offset:4096
v_mfma_f32_16x16x32_bf16 v[8:11], v[64:67], v[84:87], 0
ds_read_b64_tr_b16 v[206:207], v219 offset:6144
v_mfma_f32_16x16x32_bf16 v[4:7], v[68:71], v[80:83], 0
ds_read_b64_tr_b16 v[64:65], v216 offset:8192
v_mfma_f32_16x16x32_bf16 v[12:15], v[68:71], v[84:87], 0
ds_read_b64_tr_b16 v[66:67], v216 offset:10240
v_mfma_f32_16x16x32_bf16 v[16:19], v[72:75], v[80:83], 0
ds_read_b64_tr_b16 v[68:69], v217 offset:8192
s_add_u32 s96, s70, 4096
v_mul_f32 v32, 0x3db8aa3b, v32
v_mfma_f32_16x16x32_bf16 v[24:27], v[72:75], v[84:87], 0
ds_read_b64_tr_b16 v[70:71], v217 offset:10240
v_mul_f32 v33, 0x3db8aa3b, v33
v_mul_f32 v34, 0x3db8aa3b, v34
v_mfma_f32_16x16x32_bf16 v[20:23], v[76:79], v[80:83], 0
ds_read_b64_tr_b16 v[72:73], v218 offset:8192
s_add_u32 m0, s68, 131072
s_add_u32 s28, s84, 16384
buffer_load_dwordx4 v236, s[36:39], s28 offen offset:0 sc1 lds
v_mfma_f32_16x16x32_bf16 v[28:31], v[76:79], v[84:87], 0
ds_read_b64_tr_b16 v[74:75], v218 offset:10240
v_mul_f32 v35, 0x3db8aa3b, v35
s_waitcnt lgkmcnt(6)
v_mfma_f32_16x16x32_bf16 v[0:3], v[192:195], v[88:91], v[0:3]
ds_read_b64_tr_b16 v[76:77], v219 offset:8192
v_mul_f32 v36, 0x3db8aa3b, v36
v_mul_f32 v37, 0x3db8aa3b, v37
v_mfma_f32_16x16x32_bf16 v[8:11], v[192:195], v[92:95], v[8:11]
ds_read_b64_tr_b16 v[78:79], v219 offset:10240
buffer_load_dwordx4 v236, s[36:39], s28 offen offset:1024 sc1 lds
v_mfma_f32_16x16x32_bf16 v[4:7], v[196:199], v[88:91], v[4:7]
ds_read_b64_tr_b16 v[192:193], v216 offset:12288
v_mul_f32 v38, 0x3db8aa3b, v38
v_mul_f32 v39, 0x3db8aa3b, v39
v_mfma_f32_16x16x32_bf16 v[12:15], v[196:199], v[92:95], v[12:15]
ds_read_b64_tr_b16 v[194:195], v216 offset:14336
v_cvt_pk_bf16_f32 v32, v32, v33
v_cvt_pk_bf16_f32 v33, v34, v35
v_mfma_f32_16x16x32_bf16 v[16:19], v[200:203], v[88:91], v[16:19]
ds_read_b64_tr_b16 v[196:197], v217 offset:12288
buffer_load_dwordx4 v236, s[36:39], s28 offen offset:2048 sc1 lds
v_mfma_f32_16x16x32_bf16 v[24:27], v[200:203], v[92:95], v[24:27]
ds_read_b64_tr_b16 v[198:199], v217 offset:14336
v_cvt_pk_bf16_f32 v34, v36, v37
v_cvt_pk_bf16_f32 v35, v38, v39
v_mfma_f32_16x16x32_bf16 v[20:23], v[204:207], v[88:91], v[20:23]
ds_read_b64_tr_b16 v[200:201], v218 offset:12288
v_accvgpr_write_b32 a16, v32
v_accvgpr_write_b32 a17, v33
v_mfma_f32_16x16x32_bf16 v[28:31], v[204:207], v[92:95], v[28:31]
ds_read_b64_tr_b16 v[202:203], v218 offset:14336
buffer_load_dwordx4 v236, s[36:39], s28 offen offset:3072 sc1 lds
s_waitcnt lgkmcnt(6)
v_mfma_f32_16x16x32_bf16 v[0:3], v[64:67], v[96:99], v[0:3]
ds_read_b64_tr_b16 v[204:205], v219 offset:12288
v_accvgpr_write_b32 a18, v34
v_accvgpr_write_b32 a19, v35
v_mfma_f32_16x16x32_bf16 v[8:11], v[64:67], v[100:103], v[8:11]
ds_read_b64_tr_b16 v[206:207], v219 offset:14336
buffer_store_dwordx4 v[32:35], v236, s[60:63], s96 offen offset:0 sc1
v_mfma_f32_16x16x32_bf16 v[4:7], v[68:71], v[96:99], v[4:7]
ds_read_b64_tr_b16 v[64:65], v216 offset:16384
s_add_u32 m0, s68, 135168
s_add_u32 s28, s84, 20480
buffer_load_dwordx4 v236, s[36:39], s28 offen offset:0 sc1 lds
v_mfma_f32_16x16x32_bf16 v[12:15], v[68:71], v[100:103], v[12:15]
ds_read_b64_tr_b16 v[66:67], v216 offset:18432
v_mul_f32 v40, 0x3db8aa3b, v40
v_mul_f32 v41, 0x3db8aa3b, v41
v_mfma_f32_16x16x32_bf16 v[16:19], v[72:75], v[96:99], v[16:19]
ds_read_b64_tr_b16 v[68:69], v217 offset:16384
v_mul_f32 v42, 0x3db8aa3b, v42
v_mul_f32 v43, 0x3db8aa3b, v43
v_mfma_f32_16x16x32_bf16 v[24:27], v[72:75], v[100:103], v[24:27]
ds_read_b64_tr_b16 v[70:71], v217 offset:18432
buffer_load_dwordx4 v236, s[36:39], s28 offen offset:1024 sc1 lds
v_mfma_f32_16x16x32_bf16 v[20:23], v[76:79], v[96:99], v[20:23]
ds_read_b64_tr_b16 v[72:73], v218 offset:16384
v_mul_f32 v44, 0x3db8aa3b, v44
v_mul_f32 v45, 0x3db8aa3b, v45
v_mfma_f32_16x16x32_bf16 v[28:31], v[76:79], v[100:103], v[28:31]
ds_read_b64_tr_b16 v[74:75], v218 offset:18432
v_mul_f32 v46, 0x3db8aa3b, v46
s_waitcnt lgkmcnt(6)
v_mfma_f32_16x16x32_bf16 v[0:3], v[192:195], v[104:107], v[0:3]
ds_read_b64_tr_b16 v[76:77], v219 offset:16384
buffer_load_dwordx4 v236, s[36:39], s28 offen offset:2048 sc1 lds
v_mfma_f32_16x16x32_bf16 v[8:11], v[192:195], v[108:111], v[8:11]
ds_read_b64_tr_b16 v[78:79], v219 offset:18432
v_mul_f32 v47, 0x3db8aa3b, v47
v_cvt_pk_bf16_f32 v40, v40, v41
v_mfma_f32_16x16x32_bf16 v[4:7], v[196:199], v[104:107], v[4:7]
ds_read_b64_tr_b16 v[192:193], v216 offset:20480
v_cvt_pk_bf16_f32 v41, v42, v43
v_cvt_pk_bf16_f32 v42, v44, v45
v_mfma_f32_16x16x32_bf16 v[12:15], v[196:199], v[108:111], v[12:15]
ds_read_b64_tr_b16 v[194:195], v216 offset:22528
buffer_load_dwordx4 v236, s[36:39], s28 offen offset:3072 sc1 lds
v_mfma_f32_16x16x32_bf16 v[16:19], v[200:203], v[104:107], v[16:19]
ds_read_b64_tr_b16 v[196:197], v217 offset:20480
v_cvt_pk_bf16_f32 v43, v46, v47
v_accvgpr_write_b32 a20, v40
v_mfma_f32_16x16x32_bf16 v[24:27], v[200:203], v[108:111], v[24:27]
)ASM"
R"ASM(
ds_read_b64_tr_b16 v[198:199], v217 offset:22528
v_accvgpr_write_b32 a21, v41
v_accvgpr_write_b32 a22, v42
v_mfma_f32_16x16x32_bf16 v[20:23], v[204:207], v[104:107], v[20:23]
ds_read_b64_tr_b16 v[200:201], v218 offset:20480
v_accvgpr_write_b32 a23, v43
buffer_store_dwordx4 v[40:43], v236, s[60:63], s96 offen offset:1024 sc1
v_mfma_f32_16x16x32_bf16 v[28:31], v[204:207], v[108:111], v[28:31]
ds_read_b64_tr_b16 v[202:203], v218 offset:22528
s_add_u32 m0, s68, 32768
s_mov_b32 s99, 384
buffer_load_dwordx4 v251, s[52:55], s99 offen offset:0 lds
s_waitcnt lgkmcnt(6)
v_mfma_f32_16x16x32_bf16 v[0:3], v[64:67], v[112:115], v[0:3]
ds_read_b64_tr_b16 v[204:205], v219 offset:20480
v_mul_f32 v48, 0x3db8aa3b, v48
v_mul_f32 v49, 0x3db8aa3b, v49
v_mfma_f32_16x16x32_bf16 v[8:11], v[64:67], v[116:119], v[8:11]
ds_read_b64_tr_b16 v[206:207], v219 offset:22528
v_mul_f32 v50, 0x3db8aa3b, v50
v_mul_f32 v51, 0x3db8aa3b, v51
v_mfma_f32_16x16x32_bf16 v[4:7], v[68:71], v[112:115], v[4:7]
ds_read_b64_tr_b16 v[64:65], v216 offset:24576
s_mov_b32 s99, 130432
buffer_load_dwordx4 v251, s[52:55], s99 offen offset:1024 lds
v_mfma_f32_16x16x32_bf16 v[12:15], v[68:71], v[116:119], v[12:15]
ds_read_b64_tr_b16 v[66:67], v216 offset:26624
v_mul_f32 v52, 0x3db8aa3b, v52
v_mul_f32 v53, 0x3db8aa3b, v53
v_mfma_f32_16x16x32_bf16 v[16:19], v[72:75], v[112:115], v[16:19]
ds_read_b64_tr_b16 v[68:69], v217 offset:24576
v_mul_f32 v54, 0x3db8aa3b, v54
v_mul_f32 v55, 0x3db8aa3b, v55
v_cvt_pk_bf16_f32 v48, v48, v49
v_mfma_f32_16x16x32_bf16 v[24:27], v[72:75], v[116:119], v[24:27]
ds_read_b64_tr_b16 v[70:71], v217 offset:26624
s_mov_b32 s99, 31104
buffer_load_dwordx4 v251, s[52:55], s99 offen offset:2048 lds
v_mfma_f32_16x16x32_bf16 v[20:23], v[76:79], v[112:115], v[20:23]
ds_read_b64_tr_b16 v[72:73], v218 offset:24576
v_cvt_pk_bf16_f32 v49, v50, v51
v_cvt_pk_bf16_f32 v50, v52, v53
v_cvt_pk_bf16_f32 v51, v54, v55
v_mfma_f32_16x16x32_bf16 v[28:31], v[76:79], v[116:119], v[28:31]
ds_read_b64_tr_b16 v[74:75], v218 offset:26624
v_accvgpr_write_b32 a24, v48
v_accvgpr_write_b32 a25, v49
s_waitcnt lgkmcnt(6)
v_mfma_f32_16x16x32_bf16 v[0:3], v[192:195], v[120:123], v[0:3]
ds_read_b64_tr_b16 v[76:77], v219 offset:24576
s_mov_b32 s99, 161152
buffer_load_dwordx4 v251, s[52:55], s99 offen offset:3072 lds
v_mfma_f32_16x16x32_bf16 v[8:11], v[192:195], v[124:127], v[8:11]
ds_read_b64_tr_b16 v[78:79], v219 offset:26624
v_accvgpr_write_b32 a26, v50
v_accvgpr_write_b32 a27, v51
v_mfma_f32_16x16x32_bf16 v[4:7], v[196:199], v[120:123], v[4:7]
ds_read_b64_tr_b16 v[192:193], v216 offset:28672
buffer_store_dwordx4 v[48:51], v236, s[60:63], s96 offen offset:2048 sc1
v_mul_f32 v56, 0x3db8aa3b, v56
v_mfma_f32_16x16x32_bf16 v[12:15], v[196:199], v[124:127], v[12:15]
ds_read_b64_tr_b16 v[194:195], v216 offset:30720
s_add_u32 m0, m0, 4096
s_mov_b32 s99, 262528
buffer_load_dwordx4 v251, s[52:55], s99 offen offset:0 lds
v_mfma_f32_16x16x32_bf16 v[16:19], v[200:203], v[120:123], v[16:19]
ds_read_b64_tr_b16 v[196:197], v217 offset:28672
v_mul_f32 v57, 0x3db8aa3b, v57
v_mul_f32 v58, 0x3db8aa3b, v58
v_mul_f32 v59, 0x3db8aa3b, v59
v_mfma_f32_16x16x32_bf16 v[24:27], v[200:203], v[124:127], v[24:27]
ds_read_b64_tr_b16 v[198:199], v217 offset:30720
v_mul_f32 v60, 0x3db8aa3b, v60
v_mul_f32 v61, 0x3db8aa3b, v61
v_mul_f32 v62, 0x3db8aa3b, v62
v_mfma_f32_16x16x32_bf16 v[20:23], v[204:207], v[120:123], v[20:23]
ds_read_b64_tr_b16 v[200:201], v218 offset:28672
s_mov_b32 s99, 392576
buffer_load_dwordx4 v251, s[52:55], s99 offen offset:1024 lds
v_mfma_f32_16x16x32_bf16 v[28:31], v[204:207], v[124:127], v[28:31]
ds_read_b64_tr_b16 v[202:203], v218 offset:30720
v_mul_f32 v63, 0x3db8aa3b, v63
v_cvt_pk_bf16_f32 v56, v56, v57
s_waitcnt lgkmcnt(6)
v_mfma_f32_16x16x32_bf16 v[0:3], v[64:67], v[128:131], v[0:3]
ds_read_b64_tr_b16 v[204:205], v219 offset:28672
v_cvt_pk_bf16_f32 v57, v58, v59
v_cvt_pk_bf16_f32 v58, v60, v61
v_cvt_pk_bf16_f32 v59, v62, v63
v_mfma_f32_16x16x32_bf16 v[8:11], v[64:67], v[132:135], v[8:11]
ds_read_b64_tr_b16 v[206:207], v219 offset:30720
v_add_u32 v216, 65536, v232
v_add_u32 v217, 65536, v233
v_add_u32 v218, 65536, v234
v_add_u32 v219, 65536, v235
s_mov_b32 s99, 293248
buffer_load_dwordx4 v251, s[52:55], s99 offen offset:2048 lds
v_mfma_f32_16x16x32_bf16 v[4:7], v[68:71], v[128:131], v[4:7]
s_waitcnt vmcnt(20)
s_barrier
ds_read_b64_tr_b16 v[64:65], v216 offset:0
v_accvgpr_write_b32 a28, v56
v_mfma_f32_16x16x32_bf16 v[12:15], v[68:71], v[132:135], v[12:15]
ds_read_b64_tr_b16 v[66:67], v216 offset:2048
v_accvgpr_write_b32 a29, v57
v_accvgpr_write_b32 a30, v58
v_accvgpr_write_b32 a31, v59
v_mfma_f32_16x16x32_bf16 v[16:19], v[72:75], v[128:131], v[16:19]
ds_read_b64_tr_b16 v[68:69], v217 offset:0
s_mov_b32 s99, 423296
buffer_load_dwordx4 v251, s[52:55], s99 offen offset:3072 lds
v_mfma_f32_16x16x32_bf16 v[24:27], v[72:75], v[132:135], v[24:27]
ds_read_b64_tr_b16 v[70:71], v217 offset:2048
buffer_store_dwordx4 v[56:59], v236, s[60:63], s96 offen offset:3072 sc1
s_add_u32 s98, s76, 81920
v_mfma_f32_16x16x32_bf16 v[20:23], v[76:79], v[128:131], v[20:23]
ds_read_b64_tr_b16 v[72:73], v218 offset:0
v_accvgpr_read_b32 v208, a208
v_accvgpr_read_b32 v209, a209
v_accvgpr_read_b32 v210, a210
v_mfma_f32_16x16x32_bf16 v[28:31], v[76:79], v[132:135], v[28:31]
ds_read_b64_tr_b16 v[74:75], v218 offset:2048
v_accvgpr_read_b32 v211, a211
v_accvgpr_read_b32 v212, a212
s_waitcnt lgkmcnt(6)
v_mfma_f32_16x16x32_bf16 v[0:3], v[192:195], v[136:139], v[0:3]
ds_read_b64_tr_b16 v[76:77], v219 offset:0
v_accvgpr_read_b32 v213, a213
v_accvgpr_read_b32 v214, a214
v_accvgpr_read_b32 v215, a215
v_mfma_f32_16x16x32_bf16 v[8:11], v[192:195], v[140:143], v[8:11]
ds_read_b64_tr_b16 v[78:79], v219 offset:2048
v_cvt_pk_bf16_f32 v208, v208, v209
v_cvt_pk_bf16_f32 v209, v210, v211
v_cvt_pk_bf16_f32 v210, v212, v213
v_mfma_f32_16x16x32_bf16 v[4:7], v[196:199], v[136:139], v[4:7]
ds_read_b64_tr_b16 v[192:193], v216 offset:4096
v_cvt_pk_bf16_f32 v211, v214, v215
buffer_store_dwordx4 v[208:211], v236, s[36:39], s98 offen offset:2048 sc1
v_mfma_f32_16x16x32_bf16 v[12:15], v[196:199], v[140:143], v[12:15]
ds_read_b64_tr_b16 v[194:195], v216 offset:6144
v_accvgpr_read_b32 v224, a216
v_accvgpr_read_b32 v225, a217
v_mfma_f32_16x16x32_bf16 v[16:19], v[200:203], v[136:139], v[16:19]
ds_read_b64_tr_b16 v[196:197], v217 offset:4096
v_accvgpr_read_b32 v226, a218
v_accvgpr_read_b32 v227, a219
v_accvgpr_read_b32 v228, a220
v_mfma_f32_16x16x32_bf16 v[24:27], v[200:203], v[140:143], v[24:27]
ds_read_b64_tr_b16 v[198:199], v217 offset:6144
v_accvgpr_read_b32 v229, a221
v_accvgpr_read_b32 v230, a222
v_accvgpr_read_b32 v231, a223
v_mfma_f32_16x16x32_bf16 v[20:23], v[204:207], v[136:139], v[20:23]
ds_read_b64_tr_b16 v[200:201], v218 offset:4096
v_cvt_pk_bf16_f32 v224, v224, v225
v_cvt_pk_bf16_f32 v225, v226, v227
v_cvt_pk_bf16_f32 v226, v228, v229
v_mfma_f32_16x16x32_bf16 v[28:31], v[204:207], v[140:143], v[28:31]
ds_read_b64_tr_b16 v[202:203], v218 offset:6144
v_cvt_pk_bf16_f32 v227, v230, v231
buffer_store_dwordx4 v[224:227], v236, s[36:39], s98 offen offset:3072 sc1
s_waitcnt lgkmcnt(6)
v_mfma_f32_16x16x32_bf16 v[0:3], v[64:67], v[144:147], v[0:3]
ds_read_b64_tr_b16 v[204:205], v219 offset:4096
s_add_u32 m0, s68, 98304
s_mov_b32 s99, 2097536
buffer_load_dwordx4 v251, s[52:55], s99 offen offset:0 lds
v_mfma_f32_16x16x32_bf16 v[8:11], v[64:67], v[148:151], v[8:11]
ds_read_b64_tr_b16 v[206:207], v219 offset:6144
v_mfma_f32_16x16x32_bf16 v[4:7], v[68:71], v[144:147], v[4:7]
ds_read_b64_tr_b16 v[64:65], v216 offset:8192
v_mfma_f32_16x16x32_bf16 v[12:15], v[68:71], v[148:151], v[12:15]
ds_read_b64_tr_b16 v[66:67], v216 offset:10240
v_mfma_f32_16x16x32_bf16 v[16:19], v[72:75], v[144:147], v[16:19]
ds_read_b64_tr_b16 v[68:69], v217 offset:8192
s_add_u32 s98, s76, 81920
s_add_u32 s98, s98, s77
v_mfma_f32_16x16x32_bf16 v[24:27], v[72:75], v[148:151], v[24:27]
ds_read_b64_tr_b16 v[70:71], v217 offset:10240
s_mov_b32 s99, 2227584
buffer_load_dwordx4 v251, s[52:55], s99 offen offset:1024 lds
v_mfma_f32_16x16x32_bf16 v[20:23], v[76:79], v[144:147], v[20:23]
ds_read_b64_tr_b16 v[72:73], v218 offset:8192
v_accvgpr_read_b32 v208, a224
v_accvgpr_read_b32 v209, a225
v_mfma_f32_16x16x32_bf16 v[28:31], v[76:79], v[148:151], v[28:31]
ds_read_b64_tr_b16 v[74:75], v218 offset:10240
v_accvgpr_read_b32 v210, a226
s_waitcnt lgkmcnt(6)
v_mfma_f32_16x16x32_bf16 v[0:3], v[192:195], v[152:155], v[0:3]
ds_read_b64_tr_b16 v[76:77], v219 offset:8192
v_accvgpr_read_b32 v211, a227
v_accvgpr_read_b32 v212, a228
v_mfma_f32_16x16x32_bf16 v[8:11], v[192:195], v[156:159], v[8:11]
ds_read_b64_tr_b16 v[78:79], v219 offset:10240
v_accvgpr_read_b32 v213, a229
v_mfma_f32_16x16x32_bf16 v[4:7], v[196:199], v[152:155], v[4:7]
ds_read_b64_tr_b16 v[192:193], v216 offset:12288
s_mov_b32 s99, 2128256
)ASM"
R"ASM(
buffer_load_dwordx4 v251, s[52:55], s99 offen offset:2048 lds
v_mfma_f32_16x16x32_bf16 v[12:15], v[196:199], v[156:159], v[12:15]
ds_read_b64_tr_b16 v[194:195], v216 offset:14336
v_accvgpr_read_b32 v214, a230
v_accvgpr_read_b32 v215, a231
v_mfma_f32_16x16x32_bf16 v[16:19], v[200:203], v[152:155], v[16:19]
ds_read_b64_tr_b16 v[196:197], v217 offset:12288
v_cvt_pk_bf16_f32 v208, v208, v209
v_cvt_pk_bf16_f32 v209, v210, v211
v_mfma_f32_16x16x32_bf16 v[24:27], v[200:203], v[156:159], v[24:27]
ds_read_b64_tr_b16 v[198:199], v217 offset:14336
v_cvt_pk_bf16_f32 v210, v212, v213
v_cvt_pk_bf16_f32 v211, v214, v215
v_mfma_f32_16x16x32_bf16 v[20:23], v[204:207], v[152:155], v[20:23]
ds_read_b64_tr_b16 v[200:201], v218 offset:12288
buffer_store_dwordx4 v[208:211], v236, s[36:39], s98 offen offset:0 sc1
v_mfma_f32_16x16x32_bf16 v[28:31], v[204:207], v[156:159], v[28:31]
ds_read_b64_tr_b16 v[202:203], v218 offset:14336
s_mov_b32 s99, 2258304
buffer_load_dwordx4 v251, s[52:55], s99 offen offset:3072 lds
s_waitcnt lgkmcnt(6)
v_mfma_f32_16x16x32_bf16 v[0:3], v[64:67], v[160:163], v[0:3]
ds_read_b64_tr_b16 v[204:205], v219 offset:12288
v_accvgpr_read_b32 v224, a232
v_mfma_f32_16x16x32_bf16 v[8:11], v[64:67], v[164:167], v[8:11]
ds_read_b64_tr_b16 v[206:207], v219 offset:14336
v_accvgpr_read_b32 v225, a233
v_accvgpr_read_b32 v226, a234
v_mfma_f32_16x16x32_bf16 v[4:7], v[68:71], v[160:163], v[4:7]
ds_read_b64_tr_b16 v[64:65], v216 offset:16384
v_accvgpr_read_b32 v227, a235
v_accvgpr_read_b32 v228, a236
v_mfma_f32_16x16x32_bf16 v[12:15], v[68:71], v[164:167], v[12:15]
ds_read_b64_tr_b16 v[66:67], v216 offset:18432
v_accvgpr_read_b32 v229, a237
v_accvgpr_read_b32 v230, a238
v_mfma_f32_16x16x32_bf16 v[16:19], v[72:75], v[160:163], v[16:19]
ds_read_b64_tr_b16 v[68:69], v217 offset:16384
s_add_u32 m0, m0, 4096
s_mov_b32 s99, 2359680
buffer_load_dwordx4 v251, s[52:55], s99 offen offset:0 lds
s_waitcnt vmcnt(19)
v_mfma_f32_16x16x32_bf16 v[24:27], v[72:75], v[164:167], v[24:27]
ds_read_b64_tr_b16 v[70:71], v217 offset:18432
v_accvgpr_read_b32 v231, a239
v_cvt_pk_bf16_f32 v224, v224, v225
v_mfma_f32_16x16x32_bf16 v[20:23], v[76:79], v[160:163], v[20:23]
ds_read_b64_tr_b16 v[72:73], v218 offset:16384
ds_read_b128 a[192:195], v221 offset:0
v_cvt_pk_bf16_f32 v225, v226, v227
v_mfma_f32_16x16x32_bf16 v[28:31], v[76:79], v[164:167], v[28:31]
ds_read_b64_tr_b16 v[74:75], v218 offset:18432
v_cvt_pk_bf16_f32 v226, v228, v229
s_waitcnt lgkmcnt(7)
v_mfma_f32_16x16x32_bf16 v[0:3], v[192:195], v[168:171], v[0:3]
ds_read_b64_tr_b16 v[76:77], v219 offset:16384
ds_read_b128 a[196:199], v221 offset:1024
v_cvt_pk_bf16_f32 v227, v230, v231
v_mfma_f32_16x16x32_bf16 v[8:11], v[192:195], v[172:175], v[8:11]
ds_read_b64_tr_b16 v[78:79], v219 offset:18432
s_mov_b32 s99, 2489728
buffer_load_dwordx4 v251, s[52:55], s99 offen offset:1024 lds
v_mfma_f32_16x16x32_bf16 v[4:7], v[196:199], v[168:171], v[4:7]
ds_read_b64_tr_b16 v[192:193], v216 offset:20480
ds_read_b128 a[200:203], v221 offset:2048
buffer_store_dwordx4 v[224:227], v236, s[36:39], s98 offen offset:1024 sc1
v_mfma_f32_16x16x32_bf16 v[12:15], v[196:199], v[172:175], v[12:15]
ds_read_b64_tr_b16 v[194:195], v216 offset:22528
s_add_u32 s98, s76, 0
s_add_u32 s98, s98, s77
v_mfma_f32_16x16x32_bf16 v[16:19], v[200:203], v[168:171], v[16:19]
ds_read_b64_tr_b16 v[196:197], v217 offset:20480
ds_read_b128 a[204:207], v221 offset:3072
v_accvgpr_read_b32 v208, a32
v_mfma_f32_16x16x32_bf16 v[24:27], v[200:203], v[172:175], v[24:27]
ds_read_b64_tr_b16 v[198:199], v217 offset:22528
v_accvgpr_read_b32 v209, a33
v_accvgpr_read_b32 v210, a34
v_mfma_f32_16x16x32_bf16 v[20:23], v[204:207], v[168:171], v[20:23]
ds_read_b64_tr_b16 v[200:201], v218 offset:20480
s_mov_b32 s99, 2390400
buffer_load_dwordx4 v251, s[52:55], s99 offen offset:2048 lds
ds_read_b128 a[208:211], v221 offset:4096
v_mfma_f32_16x16x32_bf16 v[28:31], v[204:207], v[172:175], v[28:31]
ds_read_b64_tr_b16 v[202:203], v218 offset:22528
v_accvgpr_read_b32 v211, a35
s_waitcnt lgkmcnt(9)
v_mfma_f32_16x16x32_bf16 v[0:3], v[64:67], v[176:179], v[0:3]
ds_read_b64_tr_b16 v[204:205], v219 offset:20480
ds_read_b128 a[212:215], v221 offset:5120
v_accvgpr_read_b32 v212, a36
v_mfma_f32_16x16x32_bf16 v[8:11], v[64:67], v[180:183], v[8:11]
ds_read_b64_tr_b16 v[206:207], v219 offset:22528
v_add_u32 v216, 32768, v232
v_add_u32 v217, 32768, v233
v_add_u32 v218, 32768, v234
v_add_u32 v219, 32768, v235
v_mfma_f32_16x16x32_bf16 v[4:7], v[68:71], v[176:179], v[4:7]
s_waitcnt vmcnt(12)
s_barrier
ds_read_b64_tr_b16 v[64:65], v216 offset:0
ds_read_b128 a[216:219], v221 offset:6144
v_mfma_f32_16x16x32_bf16 v[12:15], v[68:71], v[180:183], v[12:15]
ds_read_b64_tr_b16 v[66:67], v216 offset:2048
s_mov_b32 s99, 2520448
buffer_load_dwordx4 v251, s[52:55], s99 offen offset:3072 lds
v_mfma_f32_16x16x32_bf16 v[16:19], v[72:75], v[176:179], v[16:19]
ds_read_b64_tr_b16 v[68:69], v217 offset:0
ds_read_b128 a[220:223], v221 offset:7168
v_accvgpr_read_b32 v213, a37
v_mfma_f32_16x16x32_bf16 v[24:27], v[72:75], v[180:183], v[24:27]
ds_read_b64_tr_b16 v[70:71], v217 offset:2048
v_accvgpr_read_b32 v214, a38
v_accvgpr_read_b32 v215, a39
v_mfma_f32_16x16x32_bf16 v[20:23], v[76:79], v[176:179], v[20:23]
ds_read_b64_tr_b16 v[72:73], v218 offset:0
v_cvt_pk_bf16_f32 v208, v208, v209
v_cvt_pk_bf16_f32 v209, v210, v211
v_mfma_f32_16x16x32_bf16 v[28:31], v[76:79], v[180:183], v[28:31]
ds_read_b64_tr_b16 v[74:75], v218 offset:2048
v_cvt_pk_bf16_f32 v210, v212, v213
s_waitcnt lgkmcnt(8)
v_mfma_f32_16x16x32_bf16 v[0:3], v[192:195], v[184:187], v[0:3]
ds_read_b64_tr_b16 v[76:77], v219 offset:0
v_cvt_pk_bf16_f32 v211, v214, v215
buffer_store_dwordx4 v[208:211], v236, s[36:39], s98 offen offset:0 sc1
v_mfma_f32_16x16x32_bf16 v[8:11], v[192:195], v[188:191], v[8:11]
ds_read_b64_tr_b16 v[78:79], v219 offset:2048
v_accvgpr_read_b32 v224, a40
v_accvgpr_read_b32 v225, a41
v_mfma_f32_16x16x32_bf16 v[4:7], v[196:199], v[184:187], v[4:7]
ds_read_b64_tr_b16 v[192:193], v216 offset:4096
v_accvgpr_read_b32 v226, a42
v_accvgpr_read_b32 v227, a43
v_mfma_f32_16x16x32_bf16 v[12:15], v[196:199], v[188:191], v[12:15]
ds_read_b64_tr_b16 v[194:195], v216 offset:6144
v_accvgpr_read_b32 v228, a44
v_accvgpr_read_b32 v229, a45
v_mfma_f32_16x16x32_bf16 v[16:19], v[200:203], v[184:187], v[16:19]
ds_read_b64_tr_b16 v[196:197], v217 offset:4096
v_accvgpr_read_b32 v230, a46
v_accvgpr_read_b32 v231, a47
v_mfma_f32_16x16x32_bf16 v[24:27], v[200:203], v[188:191], v[24:27]
ds_read_b64_tr_b16 v[198:199], v217 offset:6144
v_cvt_pk_bf16_f32 v224, v224, v225
v_cvt_pk_bf16_f32 v225, v226, v227
v_mfma_f32_16x16x32_bf16 v[20:23], v[204:207], v[184:187], v[20:23]
ds_read_b64_tr_b16 v[200:201], v218 offset:4096
v_cvt_pk_bf16_f32 v226, v228, v229
v_cvt_pk_bf16_f32 v227, v230, v231
v_mfma_f32_16x16x32_bf16 v[28:31], v[204:207], v[188:191], v[28:31]
ds_read_b64_tr_b16 v[202:203], v218 offset:6144
buffer_store_dwordx4 v[224:227], v236, s[36:39], s98 offen offset:1024 sc1
s_waitcnt lgkmcnt(6)
v_mfma_f32_16x16x32_bf16 v[32:35], v[64:67], v[80:83], 0
ds_read_b64_tr_b16 v[204:205], v219 offset:4096
v_mfma_f32_16x16x32_bf16 v[40:43], v[64:67], v[84:87], 0
ds_read_b64_tr_b16 v[206:207], v219 offset:6144
v_mfma_f32_16x16x32_bf16 v[36:39], v[68:71], v[80:83], 0
ds_read_b64_tr_b16 v[64:65], v216 offset:8192
v_mfma_f32_16x16x32_bf16 v[44:47], v[68:71], v[84:87], 0
ds_read_b64_tr_b16 v[66:67], v216 offset:10240
v_mfma_f32_16x16x32_bf16 v[48:51], v[72:75], v[80:83], 0
ds_read_b64_tr_b16 v[68:69], v217 offset:8192
s_add_u32 s96, s70, 8192
v_mul_f32 v0, 0x3db8aa3b, v0
v_mfma_f32_16x16x32_bf16 v[56:59], v[72:75], v[84:87], 0
ds_read_b64_tr_b16 v[70:71], v217 offset:10240
v_mul_f32 v1, 0x3db8aa3b, v1
v_mul_f32 v2, 0x3db8aa3b, v2
v_mfma_f32_16x16x32_bf16 v[52:55], v[76:79], v[80:83], 0
ds_read_b64_tr_b16 v[72:73], v218 offset:8192
s_add_u32 m0, s68, 131072
s_add_u32 s28, s84, 24576
buffer_load_dwordx4 v236, s[36:39], s28 offen offset:0 sc1 lds
v_mfma_f32_16x16x32_bf16 v[60:63], v[76:79], v[84:87], 0
ds_read_b64_tr_b16 v[74:75], v218 offset:10240
v_mul_f32 v3, 0x3db8aa3b, v3
s_waitcnt lgkmcnt(6)
v_mfma_f32_16x16x32_bf16 v[32:35], v[192:195], v[88:91], v[32:35]
ds_read_b64_tr_b16 v[76:77], v219 offset:8192
v_mul_f32 v4, 0x3db8aa3b, v4
v_mfma_f32_16x16x32_bf16 v[40:43], v[192:195], v[92:95], v[40:43]
ds_read_b64_tr_b16 v[78:79], v219 offset:10240
v_mul_f32 v5, 0x3db8aa3b, v5
v_mfma_f32_16x16x32_bf16 v[36:39], v[196:199], v[88:91], v[36:39]
ds_read_b64_tr_b16 v[192:193], v216 offset:12288
buffer_load_dwordx4 v236, s[36:39], s28 offen offset:1024 sc1 lds
v_mfma_f32_16x16x32_bf16 v[44:47], v[196:199], v[92:95], v[44:47]
ds_read_b64_tr_b16 v[194:195], v216 offset:14336
v_mul_f32 v6, 0x3db8aa3b, v6
v_mfma_f32_16x16x32_bf16 v[48:51], v[200:203], v[88:91], v[48:51]
ds_read_b64_tr_b16 v[196:197], v217 offset:12288
v_mul_f32 v7, 0x3db8aa3b, v7
v_cvt_pk_bf16_f32 v0, v0, v1
v_mfma_f32_16x16x32_bf16 v[56:59], v[200:203], v[92:95], v[56:59]
ds_read_b64_tr_b16 v[198:199], v217 offset:14336
v_cvt_pk_bf16_f32 v1, v2, v3
v_cvt_pk_bf16_f32 v2, v4, v5
v_mfma_f32_16x16x32_bf16 v[52:55], v[204:207], v[88:91], v[52:55]
)ASM"
R"ASM(
ds_read_b64_tr_b16 v[200:201], v218 offset:12288
buffer_load_dwordx4 v236, s[36:39], s28 offen offset:2048 sc1 lds
v_mfma_f32_16x16x32_bf16 v[60:63], v[204:207], v[92:95], v[60:63]
ds_read_b64_tr_b16 v[202:203], v218 offset:14336
v_cvt_pk_bf16_f32 v3, v6, v7
s_waitcnt lgkmcnt(6)
v_mfma_f32_16x16x32_bf16 v[32:35], v[64:67], v[96:99], v[32:35]
ds_read_b64_tr_b16 v[204:205], v219 offset:12288
v_accvgpr_write_b32 a32, v0
v_accvgpr_write_b32 a33, v1
v_mfma_f32_16x16x32_bf16 v[40:43], v[64:67], v[100:103], v[40:43]
ds_read_b64_tr_b16 v[206:207], v219 offset:14336
v_accvgpr_write_b32 a34, v2
v_accvgpr_write_b32 a35, v3
v_mfma_f32_16x16x32_bf16 v[36:39], v[68:71], v[96:99], v[36:39]
ds_read_b64_tr_b16 v[64:65], v216 offset:16384
buffer_load_dwordx4 v236, s[36:39], s28 offen offset:3072 sc1 lds
v_mfma_f32_16x16x32_bf16 v[44:47], v[68:71], v[100:103], v[44:47]
ds_read_b64_tr_b16 v[66:67], v216 offset:18432
buffer_store_dwordx4 v[0:3], v236, s[60:63], s96 offen offset:0 sc1
v_mfma_f32_16x16x32_bf16 v[48:51], v[72:75], v[96:99], v[48:51]
ds_read_b64_tr_b16 v[68:69], v217 offset:16384
v_mul_f32 v8, 0x3db8aa3b, v8
v_mfma_f32_16x16x32_bf16 v[56:59], v[72:75], v[100:103], v[56:59]
ds_read_b64_tr_b16 v[70:71], v217 offset:18432
v_mul_f32 v9, 0x3db8aa3b, v9
v_mul_f32 v10, 0x3db8aa3b, v10
v_mfma_f32_16x16x32_bf16 v[52:55], v[76:79], v[96:99], v[52:55]
ds_read_b64_tr_b16 v[72:73], v218 offset:16384
s_add_u32 m0, s68, 0
s_mov_b32 s99, 0
buffer_load_dwordx4 v242, s[88:91], s99 offen offset:0 lds
v_mfma_f32_16x16x32_bf16 v[60:63], v[76:79], v[100:103], v[60:63]
ds_read_b64_tr_b16 v[74:75], v218 offset:18432
v_mul_f32 v11, 0x3db8aa3b, v11
s_waitcnt lgkmcnt(6)
v_mfma_f32_16x16x32_bf16 v[32:35], v[192:195], v[104:107], v[32:35]
ds_read_b64_tr_b16 v[76:77], v219 offset:16384
v_mul_f32 v12, 0x3db8aa3b, v12
v_mul_f32 v13, 0x3db8aa3b, v13
v_mfma_f32_16x16x32_bf16 v[40:43], v[192:195], v[108:111], v[40:43]
ds_read_b64_tr_b16 v[78:79], v219 offset:18432
v_mul_f32 v14, 0x3db8aa3b, v14
v_mul_f32 v15, 0x3db8aa3b, v15
v_mfma_f32_16x16x32_bf16 v[36:39], v[196:199], v[104:107], v[36:39]
ds_read_b64_tr_b16 v[192:193], v216 offset:20480
s_mov_b32 s99, 27648
buffer_load_dwordx4 v242, s[88:91], s99 offen offset:1024 lds
v_mfma_f32_16x16x32_bf16 v[44:47], v[196:199], v[108:111], v[44:47]
ds_read_b64_tr_b16 v[194:195], v216 offset:22528
v_cvt_pk_bf16_f32 v8, v8, v9
v_cvt_pk_bf16_f32 v9, v10, v11
v_mfma_f32_16x16x32_bf16 v[48:51], v[200:203], v[104:107], v[48:51]
ds_read_b64_tr_b16 v[196:197], v217 offset:20480
v_cvt_pk_bf16_f32 v10, v12, v13
v_cvt_pk_bf16_f32 v11, v14, v15
v_mfma_f32_16x16x32_bf16 v[56:59], v[200:203], v[108:111], v[56:59]
ds_read_b64_tr_b16 v[198:199], v217 offset:22528
v_accvgpr_write_b32 a36, v8
v_accvgpr_write_b32 a37, v9
v_mfma_f32_16x16x32_bf16 v[52:55], v[204:207], v[104:107], v[52:55]
ds_read_b64_tr_b16 v[200:201], v218 offset:20480
v_accvgpr_write_b32 a38, v10
v_accvgpr_write_b32 a39, v11
v_mfma_f32_16x16x32_bf16 v[60:63], v[204:207], v[108:111], v[60:63]
ds_read_b64_tr_b16 v[202:203], v218 offset:22528
s_mov_b32 s99, 55296
buffer_load_dwordx4 v242, s[88:91], s99 offen offset:2048 lds
s_waitcnt lgkmcnt(6)
v_mfma_f32_16x16x32_bf16 v[32:35], v[64:67], v[112:115], v[32:35]
ds_read_b64_tr_b16 v[204:205], v219 offset:20480
buffer_store_dwordx4 v[8:11], v236, s[60:63], s96 offen offset:1024 sc1
v_mfma_f32_16x16x32_bf16 v[40:43], v[64:67], v[116:119], v[40:43]
ds_read_b64_tr_b16 v[206:207], v219 offset:22528
v_mul_f32 v16, 0x3db8aa3b, v16
v_mul_f32 v17, 0x3db8aa3b, v17
v_mfma_f32_16x16x32_bf16 v[36:39], v[68:71], v[112:115], v[36:39]
ds_read_b64_tr_b16 v[64:65], v216 offset:24576
v_mul_f32 v18, 0x3db8aa3b, v18
v_mul_f32 v19, 0x3db8aa3b, v19
v_mfma_f32_16x16x32_bf16 v[44:47], v[68:71], v[116:119], v[44:47]
ds_read_b64_tr_b16 v[66:67], v216 offset:26624
s_mov_b32 s99, 82944
buffer_load_dwordx4 v242, s[88:91], s99 offen offset:3072 lds
v_mfma_f32_16x16x32_bf16 v[48:51], v[72:75], v[112:115], v[48:51]
ds_read_b64_tr_b16 v[68:69], v217 offset:24576
v_mul_f32 v20, 0x3db8aa3b, v20
v_mul_f32 v21, 0x3db8aa3b, v21
v_mfma_f32_16x16x32_bf16 v[56:59], v[72:75], v[116:119], v[56:59]
ds_read_b64_tr_b16 v[70:71], v217 offset:26624
v_mul_f32 v22, 0x3db8aa3b, v22
v_mul_f32 v23, 0x3db8aa3b, v23
v_mfma_f32_16x16x32_bf16 v[52:55], v[76:79], v[112:115], v[52:55]
ds_read_b64_tr_b16 v[72:73], v218 offset:24576
v_cvt_pk_bf16_f32 v16, v16, v17
v_cvt_pk_bf16_f32 v17, v18, v19
v_mfma_f32_16x16x32_bf16 v[60:63], v[76:79], v[116:119], v[60:63]
ds_read_b64_tr_b16 v[74:75], v218 offset:26624
s_add_u32 m0, m0, 4096
s_mov_b32 s99, 114688
buffer_load_dwordx4 v242, s[88:91], s99 offen offset:0 lds
s_waitcnt lgkmcnt(6)
v_mfma_f32_16x16x32_bf16 v[32:35], v[192:195], v[120:123], v[32:35]
ds_read_b64_tr_b16 v[76:77], v219 offset:24576
v_cvt_pk_bf16_f32 v18, v20, v21
v_cvt_pk_bf16_f32 v19, v22, v23
v_mfma_f32_16x16x32_bf16 v[40:43], v[192:195], v[124:127], v[40:43]
ds_read_b64_tr_b16 v[78:79], v219 offset:26624
v_accvgpr_write_b32 a40, v16
v_accvgpr_write_b32 a41, v17
v_mfma_f32_16x16x32_bf16 v[36:39], v[196:199], v[120:123], v[36:39]
ds_read_b64_tr_b16 v[192:193], v216 offset:28672
v_accvgpr_write_b32 a42, v18
v_accvgpr_write_b32 a43, v19
v_mfma_f32_16x16x32_bf16 v[44:47], v[196:199], v[124:127], v[44:47]
ds_read_b64_tr_b16 v[194:195], v216 offset:30720
s_mov_b32 s99, 142336
buffer_load_dwordx4 v242, s[88:91], s99 offen offset:1024 lds
v_mfma_f32_16x16x32_bf16 v[48:51], v[200:203], v[120:123], v[48:51]
ds_read_b64_tr_b16 v[196:197], v217 offset:28672
buffer_store_dwordx4 v[16:19], v236, s[60:63], s96 offen offset:2048 sc1
v_mfma_f32_16x16x32_bf16 v[56:59], v[200:203], v[124:127], v[56:59]
ds_read_b64_tr_b16 v[198:199], v217 offset:30720
v_mul_f32 v24, 0x3db8aa3b, v24
v_mfma_f32_16x16x32_bf16 v[52:55], v[204:207], v[120:123], v[52:55]
ds_read_b64_tr_b16 v[200:201], v218 offset:28672
v_mul_f32 v25, 0x3db8aa3b, v25
v_mfma_f32_16x16x32_bf16 v[60:63], v[204:207], v[124:127], v[60:63]
ds_read_b64_tr_b16 v[202:203], v218 offset:30720
s_mov_b32 s99, 169984
buffer_load_dwordx4 v242, s[88:91], s99 offen offset:2048 lds
s_waitcnt lgkmcnt(6)
v_mfma_f32_16x16x32_bf16 v[32:35], v[64:67], v[128:131], v[32:35]
ds_read_b64_tr_b16 v[204:205], v219 offset:28672
v_mul_f32 v26, 0x3db8aa3b, v26
v_mfma_f32_16x16x32_bf16 v[40:43], v[64:67], v[132:135], v[40:43]
ds_read_b64_tr_b16 v[206:207], v219 offset:30720
v_add_u32 v216, 98304, v232
v_add_u32 v217, 98304, v233
v_add_u32 v218, 98304, v234
v_add_u32 v219, 98304, v235
v_mfma_f32_16x16x32_bf16 v[36:39], v[68:71], v[128:131], v[36:39]
s_waitcnt vmcnt(16)
s_barrier
ds_read_b64_tr_b16 v[64:65], v216 offset:0
v_mfma_f32_16x16x32_bf16 v[44:47], v[68:71], v[132:135], v[44:47]
ds_read_b64_tr_b16 v[66:67], v216 offset:2048
s_mov_b32 s99, 197632
buffer_load_dwordx4 v242, s[88:91], s99 offen offset:3072 lds
v_mfma_f32_16x16x32_bf16 v[48:51], v[72:75], v[128:131], v[48:51]
ds_read_b64_tr_b16 v[68:69], v217 offset:0
v_mul_f32 v27, 0x3db8aa3b, v27
v_mfma_f32_16x16x32_bf16 v[56:59], v[72:75], v[132:135], v[56:59]
ds_read_b64_tr_b16 v[70:71], v217 offset:2048
v_mul_f32 v28, 0x3db8aa3b, v28
v_mfma_f32_16x16x32_bf16 v[52:55], v[76:79], v[128:131], v[52:55]
ds_read_b64_tr_b16 v[72:73], v218 offset:0
v_mul_f32 v29, 0x3db8aa3b, v29
v_mfma_f32_16x16x32_bf16 v[60:63], v[76:79], v[132:135], v[60:63]
ds_read_b64_tr_b16 v[74:75], v218 offset:2048
s_waitcnt lgkmcnt(6)
v_mfma_f32_16x16x32_bf16 v[32:35], v[192:195], v[136:139], v[32:35]
ds_read_b64_tr_b16 v[76:77], v219 offset:0
v_mul_f32 v30, 0x3db8aa3b, v30
v_mfma_f32_16x16x32_bf16 v[40:43], v[192:195], v[140:143], v[40:43]
ds_read_b64_tr_b16 v[78:79], v219 offset:2048
v_mul_f32 v31, 0x3db8aa3b, v31
v_mfma_f32_16x16x32_bf16 v[36:39], v[196:199], v[136:139], v[36:39]
ds_read_b64_tr_b16 v[192:193], v216 offset:4096
v_cvt_pk_bf16_f32 v24, v24, v25
v_mfma_f32_16x16x32_bf16 v[44:47], v[196:199], v[140:143], v[44:47]
ds_read_b64_tr_b16 v[194:195], v216 offset:6144
v_cvt_pk_bf16_f32 v25, v26, v27
v_mfma_f32_16x16x32_bf16 v[48:51], v[200:203], v[136:139], v[48:51]
ds_read_b64_tr_b16 v[196:197], v217 offset:4096
v_cvt_pk_bf16_f32 v26, v28, v29
v_cvt_pk_bf16_f32 v27, v30, v31
v_mfma_f32_16x16x32_bf16 v[56:59], v[200:203], v[140:143], v[56:59]
ds_read_b64_tr_b16 v[198:199], v217 offset:6144
v_accvgpr_write_b32 a44, v24
v_accvgpr_write_b32 a45, v25
v_mfma_f32_16x16x32_bf16 v[52:55], v[204:207], v[136:139], v[52:55]
ds_read_b64_tr_b16 v[200:201], v218 offset:4096
v_accvgpr_write_b32 a46, v26
v_accvgpr_write_b32 a47, v27
v_mfma_f32_16x16x32_bf16 v[60:63], v[204:207], v[140:143], v[60:63]
ds_read_b64_tr_b16 v[202:203], v218 offset:6144
buffer_store_dwordx4 v[24:27], v236, s[60:63], s96 offen offset:3072 sc1
s_waitcnt lgkmcnt(6)
v_mfma_f32_16x16x32_bf16 v[32:35], v[64:67], v[144:147], v[32:35]
ds_read_b64_tr_b16 v[204:205], v219 offset:4096
s_add_u32 m0, s68, 65536
s_mov_b32 s99, 917504
buffer_load_dwordx4 v242, s[88:91], s99 offen offset:0 lds
v_mfma_f32_16x16x32_bf16 v[40:43], v[64:67], v[148:151], v[40:43]
ds_read_b64_tr_b16 v[206:207], v219 offset:6144
v_mfma_f32_16x16x32_bf16 v[36:39], v[68:71], v[144:147], v[36:39]
ds_read_b64_tr_b16 v[64:65], v216 offset:8192
v_mfma_f32_16x16x32_bf16 v[44:47], v[68:71], v[148:151], v[44:47]
ds_read_b64_tr_b16 v[66:67], v216 offset:10240
)ASM"
R"ASM(
v_mfma_f32_16x16x32_bf16 v[48:51], v[72:75], v[144:147], v[48:51]
ds_read_b64_tr_b16 v[68:69], v217 offset:8192
v_mfma_f32_16x16x32_bf16 v[56:59], v[72:75], v[148:151], v[56:59]
ds_read_b64_tr_b16 v[70:71], v217 offset:10240
s_mov_b32 s99, 945152
buffer_load_dwordx4 v242, s[88:91], s99 offen offset:1024 lds
v_mfma_f32_16x16x32_bf16 v[52:55], v[76:79], v[144:147], v[52:55]
ds_read_b64_tr_b16 v[72:73], v218 offset:8192
v_mfma_f32_16x16x32_bf16 v[60:63], v[76:79], v[148:151], v[60:63]
ds_read_b64_tr_b16 v[74:75], v218 offset:10240
s_waitcnt lgkmcnt(6)
v_mfma_f32_16x16x32_bf16 v[32:35], v[192:195], v[152:155], v[32:35]
ds_read_b64_tr_b16 v[76:77], v219 offset:8192
v_mfma_f32_16x16x32_bf16 v[40:43], v[192:195], v[156:159], v[40:43]
ds_read_b64_tr_b16 v[78:79], v219 offset:10240
v_mfma_f32_16x16x32_bf16 v[36:39], v[196:199], v[152:155], v[36:39]
ds_read_b64_tr_b16 v[192:193], v216 offset:12288
s_mov_b32 s99, 972800
buffer_load_dwordx4 v242, s[88:91], s99 offen offset:2048 lds
v_mfma_f32_16x16x32_bf16 v[44:47], v[196:199], v[156:159], v[44:47]
ds_read_b64_tr_b16 v[194:195], v216 offset:14336
v_accvgpr_read_b32 v208, a48
v_mfma_f32_16x16x32_bf16 v[48:51], v[200:203], v[152:155], v[48:51]
ds_read_b64_tr_b16 v[196:197], v217 offset:12288
v_accvgpr_read_b32 v209, a49
v_mfma_f32_16x16x32_bf16 v[56:59], v[200:203], v[156:159], v[56:59]
ds_read_b64_tr_b16 v[198:199], v217 offset:14336
v_accvgpr_read_b32 v210, a50
v_mfma_f32_16x16x32_bf16 v[52:55], v[204:207], v[152:155], v[52:55]
ds_read_b64_tr_b16 v[200:201], v218 offset:12288
v_accvgpr_read_b32 v211, a51
v_mfma_f32_16x16x32_bf16 v[60:63], v[204:207], v[156:159], v[60:63]
ds_read_b64_tr_b16 v[202:203], v218 offset:14336
s_mov_b32 s99, 1000448
buffer_load_dwordx4 v242, s[88:91], s99 offen offset:3072 lds
s_waitcnt lgkmcnt(6)
v_mfma_f32_16x16x32_bf16 v[32:35], v[64:67], v[160:163], v[32:35]
ds_read_b64_tr_b16 v[204:205], v219 offset:12288
v_accvgpr_read_b32 v212, a52
v_mfma_f32_16x16x32_bf16 v[40:43], v[64:67], v[164:167], v[40:43]
ds_read_b64_tr_b16 v[206:207], v219 offset:14336
v_accvgpr_read_b32 v213, a53
v_mfma_f32_16x16x32_bf16 v[36:39], v[68:71], v[160:163], v[36:39]
ds_read_b64_tr_b16 v[64:65], v216 offset:16384
v_accvgpr_read_b32 v214, a54
v_mfma_f32_16x16x32_bf16 v[44:47], v[68:71], v[164:167], v[44:47]
ds_read_b64_tr_b16 v[66:67], v216 offset:18432
v_accvgpr_read_b32 v215, a55
v_mfma_f32_16x16x32_bf16 v[48:51], v[72:75], v[160:163], v[48:51]
ds_read_b64_tr_b16 v[68:69], v217 offset:16384
s_add_u32 m0, m0, 4096
s_mov_b32 s99, 1032192
buffer_load_dwordx4 v242, s[88:91], s99 offen offset:0 lds
s_waitcnt vmcnt(17)
v_mfma_f32_16x16x32_bf16 v[56:59], v[72:75], v[164:167], v[56:59]
ds_read_b64_tr_b16 v[70:71], v217 offset:18432
v_cvt_pk_bf16_f32 v208, v208, v209
v_mfma_f32_16x16x32_bf16 v[52:55], v[76:79], v[160:163], v[52:55]
ds_read_b64_tr_b16 v[72:73], v218 offset:16384
v_cvt_pk_bf16_f32 v209, v210, v211
v_mfma_f32_16x16x32_bf16 v[60:63], v[76:79], v[164:167], v[60:63]
ds_read_b64_tr_b16 v[74:75], v218 offset:18432
ds_read_b128 a[224:227], v221 offset:0
s_waitcnt lgkmcnt(7)
v_mfma_f32_16x16x32_bf16 v[32:35], v[192:195], v[168:171], v[32:35]
ds_read_b64_tr_b16 v[76:77], v219 offset:16384
v_cvt_pk_bf16_f32 v210, v212, v213
v_mfma_f32_16x16x32_bf16 v[40:43], v[192:195], v[172:175], v[40:43]
ds_read_b64_tr_b16 v[78:79], v219 offset:18432
s_mov_b32 s99, 1059840
buffer_load_dwordx4 v242, s[88:91], s99 offen offset:1024 lds
v_mfma_f32_16x16x32_bf16 v[36:39], v[196:199], v[168:171], v[36:39]
ds_read_b64_tr_b16 v[192:193], v216 offset:20480
v_cvt_pk_bf16_f32 v211, v214, v215
v_mfma_f32_16x16x32_bf16 v[44:47], v[196:199], v[172:175], v[44:47]
ds_read_b64_tr_b16 v[194:195], v216 offset:22528
ds_read_b128 a[228:231], v221 offset:1024
v_mfma_f32_16x16x32_bf16 v[48:51], v[200:203], v[168:171], v[48:51]
ds_read_b64_tr_b16 v[196:197], v217 offset:20480
buffer_store_dwordx4 v[208:211], v236, s[36:39], s98 offen offset:2048 sc1
v_mfma_f32_16x16x32_bf16 v[56:59], v[200:203], v[172:175], v[56:59]
ds_read_b64_tr_b16 v[198:199], v217 offset:22528
v_accvgpr_read_b32 v224, a56
v_mfma_f32_16x16x32_bf16 v[52:55], v[204:207], v[168:171], v[52:55]
ds_read_b64_tr_b16 v[200:201], v218 offset:20480
s_mov_b32 s99, 1087488
buffer_load_dwordx4 v242, s[88:91], s99 offen offset:2048 lds
ds_read_b128 a[232:235], v221 offset:2048
v_mfma_f32_16x16x32_bf16 v[60:63], v[204:207], v[172:175], v[60:63]
ds_read_b64_tr_b16 v[202:203], v218 offset:22528
s_waitcnt lgkmcnt(8)
v_mfma_f32_16x16x32_bf16 v[32:35], v[64:67], v[176:179], v[32:35]
ds_read_b64_tr_b16 v[204:205], v219 offset:20480
v_accvgpr_read_b32 v225, a57
v_mfma_f32_16x16x32_bf16 v[40:43], v[64:67], v[180:183], v[40:43]
ds_read_b64_tr_b16 v[206:207], v219 offset:22528
v_add_u32 v216, 0, v232
v_add_u32 v217, 0, v233
v_add_u32 v218, 0, v234
v_add_u32 v219, 0, v235
v_mfma_f32_16x16x32_bf16 v[36:39], v[68:71], v[176:179], v[36:39]
s_waitcnt vmcnt(9)
s_barrier
ds_read_b64_tr_b16 v[64:65], v216 offset:0
ds_read_b128 a[236:239], v221 offset:3072
v_mfma_f32_16x16x32_bf16 v[44:47], v[68:71], v[180:183], v[44:47]
ds_read_b64_tr_b16 v[66:67], v216 offset:2048
s_mov_b32 s99, 1115136
buffer_load_dwordx4 v242, s[88:91], s99 offen offset:3072 lds
v_mfma_f32_16x16x32_bf16 v[48:51], v[72:75], v[176:179], v[48:51]
ds_read_b64_tr_b16 v[68:69], v217 offset:0
v_accvgpr_read_b32 v226, a58
v_mfma_f32_16x16x32_bf16 v[56:59], v[72:75], v[180:183], v[56:59]
ds_read_b64_tr_b16 v[70:71], v217 offset:2048
v_accvgpr_read_b32 v227, a59
v_mfma_f32_16x16x32_bf16 v[52:55], v[76:79], v[176:179], v[52:55]
ds_read_b64_tr_b16 v[72:73], v218 offset:0
v_accvgpr_read_b32 v228, a60
v_mfma_f32_16x16x32_bf16 v[60:63], v[76:79], v[180:183], v[60:63]
ds_read_b64_tr_b16 v[74:75], v218 offset:2048
s_waitcnt lgkmcnt(7)
v_mfma_f32_16x16x32_bf16 v[32:35], v[192:195], v[184:187], v[32:35]
ds_read_b64_tr_b16 v[76:77], v219 offset:0
v_accvgpr_read_b32 v229, a61
v_mfma_f32_16x16x32_bf16 v[40:43], v[192:195], v[188:191], v[40:43]
ds_read_b64_tr_b16 v[78:79], v219 offset:2048
v_accvgpr_read_b32 v230, a62
v_mfma_f32_16x16x32_bf16 v[36:39], v[196:199], v[184:187], v[36:39]
ds_read_b64_tr_b16 v[192:193], v216 offset:4096
v_accvgpr_read_b32 v231, a63
v_mfma_f32_16x16x32_bf16 v[44:47], v[196:199], v[188:191], v[44:47]
ds_read_b64_tr_b16 v[194:195], v216 offset:6144
v_cvt_pk_bf16_f32 v224, v224, v225
v_mfma_f32_16x16x32_bf16 v[48:51], v[200:203], v[184:187], v[48:51]
ds_read_b64_tr_b16 v[196:197], v217 offset:4096
v_cvt_pk_bf16_f32 v225, v226, v227
v_mfma_f32_16x16x32_bf16 v[56:59], v[200:203], v[188:191], v[56:59]
ds_read_b64_tr_b16 v[198:199], v217 offset:6144
v_cvt_pk_bf16_f32 v226, v228, v229
v_mfma_f32_16x16x32_bf16 v[52:55], v[204:207], v[184:187], v[52:55]
ds_read_b64_tr_b16 v[200:201], v218 offset:4096
v_cvt_pk_bf16_f32 v227, v230, v231
v_mfma_f32_16x16x32_bf16 v[60:63], v[204:207], v[188:191], v[60:63]
ds_read_b64_tr_b16 v[202:203], v218 offset:6144
buffer_store_dwordx4 v[224:227], v236, s[36:39], s98 offen offset:3072 sc1
s_waitcnt lgkmcnt(6)
v_mfma_f32_16x16x32_bf16 v[80:83], v[64:67], a[128:131], 0
ds_read_b64_tr_b16 v[204:205], v219 offset:4096
s_add_u32 m0, s68, 32768
s_mov_b32 s99, 128
buffer_load_dwordx4 v242, s[88:91], s99 offen offset:0 lds
v_mfma_f32_16x16x32_bf16 v[88:91], v[64:67], a[132:135], 0
ds_read_b64_tr_b16 v[206:207], v219 offset:6144
v_mfma_f32_16x16x32_bf16 v[84:87], v[68:71], a[128:131], 0
ds_read_b64_tr_b16 v[64:65], v216 offset:8192
v_mfma_f32_16x16x32_bf16 v[92:95], v[68:71], a[132:135], 0
ds_read_b64_tr_b16 v[66:67], v216 offset:10240
v_mfma_f32_16x16x32_bf16 v[96:99], v[72:75], a[128:131], 0
ds_read_b64_tr_b16 v[68:69], v217 offset:8192
s_add_u32 s96, s70, 12288
v_mul_f32 v32, 0x3db8aa3b, v32
v_mfma_f32_16x16x32_bf16 v[104:107], v[72:75], a[132:135], 0
ds_read_b64_tr_b16 v[70:71], v217 offset:10240
v_mul_f32 v33, 0x3db8aa3b, v33
v_mul_f32 v34, 0x3db8aa3b, v34
v_mfma_f32_16x16x32_bf16 v[100:103], v[76:79], a[128:131], 0
ds_read_b64_tr_b16 v[72:73], v218 offset:8192
v_mul_f32 v35, 0x3db8aa3b, v35
v_mul_f32 v36, 0x3db8aa3b, v36
v_mfma_f32_16x16x32_bf16 v[108:111], v[76:79], a[132:135], 0
ds_read_b64_tr_b16 v[74:75], v218 offset:10240
s_mov_b32 s99, 27776
buffer_load_dwordx4 v242, s[88:91], s99 offen offset:1024 lds
s_waitcnt lgkmcnt(6)
v_mfma_f32_16x16x32_bf16 v[80:83], v[192:195], a[136:139], v[80:83]
ds_read_b64_tr_b16 v[76:77], v219 offset:8192
v_mul_f32 v37, 0x3db8aa3b, v37
v_mfma_f32_16x16x32_bf16 v[88:91], v[192:195], a[140:143], v[88:91]
ds_read_b64_tr_b16 v[78:79], v219 offset:10240
v_mul_f32 v38, 0x3db8aa3b, v38
v_mfma_f32_16x16x32_bf16 v[84:87], v[196:199], a[136:139], v[84:87]
ds_read_b64_tr_b16 v[192:193], v216 offset:12288
v_mul_f32 v39, 0x3db8aa3b, v39
v_mfma_f32_16x16x32_bf16 v[92:95], v[196:199], a[140:143], v[92:95]
ds_read_b64_tr_b16 v[194:195], v216 offset:14336
v_cvt_pk_bf16_f32 v32, v32, v33
v_mfma_f32_16x16x32_bf16 v[96:99], v[200:203], a[136:139], v[96:99]
ds_read_b64_tr_b16 v[196:197], v217 offset:12288
v_cvt_pk_bf16_f32 v33, v34, v35
v_mfma_f32_16x16x32_bf16 v[104:107], v[200:203], a[140:143], v[104:107]
ds_read_b64_tr_b16 v[198:199], v217 offset:14336
v_cvt_pk_bf16_f32 v34, v36, v37
v_mfma_f32_16x16x32_bf16 v[100:103], v[204:207], a[136:139], v[100:103]
ds_read_b64_tr_b16 v[200:201], v218 offset:12288
s_mov_b32 s99, 55424
buffer_load_dwordx4 v242, s[88:91], s99 offen offset:2048 lds
v_mfma_f32_16x16x32_bf16 v[108:111], v[204:207], a[140:143], v[108:111]
ds_read_b64_tr_b16 v[202:203], v218 offset:14336
v_cvt_pk_bf16_f32 v35, v38, v39
s_waitcnt lgkmcnt(6)
)ASM"
R"ASM(
v_mfma_f32_16x16x32_bf16 v[80:83], v[64:67], a[144:147], v[80:83]
ds_read_b64_tr_b16 v[204:205], v219 offset:12288
v_accvgpr_write_b32 a48, v32
v_accvgpr_write_b32 a49, v33
v_mfma_f32_16x16x32_bf16 v[88:91], v[64:67], a[148:151], v[88:91]
ds_read_b64_tr_b16 v[206:207], v219 offset:14336
v_accvgpr_write_b32 a50, v34
v_accvgpr_write_b32 a51, v35
v_mfma_f32_16x16x32_bf16 v[84:87], v[68:71], a[144:147], v[84:87]
ds_read_b64_tr_b16 v[64:65], v216 offset:16384
buffer_store_dwordx4 v[32:35], v236, s[60:63], s96 offen offset:0 sc1
v_mfma_f32_16x16x32_bf16 v[92:95], v[68:71], a[148:151], v[92:95]
ds_read_b64_tr_b16 v[66:67], v216 offset:18432
v_mul_f32 v40, 0x3db8aa3b, v40
v_mfma_f32_16x16x32_bf16 v[96:99], v[72:75], a[144:147], v[96:99]
ds_read_b64_tr_b16 v[68:69], v217 offset:16384
v_mul_f32 v41, 0x3db8aa3b, v41
v_mul_f32 v42, 0x3db8aa3b, v42
v_mfma_f32_16x16x32_bf16 v[104:107], v[72:75], a[148:151], v[104:107]
ds_read_b64_tr_b16 v[70:71], v217 offset:18432
s_mov_b32 s99, 83072
buffer_load_dwordx4 v242, s[88:91], s99 offen offset:3072 lds
v_mfma_f32_16x16x32_bf16 v[100:103], v[76:79], a[144:147], v[100:103]
ds_read_b64_tr_b16 v[72:73], v218 offset:16384
v_mul_f32 v43, 0x3db8aa3b, v43
v_mul_f32 v44, 0x3db8aa3b, v44
v_mfma_f32_16x16x32_bf16 v[108:111], v[76:79], a[148:151], v[108:111]
ds_read_b64_tr_b16 v[74:75], v218 offset:18432
v_mul_f32 v45, 0x3db8aa3b, v45
s_waitcnt lgkmcnt(6)
v_mfma_f32_16x16x32_bf16 v[80:83], v[192:195], a[152:155], v[80:83]
ds_read_b64_tr_b16 v[76:77], v219 offset:16384
v_mul_f32 v46, 0x3db8aa3b, v46
v_mul_f32 v47, 0x3db8aa3b, v47
v_mfma_f32_16x16x32_bf16 v[88:91], v[192:195], a[156:159], v[88:91]
ds_read_b64_tr_b16 v[78:79], v219 offset:18432
v_cvt_pk_bf16_f32 v40, v40, v41
v_cvt_pk_bf16_f32 v41, v42, v43
v_mfma_f32_16x16x32_bf16 v[84:87], v[196:199], a[152:155], v[84:87]
ds_read_b64_tr_b16 v[192:193], v216 offset:20480
v_cvt_pk_bf16_f32 v42, v44, v45
v_cvt_pk_bf16_f32 v43, v46, v47
v_mfma_f32_16x16x32_bf16 v[92:95], v[196:199], a[156:159], v[92:95]
ds_read_b64_tr_b16 v[194:195], v216 offset:22528
v_accvgpr_write_b32 a52, v40
v_accvgpr_write_b32 a53, v41
v_mfma_f32_16x16x32_bf16 v[96:99], v[200:203], a[152:155], v[96:99]
ds_read_b64_tr_b16 v[196:197], v217 offset:20480
s_add_u32 m0, m0, 4096
s_mov_b32 s99, 114816
buffer_load_dwordx4 v242, s[88:91], s99 offen offset:0 lds
v_mfma_f32_16x16x32_bf16 v[104:107], v[200:203], a[156:159], v[104:107]
ds_read_b64_tr_b16 v[198:199], v217 offset:22528
v_accvgpr_write_b32 a54, v42
v_accvgpr_write_b32 a55, v43
v_mfma_f32_16x16x32_bf16 v[100:103], v[204:207], a[152:155], v[100:103]
ds_read_b64_tr_b16 v[200:201], v218 offset:20480
buffer_store_dwordx4 v[40:43], v236, s[60:63], s96 offen offset:1024 sc1
v_mfma_f32_16x16x32_bf16 v[108:111], v[204:207], a[156:159], v[108:111]
ds_read_b64_tr_b16 v[202:203], v218 offset:22528
v_mul_f32 v48, 0x3db8aa3b, v48
s_waitcnt lgkmcnt(6)
v_mfma_f32_16x16x32_bf16 v[80:83], v[64:67], a[160:163], v[80:83]
ds_read_b64_tr_b16 v[204:205], v219 offset:20480
v_mul_f32 v49, 0x3db8aa3b, v49
v_mul_f32 v50, 0x3db8aa3b, v50
v_mfma_f32_16x16x32_bf16 v[88:91], v[64:67], a[164:167], v[88:91]
ds_read_b64_tr_b16 v[206:207], v219 offset:22528
v_mul_f32 v51, 0x3db8aa3b, v51
v_mul_f32 v52, 0x3db8aa3b, v52
v_mfma_f32_16x16x32_bf16 v[84:87], v[68:71], a[160:163], v[84:87]
ds_read_b64_tr_b16 v[64:65], v216 offset:24576
v_mul_f32 v53, 0x3db8aa3b, v53
v_mul_f32 v54, 0x3db8aa3b, v54
v_mfma_f32_16x16x32_bf16 v[92:95], v[68:71], a[164:167], v[92:95]
ds_read_b64_tr_b16 v[66:67], v216 offset:26624
s_mov_b32 s99, 142464
buffer_load_dwordx4 v242, s[88:91], s99 offen offset:1024 lds
v_mfma_f32_16x16x32_bf16 v[96:99], v[72:75], a[160:163], v[96:99]
ds_read_b64_tr_b16 v[68:69], v217 offset:24576
v_mul_f32 v55, 0x3db8aa3b, v55
v_cvt_pk_bf16_f32 v48, v48, v49
v_mfma_f32_16x16x32_bf16 v[104:107], v[72:75], a[164:167], v[104:107]
ds_read_b64_tr_b16 v[70:71], v217 offset:26624
v_cvt_pk_bf16_f32 v49, v50, v51
v_cvt_pk_bf16_f32 v50, v52, v53
v_mfma_f32_16x16x32_bf16 v[100:103], v[76:79], a[160:163], v[100:103]
ds_read_b64_tr_b16 v[72:73], v218 offset:24576
v_cvt_pk_bf16_f32 v51, v54, v55
v_accvgpr_write_b32 a56, v48
v_mfma_f32_16x16x32_bf16 v[108:111], v[76:79], a[164:167], v[108:111]
ds_read_b64_tr_b16 v[74:75], v218 offset:26624
v_accvgpr_write_b32 a57, v49
s_waitcnt lgkmcnt(6)
v_mfma_f32_16x16x32_bf16 v[80:83], v[192:195], a[168:171], v[80:83]
ds_read_b64_tr_b16 v[76:77], v219 offset:24576
v_accvgpr_write_b32 a58, v50
v_accvgpr_write_b32 a59, v51
v_mfma_f32_16x16x32_bf16 v[88:91], v[192:195], a[172:175], v[88:91]
ds_read_b64_tr_b16 v[78:79], v219 offset:26624
buffer_store_dwordx4 v[48:51], v236, s[60:63], s96 offen offset:2048 sc1
v_mfma_f32_16x16x32_bf16 v[84:87], v[196:199], a[168:171], v[84:87]
ds_read_b64_tr_b16 v[192:193], v216 offset:28672
s_mov_b32 s99, 170112
buffer_load_dwordx4 v242, s[88:91], s99 offen offset:2048 lds
v_mfma_f32_16x16x32_bf16 v[92:95], v[196:199], a[172:175], v[92:95]
ds_read_b64_tr_b16 v[194:195], v216 offset:30720
v_mul_f32 v56, 0x3db8aa3b, v56
v_mfma_f32_16x16x32_bf16 v[96:99], v[200:203], a[168:171], v[96:99]
ds_read_b64_tr_b16 v[196:197], v217 offset:28672
v_mul_f32 v57, 0x3db8aa3b, v57
v_mul_f32 v58, 0x3db8aa3b, v58
v_mfma_f32_16x16x32_bf16 v[104:107], v[200:203], a[172:175], v[104:107]
ds_read_b64_tr_b16 v[198:199], v217 offset:30720
v_mul_f32 v59, 0x3db8aa3b, v59
v_mul_f32 v60, 0x3db8aa3b, v60
v_mfma_f32_16x16x32_bf16 v[100:103], v[204:207], a[168:171], v[100:103]
ds_read_b64_tr_b16 v[200:201], v218 offset:28672
v_mul_f32 v61, 0x3db8aa3b, v61
v_mul_f32 v62, 0x3db8aa3b, v62
v_mfma_f32_16x16x32_bf16 v[108:111], v[204:207], a[172:175], v[108:111]
ds_read_b64_tr_b16 v[202:203], v218 offset:30720
v_mul_f32 v63, 0x3db8aa3b, v63
s_waitcnt lgkmcnt(6)
v_mfma_f32_16x16x32_bf16 v[80:83], v[64:67], a[176:179], v[80:83]
ds_read_b64_tr_b16 v[204:205], v219 offset:28672
v_cvt_pk_bf16_f32 v56, v56, v57
v_cvt_pk_bf16_f32 v57, v58, v59
v_mfma_f32_16x16x32_bf16 v[88:91], v[64:67], a[180:183], v[88:91]
ds_read_b64_tr_b16 v[206:207], v219 offset:30720
v_add_u32 v216, 65536, v232
v_add_u32 v217, 65536, v233
v_add_u32 v218, 65536, v234
v_add_u32 v219, 65536, v235
s_mov_b32 s99, 197760
buffer_load_dwordx4 v242, s[88:91], s99 offen offset:3072 lds
v_mfma_f32_16x16x32_bf16 v[84:87], v[68:71], a[176:179], v[84:87]
s_waitcnt vmcnt(12)
s_barrier
ds_read_b64_tr_b16 v[64:65], v216 offset:0
v_mfma_f32_16x16x32_bf16 v[92:95], v[68:71], a[180:183], v[92:95]
ds_read_b64_tr_b16 v[66:67], v216 offset:2048
v_cvt_pk_bf16_f32 v58, v60, v61
v_cvt_pk_bf16_f32 v59, v62, v63
v_mfma_f32_16x16x32_bf16 v[96:99], v[72:75], a[176:179], v[96:99]
ds_read_b64_tr_b16 v[68:69], v217 offset:0
v_accvgpr_write_b32 a60, v56
v_accvgpr_write_b32 a61, v57
v_mfma_f32_16x16x32_bf16 v[104:107], v[72:75], a[180:183], v[104:107]
ds_read_b64_tr_b16 v[70:71], v217 offset:2048
v_accvgpr_write_b32 a62, v58
v_accvgpr_write_b32 a63, v59
v_mfma_f32_16x16x32_bf16 v[100:103], v[76:79], a[176:179], v[100:103]
ds_read_b64_tr_b16 v[72:73], v218 offset:0
buffer_store_dwordx4 v[56:59], v236, s[60:63], s96 offen offset:3072 sc1
v_mfma_f32_16x16x32_bf16 v[108:111], v[76:79], a[180:183], v[108:111]
ds_read_b64_tr_b16 v[74:75], v218 offset:2048
s_waitcnt lgkmcnt(6)
v_mfma_f32_16x16x32_bf16 v[80:83], v[192:195], a[184:187], v[80:83]
ds_read_b64_tr_b16 v[76:77], v219 offset:0
s_add_u32 s98, s76, 16384
v_mfma_f32_16x16x32_bf16 v[88:91], v[192:195], a[188:191], v[88:91]
ds_read_b64_tr_b16 v[78:79], v219 offset:2048
v_accvgpr_read_b32 v208, a64
v_accvgpr_read_b32 v209, a65
v_mfma_f32_16x16x32_bf16 v[84:87], v[196:199], a[184:187], v[84:87]
ds_read_b64_tr_b16 v[192:193], v216 offset:4096
v_accvgpr_read_b32 v210, a66
v_accvgpr_read_b32 v211, a67
v_mfma_f32_16x16x32_bf16 v[92:95], v[196:199], a[188:191], v[92:95]
ds_read_b64_tr_b16 v[194:195], v216 offset:6144
v_accvgpr_read_b32 v212, a68
v_accvgpr_read_b32 v213, a69
v_mfma_f32_16x16x32_bf16 v[96:99], v[200:203], a[184:187], v[96:99]
ds_read_b64_tr_b16 v[196:197], v217 offset:4096
v_accvgpr_read_b32 v214, a70
v_accvgpr_read_b32 v215, a71
v_mfma_f32_16x16x32_bf16 v[104:107], v[200:203], a[188:191], v[104:107]
ds_read_b64_tr_b16 v[198:199], v217 offset:6144
v_cvt_pk_bf16_f32 v208, v208, v209
v_cvt_pk_bf16_f32 v209, v210, v211
v_mfma_f32_16x16x32_bf16 v[100:103], v[204:207], a[184:187], v[100:103]
ds_read_b64_tr_b16 v[200:201], v218 offset:4096
v_cvt_pk_bf16_f32 v210, v212, v213
v_cvt_pk_bf16_f32 v211, v214, v215
v_mfma_f32_16x16x32_bf16 v[108:111], v[204:207], a[188:191], v[108:111]
ds_read_b64_tr_b16 v[202:203], v218 offset:6144
buffer_store_dwordx4 v[208:211], v236, s[36:39], s98 offen offset:0 sc1
s_waitcnt lgkmcnt(6)
v_mfma_f32_16x16x32_bf16 v[80:83], v[64:67], a[192:195], v[80:83]
ds_read_b64_tr_b16 v[204:205], v219 offset:4096
s_add_u32 m0, s68, 98304
s_mov_b32 s99, 917632
buffer_load_dwordx4 v242, s[88:91], s99 offen offset:0 lds
v_mfma_f32_16x16x32_bf16 v[88:91], v[64:67], a[196:199], v[88:91]
ds_read_b64_tr_b16 v[206:207], v219 offset:6144
s_mov_b32 s99, 945280
buffer_load_dwordx4 v242, s[88:91], s99 offen offset:1024 lds
v_mfma_f32_16x16x32_bf16 v[84:87], v[68:71], a[192:195], v[84:87]
ds_read_b64_tr_b16 v[64:65], v216 offset:8192
)ASM"
R"ASM(
v_mfma_f32_16x16x32_bf16 v[92:95], v[68:71], a[196:199], v[92:95]
ds_read_b64_tr_b16 v[66:67], v216 offset:10240
s_mov_b32 s99, 972928
buffer_load_dwordx4 v242, s[88:91], s99 offen offset:2048 lds
v_mfma_f32_16x16x32_bf16 v[96:99], v[72:75], a[192:195], v[96:99]
ds_read_b64_tr_b16 v[68:69], v217 offset:8192
v_accvgpr_read_b32 v224, a72
v_mfma_f32_16x16x32_bf16 v[104:107], v[72:75], a[196:199], v[104:107]
ds_read_b64_tr_b16 v[70:71], v217 offset:10240
s_mov_b32 s99, 1000576
buffer_load_dwordx4 v242, s[88:91], s99 offen offset:3072 lds
v_mfma_f32_16x16x32_bf16 v[100:103], v[76:79], a[192:195], v[100:103]
ds_read_b64_tr_b16 v[72:73], v218 offset:8192
s_add_u32 m0, m0, 4096
s_mov_b32 s99, 1032320
buffer_load_dwordx4 v242, s[88:91], s99 offen offset:0 lds
v_mfma_f32_16x16x32_bf16 v[108:111], v[76:79], a[196:199], v[108:111]
ds_read_b64_tr_b16 v[74:75], v218 offset:10240
s_waitcnt lgkmcnt(6)
v_mfma_f32_16x16x32_bf16 v[80:83], v[192:195], a[200:203], v[80:83]
ds_read_b64_tr_b16 v[76:77], v219 offset:8192
s_mov_b32 s99, 1059968
buffer_load_dwordx4 v242, s[88:91], s99 offen offset:1024 lds
v_mfma_f32_16x16x32_bf16 v[88:91], v[192:195], a[204:207], v[88:91]
ds_read_b64_tr_b16 v[78:79], v219 offset:10240
v_accvgpr_read_b32 v225, a73
v_mfma_f32_16x16x32_bf16 v[84:87], v[196:199], a[200:203], v[84:87]
ds_read_b64_tr_b16 v[192:193], v216 offset:12288
s_mov_b32 s99, 1087616
buffer_load_dwordx4 v242, s[88:91], s99 offen offset:2048 lds
v_mfma_f32_16x16x32_bf16 v[92:95], v[196:199], a[204:207], v[92:95]
ds_read_b64_tr_b16 v[194:195], v216 offset:14336
v_accvgpr_read_b32 v226, a74
v_mfma_f32_16x16x32_bf16 v[96:99], v[200:203], a[200:203], v[96:99]
ds_read_b64_tr_b16 v[196:197], v217 offset:12288
s_mov_b32 s99, 1115264
buffer_load_dwordx4 v242, s[88:91], s99 offen offset:3072 lds
v_mfma_f32_16x16x32_bf16 v[104:107], v[200:203], a[204:207], v[104:107]
ds_read_b64_tr_b16 v[198:199], v217 offset:14336
v_cmp_lt_u32 vcc, 511, v236
v_mfma_f32_16x16x32_bf16 v[100:103], v[204:207], a[200:203], v[100:103]
ds_read_b64_tr_b16 v[200:201], v218 offset:12288
v_accvgpr_read_b32 v227, a75
v_mfma_f32_16x16x32_bf16 v[108:111], v[204:207], a[204:207], v[108:111]
ds_read_b64_tr_b16 v[202:203], v218 offset:14336
v_mov_b32 v222, 0xc0000000
s_waitcnt lgkmcnt(6)
v_mfma_f32_16x16x32_bf16 v[80:83], v[64:67], a[208:211], v[80:83]
ds_read_b64_tr_b16 v[204:205], v219 offset:12288
v_accvgpr_read_b32 v228, a76
v_mfma_f32_16x16x32_bf16 v[88:91], v[64:67], a[212:215], v[88:91]
ds_read_b64_tr_b16 v[206:207], v219 offset:14336
s_nop 0
v_mfma_f32_16x16x32_bf16 v[84:87], v[68:71], a[208:211], v[84:87]
ds_read_b64_tr_b16 v[64:65], v216 offset:16384
v_accvgpr_read_b32 v229, a77
v_mfma_f32_16x16x32_bf16 v[92:95], v[68:71], a[212:215], v[92:95]
ds_read_b64_tr_b16 v[66:67], v216 offset:18432
v_cndmask_b32 v222, v248, v222, vcc
v_mfma_f32_16x16x32_bf16 v[96:99], v[72:75], a[208:211], v[96:99]
ds_read_b64_tr_b16 v[68:69], v217 offset:16384
s_mov_b32 s99, 0
buffer_load_dwordx2 v[144:145], v248, s[36:39], s99 offen offset:0 sc1
buffer_load_dwordx2 v[152:153], v248, s[36:39], s99 offen offset:128 sc1
v_mfma_f32_16x16x32_bf16 v[104:107], v[72:75], a[212:215], v[104:107]
ds_read_b64_tr_b16 v[70:71], v217 offset:18432
v_accvgpr_read_b32 v230, a78
v_mfma_f32_16x16x32_bf16 v[100:103], v[76:79], a[208:211], v[100:103]
ds_read_b64_tr_b16 v[72:73], v218 offset:16384
s_mov_b32 s99, 65536
buffer_load_dwordx2 v[146:147], v248, s[36:39], s99 offen offset:0 sc1
buffer_load_dwordx2 v[154:155], v248, s[36:39], s99 offen offset:128 sc1
v_mfma_f32_16x16x32_bf16 v[108:111], v[76:79], a[212:215], v[108:111]
ds_read_b64_tr_b16 v[74:75], v218 offset:18432
s_waitcnt lgkmcnt(6)
v_mfma_f32_16x16x32_bf16 v[80:83], v[192:195], a[216:219], v[80:83]
ds_read_b64_tr_b16 v[76:77], v219 offset:16384
s_mov_b32 s99, 131072
buffer_load_dwordx2 v[148:149], v248, s[36:39], s99 offen offset:0 sc1
buffer_load_dwordx2 v[156:157], v248, s[36:39], s99 offen offset:128 sc1
v_mfma_f32_16x16x32_bf16 v[88:91], v[192:195], a[220:223], v[88:91]
ds_read_b64_tr_b16 v[78:79], v219 offset:18432
v_accvgpr_read_b32 v231, a79
v_mfma_f32_16x16x32_bf16 v[84:87], v[196:199], a[216:219], v[84:87]
ds_read_b64_tr_b16 v[192:193], v216 offset:20480
s_mov_b32 s99, 196608
buffer_load_dwordx2 v[150:151], v222, s[36:39], s99 offen offset:0 sc1
buffer_load_dwordx2 v[158:159], v222, s[36:39], s99 offen offset:128 sc1
v_mfma_f32_16x16x32_bf16 v[92:95], v[196:199], a[220:223], v[92:95]
ds_read_b64_tr_b16 v[194:195], v216 offset:22528
buffer_load_dwordx4 v[160:163], v249, s[36:39], 0 offen offset:0 sc1
v_mfma_f32_16x16x32_bf16 v[96:99], v[200:203], a[216:219], v[96:99]
ds_read_b64_tr_b16 v[196:197], v217 offset:20480
v_cvt_pk_bf16_f32 v224, v224, v225
v_mfma_f32_16x16x32_bf16 v[104:107], v[200:203], a[220:223], v[104:107]
ds_read_b64_tr_b16 v[198:199], v217 offset:22528
buffer_load_dwordx4 v[164:167], v249, s[36:39], 0 offen offset:64 sc1
v_mfma_f32_16x16x32_bf16 v[100:103], v[204:207], a[216:219], v[100:103]
ds_read_b64_tr_b16 v[200:201], v218 offset:20480
v_cvt_pk_bf16_f32 v225, v226, v227
v_mfma_f32_16x16x32_bf16 v[108:111], v[204:207], a[220:223], v[108:111]
ds_read_b64_tr_b16 v[202:203], v218 offset:22528
buffer_load_dwordx4 v[168:171], v249, s[36:39], 0 offen offset:128 sc1
s_waitcnt lgkmcnt(6)
v_mfma_f32_16x16x32_bf16 v[80:83], v[64:67], a[224:227], v[80:83]
ds_read_b64_tr_b16 v[204:205], v219 offset:20480
v_cvt_pk_bf16_f32 v226, v228, v229
v_cvt_pk_bf16_f32 v227, v230, v231
v_mfma_f32_16x16x32_bf16 v[88:91], v[64:67], a[228:231], v[88:91]
ds_read_b64_tr_b16 v[206:207], v219 offset:22528
v_add_u32 v216, 32768, v232
v_add_u32 v217, 32768, v233
v_add_u32 v218, 32768, v234
v_add_u32 v219, 32768, v235
buffer_load_dwordx4 v[172:175], v249, s[36:39], 0 offen offset:192 sc1
v_mfma_f32_16x16x32_bf16 v[84:87], v[68:71], a[224:227], v[84:87]
s_waitcnt vmcnt(22)
s_barrier
ds_read_b64_tr_b16 v[64:65], v216 offset:0
buffer_load_dwordx4 v[176:179], v249, s[36:39], 0 offen offset:256 sc1
v_mfma_f32_16x16x32_bf16 v[92:95], v[68:71], a[228:231], v[92:95]
ds_read_b64_tr_b16 v[66:67], v216 offset:2048
buffer_store_dwordx4 v[224:227], v236, s[36:39], s98 offen offset:1024 sc1
v_mfma_f32_16x16x32_bf16 v[96:99], v[72:75], a[224:227], v[96:99]
ds_read_b64_tr_b16 v[68:69], v217 offset:0
buffer_load_dwordx4 v[180:183], v249, s[36:39], 0 offen offset:320 sc1
v_mfma_f32_16x16x32_bf16 v[104:107], v[72:75], a[228:231], v[104:107]
ds_read_b64_tr_b16 v[70:71], v217 offset:2048
v_accvgpr_read_b32 v208, a80
v_mfma_f32_16x16x32_bf16 v[100:103], v[76:79], a[224:227], v[100:103]
ds_read_b64_tr_b16 v[72:73], v218 offset:0
buffer_load_dwordx4 v[184:187], v249, s[36:39], 0 offen offset:384 sc1
v_mfma_f32_16x16x32_bf16 v[108:111], v[76:79], a[228:231], v[108:111]
ds_read_b64_tr_b16 v[74:75], v218 offset:2048
v_accvgpr_read_b32 v209, a81
s_waitcnt lgkmcnt(6)
v_mfma_f32_16x16x32_bf16 v[80:83], v[192:195], a[232:235], v[80:83]
ds_read_b64_tr_b16 v[76:77], v216 offset:4096
v_accvgpr_read_b32 v210, a82
v_accvgpr_read_b32 v211, a83
v_mfma_f32_16x16x32_bf16 v[88:91], v[192:195], a[236:239], v[88:91]
ds_read_b64_tr_b16 v[78:79], v216 offset:6144
v_accvgpr_read_b32 v212, a84
v_accvgpr_read_b32 v213, a85
v_mfma_f32_16x16x32_bf16 v[84:87], v[196:199], a[232:235], v[84:87]
ds_read_b64_tr_b16 v[192:193], v217 offset:4096
v_accvgpr_read_b32 v214, a86
v_accvgpr_read_b32 v215, a87
v_mfma_f32_16x16x32_bf16 v[92:95], v[196:199], a[236:239], v[92:95]
ds_read_b64_tr_b16 v[194:195], v217 offset:6144
v_cvt_pk_bf16_f32 v208, v208, v209
v_mfma_f32_16x16x32_bf16 v[96:99], v[200:203], a[232:235], v[96:99]
ds_read_b64_tr_b16 v[196:197], v218 offset:4096
v_cvt_pk_bf16_f32 v209, v210, v211
v_mfma_f32_16x16x32_bf16 v[104:107], v[200:203], a[236:239], v[104:107]
ds_read_b64_tr_b16 v[198:199], v218 offset:6144
v_cvt_pk_bf16_f32 v210, v212, v213
v_mfma_f32_16x16x32_bf16 v[100:103], v[204:207], a[232:235], v[100:103]
ds_read_b64_tr_b16 v[200:201], v216 offset:8192
v_cvt_pk_bf16_f32 v211, v214, v215
v_mfma_f32_16x16x32_bf16 v[108:111], v[204:207], a[236:239], v[108:111]
ds_read_b64_tr_b16 v[202:203], v216 offset:10240
buffer_store_dwordx4 v[208:211], v236, s[36:39], s98 offen offset:2048 sc1
s_waitcnt lgkmcnt(6)
v_mfma_f32_16x16x32_bf16 v[112:115], v[64:67], a[128:131], 0
ds_read_b64_tr_b16 v[204:205], v217 offset:8192
s_waitcnt vmcnt(26)
v_mov_b32 v239, s29
s_mov_b64 exec, 1
buffer_store_dword v239, off, s[36:39], s69 offset:128 sc1
s_mov_b64 exec, -1
v_mfma_f32_16x16x32_bf16 v[120:123], v[64:67], a[132:135], 0
ds_read_b64_tr_b16 v[206:207], v217 offset:10240
v_mfma_f32_16x16x32_bf16 v[116:119], v[68:71], a[128:131], 0
ds_read_b64_tr_b16 v[64:65], v218 offset:8192
v_mfma_f32_16x16x32_bf16 v[124:127], v[68:71], a[132:135], 0
ds_read_b64_tr_b16 v[66:67], v218 offset:10240
v_mfma_f32_16x16x32_bf16 v[128:131], v[72:75], a[128:131], 0
ds_read_b64_tr_b16 v[68:69], v216 offset:12288
v_accvgpr_read_b32 v224, a88
v_mfma_f32_16x16x32_bf16 v[132:135], v[72:75], a[132:135], 0
ds_read_b64_tr_b16 v[70:71], v216 offset:14336
v_accvgpr_read_b32 v225, a89
v_accvgpr_read_b32 v226, a90
v_mfma_f32_16x16x32_bf16 v[112:115], v[76:79], a[136:139], v[112:115]
ds_read_b64_tr_b16 v[72:73], v217 offset:12288
v_accvgpr_read_b32 v227, a91
v_accvgpr_read_b32 v228, a92
v_mfma_f32_16x16x32_bf16 v[120:123], v[76:79], a[140:143], v[120:123]
ds_read_b64_tr_b16 v[74:75], v217 offset:14336
v_accvgpr_read_b32 v229, a93
s_waitcnt lgkmcnt(6)
v_mfma_f32_16x16x32_bf16 v[116:119], v[192:195], a[136:139], v[116:119]
ds_read_b64_tr_b16 v[76:77], v218 offset:12288
v_accvgpr_read_b32 v230, a94
v_accvgpr_read_b32 v231, a95
v_mfma_f32_16x16x32_bf16 v[124:127], v[192:195], a[140:143], v[124:127]
ds_read_b64_tr_b16 v[78:79], v218 offset:14336
v_cvt_pk_bf16_f32 v224, v224, v225
v_mfma_f32_16x16x32_bf16 v[128:131], v[196:199], a[136:139], v[128:131]
)ASM"
R"ASM(
ds_read_b64_tr_b16 v[192:193], v216 offset:16384
v_cvt_pk_bf16_f32 v225, v226, v227
v_mfma_f32_16x16x32_bf16 v[132:135], v[196:199], a[140:143], v[132:135]
ds_read_b64_tr_b16 v[194:195], v216 offset:18432
v_cvt_pk_bf16_f32 v226, v228, v229
v_mfma_f32_16x16x32_bf16 v[112:115], v[200:203], a[144:147], v[112:115]
ds_read_b64_tr_b16 v[196:197], v217 offset:16384
v_cvt_pk_bf16_f32 v227, v230, v231
v_mfma_f32_16x16x32_bf16 v[120:123], v[200:203], a[148:151], v[120:123]
ds_read_b64_tr_b16 v[198:199], v217 offset:18432
buffer_store_dwordx4 v[224:227], v236, s[36:39], s98 offen offset:3072 sc1
v_mfma_f32_16x16x32_bf16 v[116:119], v[204:207], a[144:147], v[116:119]
ds_read_b64_tr_b16 v[200:201], v218 offset:16384
s_add_u32 s98, s76, 16384
s_add_u32 s98, s98, s77
v_mfma_f32_16x16x32_bf16 v[124:127], v[204:207], a[148:151], v[124:127]
ds_read_b64_tr_b16 v[202:203], v218 offset:18432
s_waitcnt lgkmcnt(6)
v_mfma_f32_16x16x32_bf16 v[128:131], v[64:67], a[144:147], v[128:131]
ds_read_b64_tr_b16 v[204:205], v216 offset:20480
v_accvgpr_read_b32 v208, a96
v_mfma_f32_16x16x32_bf16 v[132:135], v[64:67], a[148:151], v[132:135]
ds_read_b64_tr_b16 v[206:207], v216 offset:22528
v_accvgpr_read_b32 v209, a97
v_mfma_f32_16x16x32_bf16 v[112:115], v[68:71], a[152:155], v[112:115]
ds_read_b64_tr_b16 v[64:65], v217 offset:20480
v_accvgpr_read_b32 v210, a98
v_mfma_f32_16x16x32_bf16 v[120:123], v[68:71], a[156:159], v[120:123]
ds_read_b64_tr_b16 v[66:67], v217 offset:22528
v_accvgpr_read_b32 v211, a99
v_mfma_f32_16x16x32_bf16 v[116:119], v[72:75], a[152:155], v[116:119]
ds_read_b64_tr_b16 v[68:69], v218 offset:20480
v_accvgpr_read_b32 v212, a100
v_mfma_f32_16x16x32_bf16 v[124:127], v[72:75], a[156:159], v[124:127]
ds_read_b64_tr_b16 v[70:71], v218 offset:22528
v_accvgpr_read_b32 v213, a101
v_mfma_f32_16x16x32_bf16 v[128:131], v[76:79], a[152:155], v[128:131]
ds_read_b64_tr_b16 v[72:73], v216 offset:24576
v_accvgpr_read_b32 v214, a102
v_mfma_f32_16x16x32_bf16 v[132:135], v[76:79], a[156:159], v[132:135]
ds_read_b64_tr_b16 v[74:75], v216 offset:26624
s_waitcnt lgkmcnt(6)
v_mfma_f32_16x16x32_bf16 v[112:115], v[192:195], a[160:163], v[112:115]
ds_read_b64_tr_b16 v[76:77], v217 offset:24576
v_accvgpr_read_b32 v215, a103
v_mfma_f32_16x16x32_bf16 v[120:123], v[192:195], a[164:167], v[120:123]
ds_read_b64_tr_b16 v[78:79], v217 offset:26624
v_cvt_pk_bf16_f32 v208, v208, v209
v_mfma_f32_16x16x32_bf16 v[116:119], v[196:199], a[160:163], v[116:119]
ds_read_b64_tr_b16 v[192:193], v218 offset:24576
v_cvt_pk_bf16_f32 v209, v210, v211
v_mfma_f32_16x16x32_bf16 v[124:127], v[196:199], a[164:167], v[124:127]
ds_read_b64_tr_b16 v[194:195], v218 offset:26624
v_cvt_pk_bf16_f32 v210, v212, v213
v_mfma_f32_16x16x32_bf16 v[128:131], v[200:203], a[160:163], v[128:131]
ds_read_b64_tr_b16 v[196:197], v216 offset:28672
v_cvt_pk_bf16_f32 v211, v214, v215
v_mfma_f32_16x16x32_bf16 v[132:135], v[200:203], a[164:167], v[132:135]
ds_read_b64_tr_b16 v[198:199], v216 offset:30720
buffer_store_dwordx4 v[208:211], v236, s[36:39], s98 offen offset:0 sc1
v_mfma_f32_16x16x32_bf16 v[112:115], v[204:207], a[168:171], v[112:115]
ds_read_b64_tr_b16 v[200:201], v217 offset:28672
v_accvgpr_read_b32 v224, a104
v_mfma_f32_16x16x32_bf16 v[120:123], v[204:207], a[172:175], v[120:123]
ds_read_b64_tr_b16 v[202:203], v217 offset:30720
s_waitcnt lgkmcnt(6)
v_mfma_f32_16x16x32_bf16 v[116:119], v[64:67], a[168:171], v[116:119]
ds_read_b64_tr_b16 v[204:205], v218 offset:28672
v_accvgpr_read_b32 v225, a105
v_accvgpr_read_b32 v226, a106
v_mfma_f32_16x16x32_bf16 v[124:127], v[64:67], a[172:175], v[124:127]
ds_read_b64_tr_b16 v[206:207], v218 offset:30720
v_add_u32 v216, 98304, v232
v_add_u32 v217, 98304, v233
v_add_u32 v218, 98304, v234
v_add_u32 v219, 98304, v235
v_mfma_f32_16x16x32_bf16 v[128:131], v[68:71], a[168:171], v[128:131]
s_waitcnt vmcnt(19)
s_barrier
ds_read_b64_tr_b16 v[64:65], v216 offset:0
v_mfma_f32_16x16x32_bf16 v[132:135], v[68:71], a[172:175], v[132:135]
ds_read_b64_tr_b16 v[66:67], v216 offset:2048
v_accvgpr_read_b32 v227, a107
v_accvgpr_read_b32 v228, a108
v_mfma_f32_16x16x32_bf16 v[112:115], v[72:75], a[176:179], v[112:115]
ds_read_b64_tr_b16 v[68:69], v217 offset:0
v_accvgpr_read_b32 v229, a109
v_accvgpr_read_b32 v230, a110
v_mfma_f32_16x16x32_bf16 v[120:123], v[72:75], a[180:183], v[120:123]
ds_read_b64_tr_b16 v[70:71], v217 offset:2048
v_accvgpr_read_b32 v231, a111
v_cvt_pk_bf16_f32 v224, v224, v225
v_mfma_f32_16x16x32_bf16 v[116:119], v[76:79], a[176:179], v[116:119]
ds_read_b64_tr_b16 v[72:73], v218 offset:0
v_cvt_pk_bf16_f32 v225, v226, v227
v_cvt_pk_bf16_f32 v226, v228, v229
v_mfma_f32_16x16x32_bf16 v[124:127], v[76:79], a[180:183], v[124:127]
ds_read_b64_tr_b16 v[74:75], v218 offset:2048
v_cvt_pk_bf16_f32 v227, v230, v231
s_waitcnt lgkmcnt(6)
v_mfma_f32_16x16x32_bf16 v[128:131], v[192:195], a[176:179], v[128:131]
ds_read_b64_tr_b16 v[76:77], v216 offset:4096
buffer_store_dwordx4 v[224:227], v236, s[36:39], s98 offen offset:1024 sc1
v_mfma_f32_16x16x32_bf16 v[132:135], v[192:195], a[180:183], v[132:135]
ds_read_b64_tr_b16 v[78:79], v216 offset:6144
v_accvgpr_read_b32 v208, a112
v_accvgpr_read_b32 v209, a113
v_mfma_f32_16x16x32_bf16 v[112:115], v[196:199], a[184:187], v[112:115]
ds_read_b64_tr_b16 v[192:193], v217 offset:4096
v_accvgpr_read_b32 v210, a114
v_accvgpr_read_b32 v211, a115
v_mfma_f32_16x16x32_bf16 v[120:123], v[196:199], a[188:191], v[120:123]
ds_read_b64_tr_b16 v[194:195], v217 offset:6144
v_accvgpr_read_b32 v212, a116
v_accvgpr_read_b32 v213, a117
v_mfma_f32_16x16x32_bf16 v[116:119], v[200:203], a[184:187], v[116:119]
ds_read_b64_tr_b16 v[196:197], v218 offset:4096
v_accvgpr_read_b32 v214, a118
v_accvgpr_read_b32 v215, a119
v_mfma_f32_16x16x32_bf16 v[124:127], v[200:203], a[188:191], v[124:127]
ds_read_b64_tr_b16 v[198:199], v218 offset:6144
v_cvt_pk_bf16_f32 v208, v208, v209
v_cvt_pk_bf16_f32 v209, v210, v211
v_mfma_f32_16x16x32_bf16 v[128:131], v[204:207], a[184:187], v[128:131]
ds_read_b64_tr_b16 v[200:201], v216 offset:8192
v_cvt_pk_bf16_f32 v210, v212, v213
v_cvt_pk_bf16_f32 v211, v214, v215
v_mfma_f32_16x16x32_bf16 v[132:135], v[204:207], a[188:191], v[132:135]
ds_read_b64_tr_b16 v[202:203], v216 offset:10240
buffer_store_dwordx4 v[208:211], v236, s[36:39], s98 offen offset:2048 sc1
s_waitcnt lgkmcnt(6)
v_mfma_f32_16x16x32_bf16 v[112:115], v[64:67], a[192:195], v[112:115]
ds_read_b64_tr_b16 v[204:205], v217 offset:8192
v_mfma_f32_16x16x32_bf16 v[120:123], v[64:67], a[196:199], v[120:123]
ds_read_b64_tr_b16 v[206:207], v217 offset:10240
v_mfma_f32_16x16x32_bf16 v[116:119], v[68:71], a[192:195], v[116:119]
ds_read_b64_tr_b16 v[64:65], v218 offset:8192
v_mfma_f32_16x16x32_bf16 v[124:127], v[68:71], a[196:199], v[124:127]
ds_read_b64_tr_b16 v[66:67], v218 offset:10240
v_mfma_f32_16x16x32_bf16 v[128:131], v[72:75], a[192:195], v[128:131]
ds_read_b64_tr_b16 v[68:69], v216 offset:12288
v_mfma_f32_16x16x32_bf16 v[132:135], v[72:75], a[196:199], v[132:135]
ds_read_b64_tr_b16 v[70:71], v216 offset:14336
v_accvgpr_read_b32 v224, a120
v_mfma_f32_16x16x32_bf16 v[112:115], v[76:79], a[200:203], v[112:115]
ds_read_b64_tr_b16 v[72:73], v217 offset:12288
v_accvgpr_read_b32 v225, a121
v_mfma_f32_16x16x32_bf16 v[120:123], v[76:79], a[204:207], v[120:123]
ds_read_b64_tr_b16 v[74:75], v217 offset:14336
s_waitcnt lgkmcnt(6)
v_mfma_f32_16x16x32_bf16 v[116:119], v[192:195], a[200:203], v[116:119]
ds_read_b64_tr_b16 v[76:77], v218 offset:12288
v_accvgpr_read_b32 v226, a122
v_mfma_f32_16x16x32_bf16 v[124:127], v[192:195], a[204:207], v[124:127]
ds_read_b64_tr_b16 v[78:79], v218 offset:14336
v_accvgpr_read_b32 v227, a123
v_mfma_f32_16x16x32_bf16 v[128:131], v[196:199], a[200:203], v[128:131]
ds_read_b64_tr_b16 v[192:193], v216 offset:16384
v_accvgpr_read_b32 v228, a124
v_mfma_f32_16x16x32_bf16 v[132:135], v[196:199], a[204:207], v[132:135]
ds_read_b64_tr_b16 v[194:195], v216 offset:18432
v_accvgpr_read_b32 v229, a125
v_mfma_f32_16x16x32_bf16 v[112:115], v[200:203], a[208:211], v[112:115]
ds_read_b64_tr_b16 v[196:197], v217 offset:16384
v_accvgpr_read_b32 v230, a126
v_mfma_f32_16x16x32_bf16 v[120:123], v[200:203], a[212:215], v[120:123]
ds_read_b64_tr_b16 v[198:199], v217 offset:18432
v_accvgpr_read_b32 v231, a127
v_mfma_f32_16x16x32_bf16 v[116:119], v[204:207], a[208:211], v[116:119]
ds_read_b64_tr_b16 v[200:201], v218 offset:16384
v_cvt_pk_bf16_f32 v224, v224, v225
v_mfma_f32_16x16x32_bf16 v[124:127], v[204:207], a[212:215], v[124:127]
ds_read_b64_tr_b16 v[202:203], v218 offset:18432
s_waitcnt lgkmcnt(6)
v_mfma_f32_16x16x32_bf16 v[128:131], v[64:67], a[208:211], v[128:131]
ds_read_b64_tr_b16 v[204:205], v216 offset:20480
v_cvt_pk_bf16_f32 v225, v226, v227
v_mfma_f32_16x16x32_bf16 v[132:135], v[64:67], a[212:215], v[132:135]
ds_read_b64_tr_b16 v[206:207], v216 offset:22528
v_cvt_pk_bf16_f32 v226, v228, v229
v_mfma_f32_16x16x32_bf16 v[112:115], v[68:71], a[216:219], v[112:115]
ds_read_b64_tr_b16 v[64:65], v217 offset:20480
v_cvt_pk_bf16_f32 v227, v230, v231
v_mfma_f32_16x16x32_bf16 v[120:123], v[68:71], a[220:223], v[120:123]
ds_read_b64_tr_b16 v[66:67], v217 offset:22528
buffer_store_dwordx4 v[224:227], v236, s[36:39], s98 offen offset:3072 sc1
v_mfma_f32_16x16x32_bf16 v[116:119], v[72:75], a[216:219], v[116:119]
ds_read_b64_tr_b16 v[68:69], v218 offset:20480
s_add_u32 s98, s76, 81920
s_add_u32 s98, s98, s77
v_mfma_f32_16x16x32_bf16 v[124:127], v[72:75], a[220:223], v[124:127]
ds_read_b64_tr_b16 v[70:71], v218 offset:22528
v_accvgpr_read_b32 v208, a240
v_mfma_f32_16x16x32_bf16 v[128:131], v[76:79], a[216:219], v[128:131]
v_accvgpr_read_b32 v209, a241
v_accvgpr_read_b32 v210, a242
v_mfma_f32_16x16x32_bf16 v[132:135], v[76:79], a[220:223], v[132:135]
v_accvgpr_read_b32 v211, a243
s_waitcnt lgkmcnt(4)
v_mfma_f32_16x16x32_bf16 v[112:115], v[192:195], a[224:227], v[112:115]
)ASM"
R"ASM(
v_accvgpr_read_b32 v212, a244
v_accvgpr_read_b32 v213, a245
v_mfma_f32_16x16x32_bf16 v[120:123], v[192:195], a[228:231], v[120:123]
v_accvgpr_read_b32 v214, a246
v_accvgpr_read_b32 v215, a247
v_mfma_f32_16x16x32_bf16 v[116:119], v[196:199], a[224:227], v[116:119]
v_cvt_pk_bf16_f32 v208, v208, v209
v_cvt_pk_bf16_f32 v209, v210, v211
v_mfma_f32_16x16x32_bf16 v[124:127], v[196:199], a[228:231], v[124:127]
v_cvt_pk_bf16_f32 v210, v212, v213
v_cvt_pk_bf16_f32 v211, v214, v215
v_mfma_f32_16x16x32_bf16 v[128:131], v[200:203], a[224:227], v[128:131]
buffer_store_dwordx4 v[208:211], v236, s[36:39], s98 offen offset:2048 sc1
v_mfma_f32_16x16x32_bf16 v[132:135], v[200:203], a[228:231], v[132:135]
v_accvgpr_read_b32 v224, a248
v_accvgpr_read_b32 v225, a249
v_mfma_f32_16x16x32_bf16 v[112:115], v[204:207], a[232:235], v[112:115]
v_accvgpr_read_b32 v226, a250
v_accvgpr_read_b32 v227, a251
v_mfma_f32_16x16x32_bf16 v[120:123], v[204:207], a[236:239], v[120:123]
v_accvgpr_read_b32 v228, a252
s_waitcnt lgkmcnt(0)
v_mfma_f32_16x16x32_bf16 v[116:119], v[64:67], a[232:235], v[116:119]
v_accvgpr_read_b32 v229, a253
v_accvgpr_read_b32 v230, a254
v_mfma_f32_16x16x32_bf16 v[124:127], v[64:67], a[236:239], v[124:127]
v_accvgpr_read_b32 v231, a255
v_cvt_pk_bf16_f32 v224, v224, v225
v_mfma_f32_16x16x32_bf16 v[128:131], v[68:71], a[232:235], v[128:131]
v_cvt_pk_bf16_f32 v225, v226, v227
v_cvt_pk_bf16_f32 v226, v228, v229
v_mfma_f32_16x16x32_bf16 v[132:135], v[68:71], a[236:239], v[132:135]
v_cvt_pk_bf16_f32 v227, v230, v231
buffer_store_dwordx4 v[224:227], v236, s[36:39], s98 offen offset:3072 sc1
v_mov_b32 v222, 0
buffer_load_dword v239, v222, s[36:39], s75 offen offset:128 sc1
s_waitcnt vmcnt(9)
v_pk_add_f32 v[144:145], v[144:145], v[146:147]
v_pk_add_f32 v[144:145], v[144:145], v[148:149]
v_pk_add_f32 v[144:145], v[144:145], v[150:151]
v_mov_b32 v251, v144
s_nop 1
v_permlane32_swap_b32 v251, v144
s_nop 1
v_add_f32 v144, v144, v251
v_mov_b32 v251, v144
s_nop 1
v_permlane16_swap_b32 v251, v144
s_nop 1
v_add_f32 v144, v144, v251
v_mov_b32 v251, v145
s_nop 1
v_permlane32_swap_b32 v251, v145
s_nop 1
v_add_f32 v145, v145, v251
v_mov_b32 v251, v145
s_nop 1
v_permlane16_swap_b32 v251, v145
s_nop 1
v_add_f32 v145, v145, v251
v_sub_f32 v149, 1.0, v240
v_max_f32 v149, 0x0da24260, v149
v_mul_f32 v144, 0xbb124925, v144
v_mul_f32 v145, 0x3b124925, v145
v_fma_f32 v145, -v144, v144, v145
v_max_f32 v145, 0, v145
v_add_f32 v145, 0x3727c5ac, v145
v_rsq_f32 v145, v145
s_nop 0
v_mul_f32 v146, 0xc0135761, v145
v_mul_f32 v147, v145, v145
v_mul_f32 v147, v147, v145
v_mul_f32 v147, 0xbdd2d3e8, v147
v_mul_f32 v145, v145, v149
v_rcp_f32 v148, v145
s_nop 0
v_pk_add_f32 v[152:153], v[152:153], v[154:155]
v_pk_add_f32 v[152:153], v[152:153], v[156:157]
v_pk_add_f32 v[152:153], v[152:153], v[158:159]
v_mov_b32 v251, v152
s_nop 1
v_permlane32_swap_b32 v251, v152
s_nop 1
v_add_f32 v152, v152, v251
v_mov_b32 v251, v152
s_nop 1
v_permlane16_swap_b32 v251, v152
s_nop 1
v_add_f32 v152, v152, v251
v_mov_b32 v251, v153
s_nop 1
v_permlane32_swap_b32 v251, v153
s_nop 1
v_add_f32 v153, v153, v251
v_mov_b32 v251, v153
s_nop 1
v_permlane16_swap_b32 v251, v153
s_nop 1
v_add_f32 v153, v153, v251
v_sub_f32 v157, 1.0, v241
v_max_f32 v157, 0x0da24260, v157
v_mul_f32 v152, 0xbb124925, v152
v_mul_f32 v153, 0x3b124925, v153
v_fma_f32 v153, -v152, v152, v153
v_max_f32 v153, 0, v153
v_add_f32 v153, 0x3727c5ac, v153
v_rsq_f32 v153, v153
s_nop 0
v_mul_f32 v154, 0xc0135761, v153
v_mul_f32 v155, v153, v153
v_mul_f32 v155, v155, v153
v_mul_f32 v155, 0xbdd2d3e8, v155
v_mul_f32 v153, v153, v157
v_rcp_f32 v156, v153
s_nop 0
v_fmac_f32 v80, v160, v144
v_fmac_f32 v81, v161, v144
v_fmac_f32 v82, v162, v144
v_fmac_f32 v83, v163, v144
v_fmac_f32 v84, v164, v144
v_fmac_f32 v85, v165, v144
v_fmac_f32 v86, v166, v144
v_fmac_f32 v87, v167, v144
v_mul_f32 v0, v80, v80
v_mul_f32 v1, v81, v81
v_mul_f32 v2, v82, v82
v_mul_f32 v3, v83, v83
v_mul_f32 v4, v84, v84
v_mul_f32 v5, v85, v85
v_mul_f32 v6, v86, v86
v_mul_f32 v7, v87, v87
v_fma_f32 v0, v0, v147, v146
v_fma_f32 v1, v1, v147, v146
v_fma_f32 v2, v2, v147, v146
v_fma_f32 v3, v3, v147, v146
v_fma_f32 v4, v4, v147, v146
v_fma_f32 v5, v5, v147, v146
v_fma_f32 v6, v6, v147, v146
v_fma_f32 v7, v7, v147, v146
v_mul_f32 v0, v0, v80
v_mul_f32 v1, v1, v81
v_mul_f32 v2, v2, v82
v_mul_f32 v3, v3, v83
v_mul_f32 v4, v4, v84
v_mul_f32 v5, v5, v85
v_mul_f32 v6, v6, v86
v_mul_f32 v7, v7, v87
v_exp_f32 v0, v0
v_exp_f32 v1, v1
v_exp_f32 v2, v2
v_exp_f32 v3, v3
v_exp_f32 v4, v4
v_exp_f32 v5, v5
v_exp_f32 v6, v6
v_exp_f32 v7, v7
v_fma_f32 v0, v0, v148, v148
v_fma_f32 v1, v1, v148, v148
v_fma_f32 v2, v2, v148, v148
v_fma_f32 v3, v3, v148, v148
v_fma_f32 v4, v4, v148, v148
v_fma_f32 v5, v5, v148, v148
v_fma_f32 v6, v6, v148, v148
v_fma_f32 v7, v7, v148, v148
v_rcp_f32 v0, v0
v_rcp_f32 v1, v1
v_rcp_f32 v2, v2
v_rcp_f32 v3, v3
v_rcp_f32 v4, v4
v_rcp_f32 v5, v5
v_rcp_f32 v6, v6
v_rcp_f32 v7, v7
v_mul_f32 v80, v80, v0
v_mul_f32 v81, v81, v1
v_mul_f32 v82, v82, v2
v_mul_f32 v83, v83, v3
v_mul_f32 v84, v84, v4
v_mul_f32 v85, v85, v5
v_mul_f32 v86, v86, v6
v_mul_f32 v87, v87, v7
v_cvt_pk_bf16_f32 v0, v80, v81
v_cvt_pk_bf16_f32 v1, v82, v83
v_cvt_pk_bf16_f32 v2, v84, v85
v_cvt_pk_bf16_f32 v3, v86, v87
v_accvgpr_write_b32 a224, v0
v_accvgpr_write_b32 a225, v1
v_accvgpr_write_b32 a226, v2
v_accvgpr_write_b32 a227, v3
s_waitcnt vmcnt(0)
s_barrier
s_mov_b32 s97, 0
.Lp3:
v_cmp_ne_u32 vcc, s29, v239
s_add_u32 s97, s97, 1
s_nop 2
s_cbranch_vccz .Lq3
s_cmp_ge_u32 s97, 400000
s_cbranch_scc1 .Lq3
s_sleep 1
buffer_load_dword v239, v222, s[36:39], s75 offen offset:128 sc1
s_waitcnt vmcnt(0)
)ASM"
R"ASM(
s_branch .Lp3
.Lq3:
v_mov_b32 v239, s29
s_mov_b64 exec, 1
buffer_store_dword v239, off, s[36:39], s67 offset:0 sc1
s_mov_b64 exec, -1
v_lshlrev_b32 v222, 6, v247
s_add_u32 s28, s70, 0
buffer_load_dwordx4 a[64:67], v236, s[36:39], s28 offen offset:0 sc1
v_fmac_f32 v88, v160, v152
v_fmac_f32 v89, v161, v152
v_fmac_f32 v90, v162, v152
v_fmac_f32 v91, v163, v152
v_fmac_f32 v92, v164, v152
v_fmac_f32 v93, v165, v152
v_fmac_f32 v94, v166, v152
v_fmac_f32 v95, v167, v152
v_mul_f32 v8, v88, v88
v_mul_f32 v9, v89, v89
v_mul_f32 v10, v90, v90
v_mul_f32 v11, v91, v91
v_mul_f32 v12, v92, v92
buffer_load_dwordx4 a[68:71], v236, s[36:39], s28 offen offset:1024 sc1
v_mul_f32 v13, v93, v93
v_mul_f32 v14, v94, v94
v_mul_f32 v15, v95, v95
v_fma_f32 v8, v8, v155, v154
v_fma_f32 v9, v9, v155, v154
v_fma_f32 v10, v10, v155, v154
v_fma_f32 v11, v11, v155, v154
v_fma_f32 v12, v12, v155, v154
v_fma_f32 v13, v13, v155, v154
v_fma_f32 v14, v14, v155, v154
v_fma_f32 v15, v15, v155, v154
v_mul_f32 v8, v8, v88
v_mul_f32 v9, v9, v89
buffer_load_dwordx4 a[72:75], v236, s[36:39], s28 offen offset:2048 sc1
v_mul_f32 v10, v10, v90
v_mul_f32 v11, v11, v91
v_mul_f32 v12, v12, v92
v_mul_f32 v13, v13, v93
v_mul_f32 v14, v14, v94
v_mul_f32 v15, v15, v95
v_exp_f32 v8, v8
v_exp_f32 v9, v9
v_exp_f32 v10, v10
v_exp_f32 v11, v11
v_exp_f32 v12, v12
v_exp_f32 v13, v13
v_exp_f32 v14, v14
buffer_load_dwordx4 a[76:79], v236, s[36:39], s28 offen offset:3072 sc1
v_exp_f32 v15, v15
v_fma_f32 v8, v8, v156, v156
v_fma_f32 v9, v9, v156, v156
v_fma_f32 v10, v10, v156, v156
v_fma_f32 v11, v11, v156, v156
v_fma_f32 v12, v12, v156, v156
v_fma_f32 v13, v13, v156, v156
v_fma_f32 v14, v14, v156, v156
v_fma_f32 v15, v15, v156, v156
v_rcp_f32 v8, v8
v_rcp_f32 v9, v9
v_rcp_f32 v10, v10
v_rcp_f32 v11, v11
s_add_u32 s28, s70, 4096
buffer_load_dwordx4 a[80:83], v236, s[36:39], s28 offen offset:0 sc1
v_rcp_f32 v12, v12
v_rcp_f32 v13, v13
v_rcp_f32 v14, v14
v_rcp_f32 v15, v15
v_mul_f32 v88, v88, v8
v_mul_f32 v89, v89, v9
v_mul_f32 v90, v90, v10
v_mul_f32 v91, v91, v11
v_mul_f32 v92, v92, v12
v_mul_f32 v93, v93, v13
v_mul_f32 v94, v94, v14
v_mul_f32 v95, v95, v15
v_cvt_pk_bf16_f32 v8, v88, v89
buffer_load_dwordx4 a[84:87], v236, s[36:39], s28 offen offset:1024 sc1
v_cvt_pk_bf16_f32 v9, v90, v91
v_cvt_pk_bf16_f32 v10, v92, v93
v_cvt_pk_bf16_f32 v11, v94, v95
v_accvgpr_write_b32 a228, v8
v_accvgpr_write_b32 a229, v9
v_accvgpr_write_b32 a230, v10
v_accvgpr_write_b32 a231, v11
v_fmac_f32 v96, v168, v144
v_fmac_f32 v97, v169, v144
v_fmac_f32 v98, v170, v144
v_fmac_f32 v99, v171, v144
v_fmac_f32 v100, v172, v144
v_fmac_f32 v101, v173, v144
buffer_load_dwordx4 a[88:91], v236, s[36:39], s28 offen offset:2048 sc1
v_fmac_f32 v102, v174, v144
v_fmac_f32 v103, v175, v144
v_mul_f32 v0, v96, v96
v_mul_f32 v1, v97, v97
v_mul_f32 v2, v98, v98
v_mul_f32 v3, v99, v99
v_mul_f32 v4, v100, v100
v_mul_f32 v5, v101, v101
v_mul_f32 v6, v102, v102
v_mul_f32 v7, v103, v103
v_fma_f32 v0, v0, v147, v146
v_fma_f32 v1, v1, v147, v146
v_fma_f32 v2, v2, v147, v146
buffer_load_dwordx4 a[92:95], v236, s[36:39], s28 offen offset:3072 sc1
v_fma_f32 v3, v3, v147, v146
v_fma_f32 v4, v4, v147, v146
v_fma_f32 v5, v5, v147, v146
v_fma_f32 v6, v6, v147, v146
v_fma_f32 v7, v7, v147, v146
v_mul_f32 v0, v0, v96
v_mul_f32 v1, v1, v97
v_mul_f32 v2, v2, v98
v_mul_f32 v3, v3, v99
v_mul_f32 v4, v4, v100
v_mul_f32 v5, v5, v101
v_mul_f32 v6, v6, v102
v_mul_f32 v7, v7, v103
s_add_u32 s28, s70, 8192
buffer_load_dwordx4 a[96:99], v236, s[36:39], s28 offen offset:0 sc1
v_exp_f32 v0, v0
v_exp_f32 v1, v1
v_exp_f32 v2, v2
v_exp_f32 v3, v3
v_exp_f32 v4, v4
v_exp_f32 v5, v5
v_exp_f32 v6, v6
v_exp_f32 v7, v7
v_fma_f32 v0, v0, v148, v148
v_fma_f32 v1, v1, v148, v148
v_fma_f32 v2, v2, v148, v148
v_fma_f32 v3, v3, v148, v148
v_fma_f32 v4, v4, v148, v148
buffer_load_dwordx4 a[100:103], v236, s[36:39], s28 offen offset:1024 sc1
v_fma_f32 v5, v5, v148, v148
v_fma_f32 v6, v6, v148, v148
v_fma_f32 v7, v7, v148, v148
v_rcp_f32 v0, v0
v_rcp_f32 v1, v1
v_rcp_f32 v2, v2
v_rcp_f32 v3, v3
v_rcp_f32 v4, v4
v_rcp_f32 v5, v5
v_rcp_f32 v6, v6
v_rcp_f32 v7, v7
v_mul_f32 v96, v96, v0
v_mul_f32 v97, v97, v1
buffer_load_dwordx4 a[104:107], v236, s[36:39], s28 offen offset:2048 sc1
v_mul_f32 v98, v98, v2
v_mul_f32 v99, v99, v3
v_mul_f32 v100, v100, v4
v_mul_f32 v101, v101, v5
v_mul_f32 v102, v102, v6
v_mul_f32 v103, v103, v7
v_cvt_pk_bf16_f32 v0, v96, v97
v_cvt_pk_bf16_f32 v1, v98, v99
v_cvt_pk_bf16_f32 v2, v100, v101
v_cvt_pk_bf16_f32 v3, v102, v103
v_accvgpr_write_b32 a232, v0
v_accvgpr_write_b32 a233, v1
v_accvgpr_write_b32 a234, v2
buffer_load_dwordx4 a[108:111], v236, s[36:39], s28 offen offset:3072 sc1
v_accvgpr_write_b32 a235, v3
v_fmac_f32 v104, v168, v152
v_fmac_f32 v105, v169, v152
v_fmac_f32 v106, v170, v152
v_fmac_f32 v107, v171, v152
v_fmac_f32 v108, v172, v152
v_fmac_f32 v109, v173, v152
v_fmac_f32 v110, v174, v152
v_fmac_f32 v111, v175, v152
v_mul_f32 v8, v104, v104
v_mul_f32 v9, v105, v105
v_mul_f32 v10, v106, v106
v_mul_f32 v11, v107, v107
s_add_u32 s28, s70, 12288
buffer_load_dwordx4 a[112:115], v236, s[36:39], s28 offen offset:0 sc1
v_mul_f32 v12, v108, v108
v_mul_f32 v13, v109, v109
v_mul_f32 v14, v110, v110
v_mul_f32 v15, v111, v111
v_fma_f32 v8, v8, v155, v154
v_fma_f32 v9, v9, v155, v154
v_fma_f32 v10, v10, v155, v154
v_fma_f32 v11, v11, v155, v154
v_fma_f32 v12, v12, v155, v154
v_fma_f32 v13, v13, v155, v154
v_fma_f32 v14, v14, v155, v154
v_fma_f32 v15, v15, v155, v154
v_mul_f32 v8, v8, v104
buffer_load_dwordx4 a[116:119], v236, s[36:39], s28 offen offset:1024 sc1
v_mul_f32 v9, v9, v105
v_mul_f32 v10, v10, v106
v_mul_f32 v11, v11, v107
v_mul_f32 v12, v12, v108
v_mul_f32 v13, v13, v109
v_mul_f32 v14, v14, v110
)ASM"
R"ASM(
v_mul_f32 v15, v15, v111
v_exp_f32 v8, v8
v_exp_f32 v9, v9
v_exp_f32 v10, v10
v_exp_f32 v11, v11
v_exp_f32 v12, v12
v_exp_f32 v13, v13
buffer_load_dwordx4 a[120:123], v236, s[36:39], s28 offen offset:2048 sc1
v_exp_f32 v14, v14
v_exp_f32 v15, v15
v_fma_f32 v8, v8, v156, v156
v_fma_f32 v9, v9, v156, v156
v_fma_f32 v10, v10, v156, v156
v_fma_f32 v11, v11, v156, v156
v_fma_f32 v12, v12, v156, v156
v_fma_f32 v13, v13, v156, v156
v_fma_f32 v14, v14, v156, v156
v_fma_f32 v15, v15, v156, v156
v_rcp_f32 v8, v8
v_rcp_f32 v9, v9
v_rcp_f32 v10, v10
buffer_load_dwordx4 a[124:127], v236, s[36:39], s28 offen offset:3072 sc1
v_rcp_f32 v11, v11
v_rcp_f32 v12, v12
v_rcp_f32 v13, v13
v_rcp_f32 v14, v14
v_rcp_f32 v15, v15
v_mul_f32 v104, v104, v8
v_mul_f32 v105, v105, v9
v_mul_f32 v106, v106, v10
v_mul_f32 v107, v107, v11
v_mul_f32 v108, v108, v12
v_mul_f32 v109, v109, v13
v_mul_f32 v110, v110, v14
v_mul_f32 v111, v111, v15
v_cvt_pk_bf16_f32 v8, v104, v105
v_cvt_pk_bf16_f32 v9, v106, v107
v_cvt_pk_bf16_f32 v10, v108, v109
v_cvt_pk_bf16_f32 v11, v110, v111
v_accvgpr_write_b32 a236, v8
v_accvgpr_write_b32 a237, v9
v_accvgpr_write_b32 a238, v10
v_accvgpr_write_b32 a239, v11
buffer_load_dword v239, v222, s[36:39], s74 offen offset:0 sc1
s_add_u32 s4, s64, s65
s_add_u32 s5, s4, 32768
s_mov_b32 s6, -1
s_mov_b32 s7, 0
s_mov_b32 s8, 0
s_mov_b32 s10, 0xe0ad78ec
s_mov_b32 s11, 1
s_mov_b32 s13, 0xff800000
s_waitcnt vmcnt(0)
s_mov_b32 s97, 0
.Lp4:
v_cmp_ne_u32 vcc, s29, v239
s_add_u32 s97, s97, 1
s_nop 2
s_cbranch_vccz .Lq4
s_cmp_ge_u32 s97, 400000
s_cbranch_scc1 .Lq4
s_sleep 1
buffer_load_dword v239, v222, s[36:39], s74 offen offset:0 sc1
s_waitcnt vmcnt(0)
s_branch .Lp4
.Lq4:
s_add_u32 m0, s8, s68
s_nop 0
buffer_load_dwordx4 v236, s[36:39], s4 offen offset:0 sc1 lds
buffer_load_dwordx4 v236, s[36:39], s4 offen offset:1024 sc1 lds
buffer_load_dwordx4 v236, s[36:39], s4 offen offset:2048 sc1 lds
buffer_load_dwordx4 v236, s[36:39], s4 offen offset:3072 sc1 lds
s_add_u32 m0, m0, 4096
s_add_u32 s9, s4, 4096
buffer_load_dwordx4 v236, s[36:39], s9 offen offset:0 sc1 lds
buffer_load_dwordx4 v236, s[36:39], s9 offen offset:1024 sc1 lds
buffer_load_dwordx4 v236, s[36:39], s9 offen offset:2048 sc1 lds
buffer_load_dwordx4 v236, s[36:39], s9 offen offset:3072 sc1 lds
s_add_u32 s8, s8, 32768
s_cmp_eq_u32 s8, 163840
s_cselect_b32 s8, 0, s8
s_add_u32 s4, s4, 65536
s_add_u32 s9, s6, 2
s_cmp_eq_u32 s9, s66
s_cselect_b32 s4, s64, s4
s_add_u32 m0, s8, s68
s_nop 0
buffer_load_dwordx4 v236, s[36:39], s4 offen offset:0 sc1 lds
v_fmac_f32 v112, v176, v144
v_fmac_f32 v113, v177, v144
v_fmac_f32 v114, v178, v144
v_fmac_f32 v115, v179, v144
v_fmac_f32 v116, v180, v144
v_fmac_f32 v117, v181, v144
v_fmac_f32 v118, v182, v144
v_fmac_f32 v119, v183, v144
v_mul_f32 v0, v112, v112
v_mul_f32 v1, v113, v113
v_mul_f32 v2, v114, v114
v_mul_f32 v3, v115, v115
buffer_load_dwordx4 v236, s[36:39], s4 offen offset:1024 sc1 lds
v_mul_f32 v4, v116, v116
v_mul_f32 v5, v117, v117
v_mul_f32 v6, v118, v118
v_mul_f32 v7, v119, v119
v_fma_f32 v0, v0, v147, v146
v_fma_f32 v1, v1, v147, v146
v_fma_f32 v2, v2, v147, v146
v_fma_f32 v3, v3, v147, v146
v_fma_f32 v4, v4, v147, v146
v_fma_f32 v5, v5, v147, v146
v_fma_f32 v6, v6, v147, v146
v_fma_f32 v7, v7, v147, v146
buffer_load_dwordx4 v236, s[36:39], s4 offen offset:2048 sc1 lds
v_mul_f32 v0, v0, v112
v_mul_f32 v1, v1, v113
v_mul_f32 v2, v2, v114
v_mul_f32 v3, v3, v115
v_mul_f32 v4, v4, v116
v_mul_f32 v5, v5, v117
v_mul_f32 v6, v6, v118
v_mul_f32 v7, v7, v119
v_exp_f32 v0, v0
v_exp_f32 v1, v1
v_exp_f32 v2, v2
v_exp_f32 v3, v3
buffer_load_dwordx4 v236, s[36:39], s4 offen offset:3072 sc1 lds
v_exp_f32 v4, v4
v_exp_f32 v5, v5
v_exp_f32 v6, v6
v_exp_f32 v7, v7
v_fma_f32 v0, v0, v148, v148
v_fma_f32 v1, v1, v148, v148
v_fma_f32 v2, v2, v148, v148
v_fma_f32 v3, v3, v148, v148
v_fma_f32 v4, v4, v148, v148
v_fma_f32 v5, v5, v148, v148
v_fma_f32 v6, v6, v148, v148
v_fma_f32 v7, v7, v148, v148
s_add_u32 m0, m0, 4096
s_add_u32 s9, s4, 4096
buffer_load_dwordx4 v236, s[36:39], s9 offen offset:0 sc1 lds
v_rcp_f32 v0, v0
v_rcp_f32 v1, v1
v_rcp_f32 v2, v2
v_rcp_f32 v3, v3
v_rcp_f32 v4, v4
v_rcp_f32 v5, v5
v_rcp_f32 v6, v6
v_rcp_f32 v7, v7
v_mul_f32 v112, v112, v0
v_mul_f32 v113, v113, v1
v_mul_f32 v114, v114, v2
v_mul_f32 v115, v115, v3
buffer_load_dwordx4 v236, s[36:39], s9 offen offset:1024 sc1 lds
v_mul_f32 v116, v116, v4
v_mul_f32 v117, v117, v5
v_mul_f32 v118, v118, v6
v_mul_f32 v119, v119, v7
v_cvt_pk_bf16_f32 v0, v112, v113
v_cvt_pk_bf16_f32 v1, v114, v115
v_cvt_pk_bf16_f32 v2, v116, v117
v_cvt_pk_bf16_f32 v3, v118, v119
v_accvgpr_write_b32 a240, v0
v_accvgpr_write_b32 a241, v1
v_accvgpr_write_b32 a242, v2
v_accvgpr_write_b32 a243, v3
buffer_load_dwordx4 v236, s[36:39], s9 offen offset:2048 sc1 lds
v_fmac_f32 v120, v176, v152
v_fmac_f32 v121, v177, v152
v_fmac_f32 v122, v178, v152
v_fmac_f32 v123, v179, v152
v_fmac_f32 v124, v180, v152
v_fmac_f32 v125, v181, v152
v_fmac_f32 v126, v182, v152
v_fmac_f32 v127, v183, v152
v_mul_f32 v8, v120, v120
v_mul_f32 v9, v121, v121
v_mul_f32 v10, v122, v122
v_mul_f32 v11, v123, v123
buffer_load_dwordx4 v236, s[36:39], s9 offen offset:3072 sc1 lds
v_mul_f32 v12, v124, v124
v_mul_f32 v13, v125, v125
v_mul_f32 v14, v126, v126
v_mul_f32 v15, v127, v127
v_fma_f32 v8, v8, v155, v154
v_fma_f32 v9, v9, v155, v154
v_fma_f32 v10, v10, v155, v154
v_fma_f32 v11, v11, v155, v154
v_fma_f32 v12, v12, v155, v154
v_fma_f32 v13, v13, v155, v154
v_fma_f32 v14, v14, v155, v154
v_fma_f32 v15, v15, v155, v154
s_add_u32 s8, s8, 32768
s_cmp_eq_u32 s8, 163840
s_cselect_b32 s8, 0, s8
v_mul_f32 v8, v8, v120
v_mul_f32 v9, v9, v121
v_mul_f32 v10, v10, v122
v_mul_f32 v11, v11, v123
)ASM"
R"ASM(
v_mul_f32 v12, v12, v124
v_mul_f32 v13, v13, v125
v_mul_f32 v14, v14, v126
v_mul_f32 v15, v15, v127
v_exp_f32 v8, v8
v_exp_f32 v9, v9
v_exp_f32 v10, v10
v_exp_f32 v11, v11
s_add_u32 m0, s8, s68
s_nop 0
buffer_load_dwordx4 v236, s[36:39], s5 offen offset:0 sc1 lds
v_exp_f32 v12, v12
v_exp_f32 v13, v13
v_exp_f32 v14, v14
v_exp_f32 v15, v15
v_fma_f32 v8, v8, v156, v156
v_fma_f32 v9, v9, v156, v156
v_fma_f32 v10, v10, v156, v156
v_fma_f32 v11, v11, v156, v156
v_fma_f32 v12, v12, v156, v156
v_fma_f32 v13, v13, v156, v156
v_fma_f32 v14, v14, v156, v156
v_fma_f32 v15, v15, v156, v156
buffer_load_dwordx4 v236, s[36:39], s5 offen offset:1024 sc1 lds
v_rcp_f32 v8, v8
v_rcp_f32 v9, v9
v_rcp_f32 v10, v10
v_rcp_f32 v11, v11
v_rcp_f32 v12, v12
v_rcp_f32 v13, v13
v_rcp_f32 v14, v14
v_rcp_f32 v15, v15
v_mul_f32 v120, v120, v8
v_mul_f32 v121, v121, v9
v_mul_f32 v122, v122, v10
v_mul_f32 v123, v123, v11
buffer_load_dwordx4 v236, s[36:39], s5 offen offset:2048 sc1 lds
v_mul_f32 v124, v124, v12
v_mul_f32 v125, v125, v13
v_mul_f32 v126, v126, v14
v_mul_f32 v127, v127, v15
v_cvt_pk_bf16_f32 v8, v120, v121
v_cvt_pk_bf16_f32 v9, v122, v123
v_cvt_pk_bf16_f32 v10, v124, v125
v_cvt_pk_bf16_f32 v11, v126, v127
v_accvgpr_write_b32 a244, v8
v_accvgpr_write_b32 a245, v9
v_accvgpr_write_b32 a246, v10
v_accvgpr_write_b32 a247, v11
buffer_load_dwordx4 v236, s[36:39], s5 offen offset:3072 sc1 lds
v_fmac_f32 v128, v184, v144
v_fmac_f32 v129, v185, v144
v_fmac_f32 v130, v186, v144
v_fmac_f32 v131, v187, v144
v_fmac_f32 v132, v184, v152
v_fmac_f32 v133, v185, v152
v_fmac_f32 v134, v186, v152
v_fmac_f32 v135, v187, v152
v_mul_f32 v0, v128, v128
v_mul_f32 v1, v129, v129
v_mul_f32 v2, v130, v130
v_mul_f32 v3, v131, v131
s_add_u32 m0, m0, 4096
s_add_u32 s9, s5, 4096
buffer_load_dwordx4 v236, s[36:39], s9 offen offset:0 sc1 lds
v_mul_f32 v4, v132, v132
v_mul_f32 v5, v133, v133
v_mul_f32 v6, v134, v134
v_mul_f32 v7, v135, v135
v_fma_f32 v0, v0, v147, v146
v_fma_f32 v1, v1, v147, v146
v_fma_f32 v2, v2, v147, v146
v_fma_f32 v3, v3, v147, v146
v_fma_f32 v4, v4, v155, v154
v_fma_f32 v5, v5, v155, v154
v_fma_f32 v6, v6, v155, v154
v_fma_f32 v7, v7, v155, v154
buffer_load_dwordx4 v236, s[36:39], s9 offen offset:1024 sc1 lds
v_mul_f32 v0, v0, v128
v_mul_f32 v1, v1, v129
v_mul_f32 v2, v2, v130
v_mul_f32 v3, v3, v131
v_mul_f32 v4, v4, v132
v_mul_f32 v5, v5, v133
v_mul_f32 v6, v6, v134
v_mul_f32 v7, v7, v135
v_exp_f32 v0, v0
v_exp_f32 v1, v1
v_exp_f32 v2, v2
v_exp_f32 v3, v3
buffer_load_dwordx4 v236, s[36:39], s9 offen offset:2048 sc1 lds
v_exp_f32 v4, v4
v_exp_f32 v5, v5
v_exp_f32 v6, v6
v_exp_f32 v7, v7
v_fma_f32 v0, v0, v148, v148
v_fma_f32 v1, v1, v148, v148
v_fma_f32 v2, v2, v148, v148
v_fma_f32 v3, v3, v148, v148
v_fma_f32 v4, v4, v156, v156
v_fma_f32 v5, v5, v156, v156
v_fma_f32 v6, v6, v156, v156
v_fma_f32 v7, v7, v156, v156
buffer_load_dwordx4 v236, s[36:39], s9 offen offset:3072 sc1 lds
v_rcp_f32 v0, v0
v_rcp_f32 v1, v1
v_rcp_f32 v2, v2
v_rcp_f32 v3, v3
v_rcp_f32 v4, v4
v_rcp_f32 v5, v5
v_rcp_f32 v6, v6
v_rcp_f32 v7, v7
v_mul_f32 v128, v128, v0
v_mul_f32 v129, v129, v1
v_mul_f32 v130, v130, v2
v_mul_f32 v131, v131, v3
s_add_u32 s8, s8, 32768
s_cmp_eq_u32 s8, 163840
s_cselect_b32 s8, 0, s8
v_mul_f32 v132, v132, v4
v_mul_f32 v133, v133, v5
v_mul_f32 v134, v134, v6
v_mul_f32 v135, v135, v7
v_cvt_pk_bf16_f32 v0, v128, v129
v_cvt_pk_bf16_f32 v1, v130, v131
v_cvt_pk_bf16_f32 v2, v132, v133
v_cvt_pk_bf16_f32 v3, v134, v135
v_accvgpr_write_b32 a248, v0
v_accvgpr_write_b32 a249, v1
v_accvgpr_write_b32 a250, v2
v_accvgpr_write_b32 a251, v3
v_lshrrev_b32 v188, 4, v236
v_add_u32 v188, s87, v188
v_and_b32 v189, 0, v188
v_lshrrev_b32 v188, 0, v188
v_mul_u32_u24 v189, 0, v189
v_mul_u32_u24 v188, 128, v188
v_add_u32 v188, v188, v189
s_bfm_b64 exec, 28, 0
buffer_load_dword v189, v188, s[44:47], 0 offen offset:0 sc1
s_mov_b64 exec, -1
v_lshrrev_b32 v190, 4, v236
v_add_u32 v190, s87, v190
v_and_b32 v191, 0, v190
v_lshrrev_b32 v190, 0, v190
v_mul_u32_u24 v191, 0, v191
v_mul_u32_u24 v190, 64, v190
v_add_u32 v190, v190, v191
s_bfm_b64 exec, 28, 0
buffer_load_dword v191, v190, s[56:59], 0 offen offset:0 sc1
s_mov_b64 exec, -1
s_mov_b32 s6, 0
v_cmp_gt_i32 s[16:17], 0, v246
v_cmp_gt_i32 s[18:19], 1, v246
v_cmp_gt_i32 s[20:21], 2, v246
v_cmp_gt_i32 s[22:23], 3, v246
v_mov_b64 v[0:1], 0
v_mov_b64 v[2:3], 0
v_mov_b64 v[4:5], 0
v_mov_b64 v[6:7], 0
v_mov_b64 v[8:9], 0
v_mov_b64 v[10:11], 0
v_mov_b64 v[12:13], 0
v_mov_b64 v[14:15], 0
v_mov_b64 v[16:17], 0
v_mov_b64 v[18:19], 0
v_mov_b64 v[20:21], 0
v_mov_b64 v[22:23], 0
v_mov_b64 v[24:25], 0
v_mov_b64 v[26:27], 0
v_mov_b64 v[28:29], 0
v_mov_b64 v[30:31], 0
v_mov_b64 v[32:33], 0
v_mov_b64 v[34:35], 0
v_mov_b64 v[36:37], 0
v_mov_b64 v[38:39], 0
v_mov_b64 v[40:41], 0
v_mov_b64 v[42:43], 0
v_mov_b64 v[44:45], 0
v_mov_b64 v[46:47], 0
v_mov_b64 v[48:49], 0
v_mov_b64 v[50:51], 0
v_mov_b64 v[52:53], 0
v_mov_b64 v[54:55], 0
v_mov_b64 v[56:57], 0
v_mov_b64 v[58:59], 0
v_mov_b64 v[60:61], 0
v_mov_b64 v[62:63], 0
v_mov_b64 v[64:65], 0
v_mov_b64 v[66:67], 0
v_mov_b64 v[68:69], 0
v_mov_b64 v[70:71], 0
v_mov_b64 v[72:73], 0
v_mov_b64 v[74:75], 0
v_mov_b64 v[76:77], 0
v_mov_b64 v[78:79], 0
v_mov_b64 v[80:81], 0
v_mov_b64 v[82:83], 0
v_mov_b64 v[84:85], 0
v_mov_b64 v[86:87], 0
)ASM"
R"ASM(
v_mov_b64 v[88:89], 0
v_mov_b64 v[90:91], 0
v_mov_b64 v[92:93], 0
v_mov_b64 v[94:95], 0
v_mov_b64 v[96:97], 0
v_mov_b64 v[98:99], 0
v_mov_b64 v[100:101], 0
v_mov_b64 v[102:103], 0
v_mov_b64 v[104:105], 0
v_mov_b64 v[106:107], 0
v_mov_b64 v[108:109], 0
v_mov_b64 v[110:111], 0
v_mov_b64 v[112:113], 0
v_mov_b64 v[114:115], 0
v_mov_b64 v[116:117], 0
v_mov_b64 v[118:119], 0
v_mov_b64 v[120:121], 0
v_mov_b64 v[122:123], 0
v_mov_b64 v[124:125], 0
v_mov_b64 v[126:127], 0
v_mov_b64 v[176:177], 0
v_mov_b64 v[178:179], 0
v_mov_b64 v[180:181], 0
v_mov_b64 v[182:183], 0
v_mov_b64 v[226:227], 0
s_waitcnt vmcnt(16)
s_barrier
v_add_u32 v242, s7, v236
ds_read_b128 v[200:203], v242 offset:0
ds_read_b128 v[204:207], v242 offset:1024
ds_read_b128 v[208:211], v242 offset:2048
ds_read_b128 v[212:215], v242 offset:3072
ds_read_b128 v[216:219], v242 offset:4096
ds_read_b128 v[220:223], v242 offset:5120
ds_read_b128 v[192:195], v242 offset:6144
s_waitcnt lgkmcnt(3)
v_mfma_f32_16x16x32_bf16 v[128:131], v[200:203], a[64:67], v[176:179]
ds_read_b128 v[196:199], v242 offset:7168
s_add_u32 s4, s4, 65536
s_add_u32 s9, s6, 2
s_cmp_eq_u32 s9, s66
s_cselect_b32 s4, s64, s4
v_mfma_f32_16x16x32_bf16 v[136:139], v[200:203], a[68:71], v[180:183]
v_mfma_f32_16x16x32_bf16 v[128:131], v[204:207], a[72:75], v[128:131]
ds_read_b128 v[200:203], v242 offset:8192
v_mfma_f32_16x16x32_bf16 v[136:139], v[204:207], a[76:79], v[136:139]
v_mfma_f32_16x16x32_bf16 v[128:131], v[208:211], a[80:83], v[128:131]
ds_read_b128 v[204:207], v242 offset:9216
s_add_u32 m0, s8, s68
s_nop 0
buffer_load_dwordx4 v236, s[36:39], s4 offen offset:0 sc1 lds
v_mfma_f32_16x16x32_bf16 v[136:139], v[208:211], a[84:87], v[136:139]
v_mfma_f32_16x16x32_bf16 v[128:131], v[212:215], a[88:91], v[128:131]
ds_read_b128 v[208:211], v242 offset:10240
v_mfma_f32_16x16x32_bf16 v[136:139], v[212:215], a[92:95], v[136:139]
s_waitcnt lgkmcnt(3)
v_mfma_f32_16x16x32_bf16 v[128:131], v[216:219], a[96:99], v[128:131]
ds_read_b128 v[212:215], v242 offset:11264
buffer_load_dwordx4 v236, s[36:39], s4 offen offset:1024 sc1 lds
v_mfma_f32_16x16x32_bf16 v[136:139], v[216:219], a[100:103], v[136:139]
v_mfma_f32_16x16x32_bf16 v[128:131], v[220:223], a[104:107], v[128:131]
ds_read_b128 v[216:219], v242 offset:12288
v_mfma_f32_16x16x32_bf16 v[136:139], v[220:223], a[108:111], v[136:139]
v_mfma_f32_16x16x32_bf16 v[128:131], v[192:195], a[112:115], v[128:131]
ds_read_b128 v[220:223], v242 offset:13312
buffer_load_dwordx4 v236, s[36:39], s4 offen offset:2048 sc1 lds
v_mfma_f32_16x16x32_bf16 v[136:139], v[192:195], a[116:119], v[136:139]
v_mfma_f32_16x16x32_bf16 v[128:131], v[196:199], a[120:123], v[128:131]
ds_read_b128 v[192:195], v242 offset:14336
v_mfma_f32_16x16x32_bf16 v[136:139], v[196:199], a[124:127], v[136:139]
s_waitcnt lgkmcnt(3)
v_mfma_f32_16x16x32_bf16 v[132:135], v[200:203], a[64:67], v[176:179]
ds_read_b128 v[196:199], v242 offset:15360
buffer_load_dwordx4 v236, s[36:39], s4 offen offset:3072 sc1 lds
v_mfma_f32_16x16x32_bf16 v[140:143], v[200:203], a[68:71], v[180:183]
v_mfma_f32_16x16x32_bf16 v[132:135], v[204:207], a[72:75], v[132:135]
ds_read_b128 v[200:203], v242 offset:16384
v_mfma_f32_16x16x32_bf16 v[140:143], v[204:207], a[76:79], v[140:143]
v_mfma_f32_16x16x32_bf16 v[132:135], v[208:211], a[80:83], v[132:135]
ds_read_b128 v[204:207], v242 offset:17408
s_add_u32 m0, m0, 4096
s_add_u32 s9, s4, 4096
buffer_load_dwordx4 v236, s[36:39], s9 offen offset:0 sc1 lds
v_mfma_f32_16x16x32_bf16 v[140:143], v[208:211], a[84:87], v[140:143]
v_mfma_f32_16x16x32_bf16 v[132:135], v[212:215], a[88:91], v[132:135]
ds_read_b128 v[208:211], v242 offset:18432
v_mfma_f32_16x16x32_bf16 v[140:143], v[212:215], a[92:95], v[140:143]
s_waitcnt lgkmcnt(3)
v_mfma_f32_16x16x32_bf16 v[132:135], v[216:219], a[96:99], v[132:135]
ds_read_b128 v[212:215], v242 offset:19456
buffer_load_dwordx4 v236, s[36:39], s9 offen offset:1024 sc1 lds
v_mfma_f32_16x16x32_bf16 v[140:143], v[216:219], a[100:103], v[140:143]
v_mfma_f32_16x16x32_bf16 v[132:135], v[220:223], a[104:107], v[132:135]
ds_read_b128 v[216:219], v242 offset:20480
v_mfma_f32_16x16x32_bf16 v[140:143], v[220:223], a[108:111], v[140:143]
v_mfma_f32_16x16x32_bf16 v[132:135], v[192:195], a[112:115], v[132:135]
ds_read_b128 v[220:223], v242 offset:21504
buffer_load_dwordx4 v236, s[36:39], s9 offen offset:2048 sc1 lds
v_mfma_f32_16x16x32_bf16 v[140:143], v[192:195], a[116:119], v[140:143]
v_mfma_f32_16x16x32_bf16 v[132:135], v[196:199], a[120:123], v[132:135]
ds_read_b128 v[192:195], v242 offset:22528
v_mfma_f32_16x16x32_bf16 v[140:143], v[196:199], a[124:127], v[140:143]
s_waitcnt lgkmcnt(3)
v_mfma_f32_16x16x32_bf16 v[144:147], v[200:203], a[64:67], v[176:179]
ds_read_b128 v[196:199], v242 offset:23552
buffer_load_dwordx4 v236, s[36:39], s9 offen offset:3072 sc1 lds
v_mfma_f32_16x16x32_bf16 v[152:155], v[200:203], a[68:71], v[180:183]
v_mfma_f32_16x16x32_bf16 v[144:147], v[204:207], a[72:75], v[144:147]
ds_read_b128 v[200:203], v242 offset:24576
v_mfma_f32_16x16x32_bf16 v[152:155], v[204:207], a[76:79], v[152:155]
v_mfma_f32_16x16x32_bf16 v[144:147], v[208:211], a[80:83], v[144:147]
ds_read_b128 v[204:207], v242 offset:25600
s_add_u32 s8, s8, 32768
s_cmp_eq_u32 s8, 163840
s_cselect_b32 s8, 0, s8
v_mfma_f32_16x16x32_bf16 v[152:155], v[208:211], a[84:87], v[152:155]
v_max3_f32 v186, v128, v129, v130
v_mfma_f32_16x16x32_bf16 v[144:147], v[212:215], a[88:91], v[144:147]
ds_read_b128 v[208:211], v242 offset:26624
v_mfma_f32_16x16x32_bf16 v[152:155], v[212:215], a[92:95], v[152:155]
s_waitcnt lgkmcnt(3)
v_mfma_f32_16x16x32_bf16 v[144:147], v[216:219], a[96:99], v[144:147]
ds_read_b128 v[212:215], v242 offset:27648
v_mfma_f32_16x16x32_bf16 v[152:155], v[216:219], a[100:103], v[152:155]
v_max3_f32 v186, v186, v131, v132
v_mfma_f32_16x16x32_bf16 v[144:147], v[220:223], a[104:107], v[144:147]
ds_read_b128 v[216:219], v242 offset:28672
v_mfma_f32_16x16x32_bf16 v[152:155], v[220:223], a[108:111], v[152:155]
v_mfma_f32_16x16x32_bf16 v[144:147], v[192:195], a[112:115], v[144:147]
ds_read_b128 v[220:223], v242 offset:29696
v_mfma_f32_16x16x32_bf16 v[152:155], v[192:195], a[116:119], v[152:155]
v_mfma_f32_16x16x32_bf16 v[144:147], v[196:199], a[120:123], v[144:147]
ds_read_b128 v[192:195], v242 offset:30720
s_add_u32 s7, s7, 32768
s_cmp_eq_u32 s7, 163840
s_cselect_b32 s7, 0, s7
v_mfma_f32_16x16x32_bf16 v[152:155], v[196:199], a[124:127], v[152:155]
s_waitcnt lgkmcnt(3)
v_mfma_f32_16x16x32_bf16 v[148:151], v[200:203], a[64:67], v[176:179]
ds_read_b128 v[196:199], v242 offset:31744
v_mfma_f32_16x16x32_bf16 v[156:159], v[200:203], a[68:71], v[180:183]
v_add_u32 v242, s7, v236
v_mfma_f32_16x16x32_bf16 v[148:151], v[204:207], a[72:75], v[148:151]
s_waitcnt vmcnt(16)
s_barrier
ds_read_b128 v[200:203], v242 offset:0
v_mfma_f32_16x16x32_bf16 v[156:159], v[204:207], a[76:79], v[156:159]
v_max3_f32 v186, v186, v133, v134
v_mfma_f32_16x16x32_bf16 v[148:151], v[208:211], a[80:83], v[148:151]
ds_read_b128 v[204:207], v242 offset:1024
v_mfma_f32_16x16x32_bf16 v[156:159], v[208:211], a[84:87], v[156:159]
v_max3_f32 v186, v186, v135, v136
v_mfma_f32_16x16x32_bf16 v[148:151], v[212:215], a[88:91], v[148:151]
ds_read_b128 v[208:211], v242 offset:2048
v_mfma_f32_16x16x32_bf16 v[156:159], v[212:215], a[92:95], v[156:159]
s_waitcnt lgkmcnt(3)
v_mfma_f32_16x16x32_bf16 v[148:151], v[216:219], a[96:99], v[148:151]
ds_read_b128 v[212:215], v242 offset:3072
v_mfma_f32_16x16x32_bf16 v[156:159], v[216:219], a[100:103], v[156:159]
v_max3_f32 v186, v186, v137, v138
v_mfma_f32_16x16x32_bf16 v[148:151], v[220:223], a[104:107], v[148:151]
ds_read_b128 v[216:219], v242 offset:4096
v_mfma_f32_16x16x32_bf16 v[156:159], v[220:223], a[108:111], v[156:159]
v_max3_f32 v186, v186, v139, v140
v_mfma_f32_16x16x32_bf16 v[148:151], v[192:195], a[112:115], v[148:151]
ds_read_b128 v[220:223], v242 offset:5120
v_mfma_f32_16x16x32_bf16 v[156:159], v[192:195], a[116:119], v[156:159]
v_max3_f32 v186, v186, v141, v142
v_mfma_f32_16x16x32_bf16 v[148:151], v[196:199], a[120:123], v[148:151]
ds_read_b128 v[192:195], v242 offset:6144
v_mfma_f32_16x16x32_bf16 v[156:159], v[196:199], a[124:127], v[156:159]
v_max_f32 v186, v186, v143
s_nop 7
s_bitcmp1_b32 s72, s6
s_cbranch_scc0 .Ls5
s_call_b64 s[24:25], .Lmask
.Ls5:
v_max3_f32 v186, v186, v144, v145
v_max3_f32 v186, v186, v146, v147
v_max3_f32 v186, v186, v148, v149
v_max3_f32 v186, v186, v150, v151
v_max3_f32 v186, v186, v152, v153
v_max3_f32 v186, v186, v154, v155
v_max3_f32 v186, v186, v156, v157
v_max3_f32 v186, v186, v158, v159
v_cmp_lt_f32 vcc, s13, v186
s_nop 0
s_cbranch_vccz .Ls6
s_call_b64 s[24:25], .Lslow
.Ls6:
v_exp_f32 v128, v128
v_exp_f32 v129, v129
v_add_f32 v226, v226, v128
v_exp_f32 v130, v130
v_add_f32 v226, v226, v129
v_exp_f32 v131, v131
v_add_f32 v226, v226, v130
v_exp_f32 v132, v132
v_add_f32 v226, v226, v131
v_exp_f32 v133, v133
)ASM"
R"ASM(
v_add_f32 v226, v226, v132
v_exp_f32 v134, v134
v_add_f32 v226, v226, v133
v_exp_f32 v135, v135
v_add_f32 v226, v226, v134
v_exp_f32 v136, v136
v_add_f32 v226, v226, v135
v_exp_f32 v137, v137
v_add_f32 v227, v227, v136
v_exp_f32 v138, v138
v_add_f32 v227, v227, v137
v_exp_f32 v139, v139
v_add_f32 v227, v227, v138
s_nop 0
v_add_f32 v227, v227, v139
v_exp_f32 v140, v140
v_cvt_pk_bf16_f32 v160, v128, v129
v_exp_f32 v141, v141
v_add_f32 v227, v227, v140
v_cvt_pk_bf16_f32 v161, v130, v131
v_exp_f32 v142, v142
v_add_f32 v227, v227, v141
v_cvt_pk_bf16_f32 v162, v132, v133
v_exp_f32 v143, v143
v_add_f32 v227, v227, v142
v_cvt_pk_bf16_f32 v163, v134, v135
v_exp_f32 v144, v144
v_add_f32 v227, v227, v143
v_cvt_pk_bf16_f32 v164, v136, v137
v_exp_f32 v145, v145
v_add_f32 v226, v226, v144
v_cvt_pk_bf16_f32 v165, v138, v139
v_exp_f32 v146, v146
v_add_f32 v226, v226, v145
v_cvt_pk_bf16_f32 v166, v140, v141
v_exp_f32 v147, v147
v_add_f32 v226, v226, v146
v_exp_f32 v148, v148
v_add_f32 v226, v226, v147
v_exp_f32 v149, v149
v_add_f32 v226, v226, v148
v_cvt_pk_bf16_f32 v167, v142, v143
v_add_f32 v226, v226, v149
s_bitcmp1_b32 s73, s6
s_cbranch_scc0 .Ls7
s_call_b64 s[24:25], .Lev2
.Ls7:
s_add_u32 s6, s6, 1
.p2align 3
.Lloop:
s_waitcnt lgkmcnt(3)
v_mfma_f32_16x16x32_bf16 v[128:131], v[200:203], a[64:67], v[176:179]
ds_read_b128 v[196:199], v242 offset:7168
v_exp_f32 v150, v150
v_mfma_f32_16x16x32_bf16 v[136:139], v[200:203], a[68:71], v[180:183]
v_exp_f32 v151, v151
v_mfma_f32_16x16x32_bf16 v[128:131], v[204:207], a[72:75], v[128:131]
ds_read_b128 v[200:203], v242 offset:8192
v_add_f32 v226, v226, v150
v_cvt_pk_bf16_f32 v168, v144, v145
v_mfma_f32_16x16x32_bf16 v[136:139], v[204:207], a[76:79], v[136:139]
v_exp_f32 v152, v152
v_add_f32 v226, v226, v151
v_mfma_f32_16x16x32_bf16 v[128:131], v[208:211], a[80:83], v[128:131]
ds_read_b128 v[204:207], v242 offset:9216
s_add_u32 s5, s5, 65536
s_add_u32 s9, s64, 32768
s_cmp_eq_u32 s6, s66
s_cselect_b32 s5, s9, s5
v_mfma_f32_16x16x32_bf16 v[136:139], v[208:211], a[84:87], v[136:139]
v_cvt_pk_bf16_f32 v169, v146, v147
v_exp_f32 v153, v153
v_mfma_f32_16x16x32_bf16 v[128:131], v[212:215], a[88:91], v[128:131]
ds_read_b128 v[208:211], v242 offset:10240
v_add_f32 v227, v227, v152
v_mfma_f32_16x16x32_bf16 v[136:139], v[212:215], a[92:95], v[136:139]
v_cvt_pk_bf16_f32 v170, v148, v149
s_waitcnt lgkmcnt(3)
v_mfma_f32_16x16x32_bf16 v[128:131], v[216:219], a[96:99], v[128:131]
ds_read_b128 v[212:215], v242 offset:11264
s_add_u32 m0, s8, s68
s_nop 0
buffer_load_dwordx4 v236, s[36:39], s5 offen offset:0 sc1 lds
v_mfma_f32_16x16x32_bf16 v[136:139], v[216:219], a[100:103], v[136:139]
v_exp_f32 v154, v154
v_add_f32 v227, v227, v153
v_mfma_f32_16x16x32_bf16 v[128:131], v[220:223], a[104:107], v[128:131]
ds_read_b128 v[216:219], v242 offset:12288
v_cvt_pk_bf16_f32 v171, v150, v151
v_mfma_f32_16x16x32_bf16 v[136:139], v[220:223], a[108:111], v[136:139]
v_exp_f32 v155, v155
v_mfma_f32_16x16x32_bf16 v[128:131], v[192:195], a[112:115], v[128:131]
ds_read_b128 v[220:223], v242 offset:13312
v_add_f32 v227, v227, v154
v_mfma_f32_16x16x32_bf16 v[136:139], v[192:195], a[116:119], v[136:139]
buffer_load_dwordx4 v236, s[36:39], s5 offen offset:1024 sc1 lds
v_mfma_f32_16x16x32_bf16 v[128:131], v[196:199], a[120:123], v[128:131]
ds_read_b128 v[192:195], v242 offset:14336
v_cvt_pk_bf16_f32 v172, v152, v153
v_mfma_f32_16x16x32_bf16 v[136:139], v[196:199], a[124:127], v[136:139]
v_exp_f32 v156, v156
s_waitcnt lgkmcnt(3)
v_mfma_f32_16x16x32_bf16 v[132:135], v[200:203], a[64:67], v[176:179]
ds_read_b128 v[196:199], v242 offset:15360
v_add_f32 v227, v227, v155
v_mfma_f32_16x16x32_bf16 v[140:143], v[200:203], a[68:71], v[180:183]
buffer_load_dwordx4 v236, s[36:39], s5 offen offset:2048 sc1 lds
v_mfma_f32_16x16x32_bf16 v[132:135], v[204:207], a[72:75], v[132:135]
ds_read_b128 v[200:203], v242 offset:16384
v_cvt_pk_bf16_f32 v173, v154, v155
v_mfma_f32_16x16x32_bf16 v[140:143], v[204:207], a[76:79], v[140:143]
v_exp_f32 v157, v157
v_mfma_f32_16x16x32_bf16 v[132:135], v[208:211], a[80:83], v[132:135]
ds_read_b128 v[204:207], v242 offset:17408
v_add_f32 v227, v227, v156
v_mfma_f32_16x16x32_bf16 v[140:143], v[208:211], a[84:87], v[140:143]
v_exp_f32 v158, v158
v_mfma_f32_16x16x32_bf16 v[132:135], v[212:215], a[88:91], v[132:135]
ds_read_b128 v[208:211], v242 offset:18432
buffer_load_dwordx4 v236, s[36:39], s5 offen offset:3072 sc1 lds
v_mfma_f32_16x16x32_bf16 v[140:143], v[212:215], a[92:95], v[140:143]
s_waitcnt lgkmcnt(3)
v_mfma_f32_16x16x32_bf16 v[132:135], v[216:219], a[96:99], v[132:135]
ds_read_b128 v[212:215], v242 offset:19456
v_add_f32 v227, v227, v157
v_mfma_f32_16x16x32_bf16 v[140:143], v[216:219], a[100:103], v[140:143]
v_exp_f32 v159, v159
v_mfma_f32_16x16x32_bf16 v[132:135], v[220:223], a[104:107], v[132:135]
ds_read_b128 v[216:219], v242 offset:20480
v_add_f32 v227, v227, v158
v_mfma_f32_16x16x32_bf16 v[140:143], v[220:223], a[108:111], v[140:143]
s_add_u32 m0, m0, 4096
s_add_u32 s9, s5, 4096
buffer_load_dwordx4 v236, s[36:39], s9 offen offset:0 sc1 lds
v_mfma_f32_16x16x32_bf16 v[132:135], v[192:195], a[112:115], v[132:135]
ds_read_b128 v[220:223], v242 offset:21504
v_cvt_pk_bf16_f32 v174, v156, v157
v_mfma_f32_16x16x32_bf16 v[140:143], v[192:195], a[116:119], v[140:143]
v_add_f32 v227, v227, v159
v_cvt_pk_bf16_f32 v175, v158, v159
v_mfma_f32_16x16x32_bf16 v[132:135], v[196:199], a[120:123], v[132:135]
ds_read_b128 v[192:195], v242 offset:22528
v_mfma_f32_16x16x32_bf16 v[140:143], v[196:199], a[124:127], v[140:143]
buffer_load_dwordx4 v236, s[36:39], s9 offen offset:1024 sc1 lds
s_waitcnt lgkmcnt(3)
v_mfma_f32_16x16x32_bf16 v[144:147], v[200:203], a[64:67], v[176:179]
ds_read_b128 v[196:199], v242 offset:23552
v_mfma_f32_16x16x32_bf16 v[152:155], v[200:203], a[68:71], v[180:183]
v_mfma_f32_16x16x32_bf16 v[144:147], v[204:207], a[72:75], v[144:147]
ds_read_b128 v[200:203], v242 offset:24576
v_mfma_f32_16x16x32_bf16 v[152:155], v[204:207], a[76:79], v[152:155]
v_mfma_f32_16x16x32_bf16 v[144:147], v[208:211], a[80:83], v[144:147]
ds_read_b128 v[204:207], v242 offset:25600
buffer_load_dwordx4 v236, s[36:39], s9 offen offset:2048 sc1 lds
v_mfma_f32_16x16x32_bf16 v[152:155], v[208:211], a[84:87], v[152:155]
v_mfma_f32_16x16x32_bf16 v[144:147], v[212:215], a[88:91], v[144:147]
ds_read_b128 v[208:211], v242 offset:26624
v_mfma_f32_16x16x32_bf16 v[152:155], v[212:215], a[92:95], v[152:155]
s_waitcnt lgkmcnt(3)
v_mfma_f32_16x16x32_bf16 v[144:147], v[216:219], a[96:99], v[144:147]
ds_read_b128 v[212:215], v242 offset:27648
buffer_load_dwordx4 v236, s[36:39], s9 offen offset:3072 sc1 lds
v_mfma_f32_16x16x32_bf16 v[152:155], v[216:219], a[100:103], v[152:155]
v_max3_f32 v186, v128, v129, v130
v_mfma_f32_16x16x32_bf16 v[144:147], v[220:223], a[104:107], v[144:147]
ds_read_b128 v[216:219], v242 offset:28672
v_mfma_f32_16x16x32_bf16 v[152:155], v[220:223], a[108:111], v[152:155]
v_max3_f32 v186, v186, v131, v132
v_mfma_f32_16x16x32_bf16 v[144:147], v[192:195], a[112:115], v[144:147]
ds_read_b128 v[220:223], v242 offset:29696
v_mfma_f32_16x16x32_bf16 v[152:155], v[192:195], a[116:119], v[152:155]
s_add_u32 s8, s8, 32768
s_cmp_eq_u32 s8, 163840
s_cselect_b32 s8, 0, s8
v_mfma_f32_16x16x32_bf16 v[144:147], v[196:199], a[120:123], v[144:147]
ds_read_b128 v[192:195], v242 offset:30720
s_add_u32 s7, s7, 32768
s_cmp_eq_u32 s7, 163840
s_cselect_b32 s7, 0, s7
v_mfma_f32_16x16x32_bf16 v[152:155], v[196:199], a[124:127], v[152:155]
s_waitcnt lgkmcnt(3)
v_mfma_f32_16x16x32_bf16 v[148:151], v[200:203], a[64:67], v[176:179]
ds_read_b128 v[196:199], v242 offset:31744
v_mfma_f32_16x16x32_bf16 v[156:159], v[200:203], a[68:71], v[180:183]
v_add_u32 v242, s7, v236
v_mfma_f32_16x16x32_bf16 v[148:151], v[204:207], a[72:75], v[148:151]
s_waitcnt vmcnt(16)
s_barrier
ds_read_b128 v[200:203], v242 offset:0
v_mfma_f32_16x16x32_bf16 v[156:159], v[204:207], a[76:79], v[156:159]
v_max3_f32 v186, v186, v133, v134
v_mfma_f32_16x16x32_bf16 v[148:151], v[208:211], a[80:83], v[148:151]
ds_read_b128 v[204:207], v242 offset:1024
v_mfma_f32_16x16x32_bf16 v[156:159], v[208:211], a[84:87], v[156:159]
v_max3_f32 v186, v186, v135, v136
v_mfma_f32_16x16x32_bf16 v[148:151], v[212:215], a[88:91], v[148:151]
ds_read_b128 v[208:211], v242 offset:2048
v_mfma_f32_16x16x32_bf16 v[156:159], v[212:215], a[92:95], v[156:159]
s_waitcnt lgkmcnt(3)
v_mfma_f32_16x16x32_bf16 v[148:151], v[216:219], a[96:99], v[148:151]
)ASM"
R"ASM(
ds_read_b128 v[212:215], v242 offset:3072
v_mfma_f32_16x16x32_bf16 v[156:159], v[216:219], a[100:103], v[156:159]
v_max3_f32 v186, v186, v137, v138
v_mfma_f32_16x16x32_bf16 v[148:151], v[220:223], a[104:107], v[148:151]
ds_read_b128 v[216:219], v242 offset:4096
v_mfma_f32_16x16x32_bf16 v[156:159], v[220:223], a[108:111], v[156:159]
v_max3_f32 v186, v186, v139, v140
v_mfma_f32_16x16x32_bf16 v[148:151], v[192:195], a[112:115], v[148:151]
ds_read_b128 v[220:223], v242 offset:5120
v_mfma_f32_16x16x32_bf16 v[156:159], v[192:195], a[116:119], v[156:159]
v_max3_f32 v186, v186, v141, v142
v_mfma_f32_16x16x32_bf16 v[148:151], v[196:199], a[120:123], v[148:151]
ds_read_b128 v[192:195], v242 offset:6144
v_mfma_f32_16x16x32_bf16 v[156:159], v[196:199], a[124:127], v[156:159]
v_max_f32 v186, v186, v143
s_waitcnt lgkmcnt(3)
v_mfma_f32_16x16x32_bf16 v[0:3], v[200:203], v[160:163], v[0:3]
ds_read_b128 v[196:199], v242 offset:7168
s_add_u32 s4, s4, 65536
s_add_u32 s9, s6, 2
s_cmp_eq_u32 s9, s66
s_cselect_b32 s4, s64, s4
v_mfma_f32_16x16x32_bf16 v[8:11], v[200:203], v[164:167], v[8:11]
v_mfma_f32_16x16x32_bf16 v[4:7], v[204:207], v[160:163], v[4:7]
ds_read_b128 v[200:203], v242 offset:8192
s_bitcmp1_b32 s72, s6
s_cbranch_scc0 .Ls8
s_call_b64 s[24:25], .Lmask
.Ls8:
v_mfma_f32_16x16x32_bf16 v[12:15], v[204:207], v[164:167], v[12:15]
v_mfma_f32_16x16x32_bf16 v[16:19], v[208:211], v[160:163], v[16:19]
ds_read_b128 v[204:207], v242 offset:9216
v_max3_f32 v186, v186, v144, v145
v_mfma_f32_16x16x32_bf16 v[24:27], v[208:211], v[164:167], v[24:27]
s_add_u32 m0, s8, s68
s_nop 0
buffer_load_dwordx4 v236, s[36:39], s4 offen offset:0 sc1 lds
v_mfma_f32_16x16x32_bf16 v[20:23], v[212:215], v[160:163], v[20:23]
ds_read_b128 v[208:211], v242 offset:10240
v_max3_f32 v186, v186, v146, v147
v_max3_f32 v186, v186, v148, v149
v_mfma_f32_16x16x32_bf16 v[28:31], v[212:215], v[164:167], v[28:31]
v_max3_f32 v186, v186, v150, v151
v_max3_f32 v186, v186, v152, v153
s_waitcnt lgkmcnt(3)
v_mfma_f32_16x16x32_bf16 v[32:35], v[216:219], v[160:163], v[32:35]
ds_read_b128 v[212:215], v242 offset:11264
v_max3_f32 v186, v186, v154, v155
v_max3_f32 v186, v186, v156, v157
v_mfma_f32_16x16x32_bf16 v[40:43], v[216:219], v[164:167], v[40:43]
v_max3_f32 v186, v186, v158, v159
v_cmp_lt_f32 vcc, s13, v186
v_mfma_f32_16x16x32_bf16 v[36:39], v[220:223], v[160:163], v[36:39]
ds_read_b128 v[216:219], v242 offset:12288
buffer_load_dwordx4 v236, s[36:39], s4 offen offset:1024 sc1 lds
s_nop 0
s_cbranch_vccz .Ls9
s_call_b64 s[24:25], .Lslow
.Ls9:
v_mfma_f32_16x16x32_bf16 v[44:47], v[220:223], v[164:167], v[44:47]
v_mfma_f32_16x16x32_bf16 v[48:51], v[192:195], v[160:163], v[48:51]
ds_read_b128 v[220:223], v242 offset:13312
v_exp_f32 v128, v128
v_mfma_f32_16x16x32_bf16 v[56:59], v[192:195], v[164:167], v[56:59]
v_exp_f32 v129, v129
v_add_f32 v226, v226, v128
v_mfma_f32_16x16x32_bf16 v[52:55], v[196:199], v[160:163], v[52:55]
ds_read_b128 v[192:195], v242 offset:14336
v_exp_f32 v130, v130
v_mfma_f32_16x16x32_bf16 v[60:63], v[196:199], v[164:167], v[60:63]
buffer_load_dwordx4 v236, s[36:39], s4 offen offset:2048 sc1 lds
s_waitcnt lgkmcnt(3)
v_mfma_f32_16x16x32_bf16 v[64:67], v[200:203], v[160:163], v[64:67]
ds_read_b128 v[196:199], v242 offset:15360
v_add_f32 v226, v226, v129
v_mfma_f32_16x16x32_bf16 v[72:75], v[200:203], v[164:167], v[72:75]
v_exp_f32 v131, v131
v_mfma_f32_16x16x32_bf16 v[68:71], v[204:207], v[160:163], v[68:71]
ds_read_b128 v[200:203], v242 offset:16384
v_add_f32 v226, v226, v130
v_mfma_f32_16x16x32_bf16 v[76:79], v[204:207], v[164:167], v[76:79]
v_exp_f32 v132, v132
v_add_f32 v226, v226, v131
v_mfma_f32_16x16x32_bf16 v[80:83], v[208:211], v[160:163], v[80:83]
ds_read_b128 v[204:207], v242 offset:17408
buffer_load_dwordx4 v236, s[36:39], s4 offen offset:3072 sc1 lds
v_mfma_f32_16x16x32_bf16 v[88:91], v[208:211], v[164:167], v[88:91]
v_exp_f32 v133, v133
v_add_f32 v226, v226, v132
v_mfma_f32_16x16x32_bf16 v[84:87], v[212:215], v[160:163], v[84:87]
ds_read_b128 v[208:211], v242 offset:18432
v_exp_f32 v134, v134
v_mfma_f32_16x16x32_bf16 v[92:95], v[212:215], v[164:167], v[92:95]
v_add_f32 v226, v226, v133
s_waitcnt lgkmcnt(3)
v_mfma_f32_16x16x32_bf16 v[96:99], v[216:219], v[160:163], v[96:99]
ds_read_b128 v[212:215], v242 offset:19456
v_exp_f32 v135, v135
v_mfma_f32_16x16x32_bf16 v[104:107], v[216:219], v[164:167], v[104:107]
s_add_u32 m0, m0, 4096
s_add_u32 s9, s4, 4096
buffer_load_dwordx4 v236, s[36:39], s9 offen offset:0 sc1 lds
v_mfma_f32_16x16x32_bf16 v[100:103], v[220:223], v[160:163], v[100:103]
ds_read_b128 v[216:219], v242 offset:20480
v_add_f32 v226, v226, v134
v_mfma_f32_16x16x32_bf16 v[108:111], v[220:223], v[164:167], v[108:111]
v_exp_f32 v136, v136
v_add_f32 v226, v226, v135
v_mfma_f32_16x16x32_bf16 v[112:115], v[192:195], v[160:163], v[112:115]
ds_read_b128 v[220:223], v242 offset:21504
v_exp_f32 v137, v137
v_mfma_f32_16x16x32_bf16 v[120:123], v[192:195], v[164:167], v[120:123]
v_add_f32 v227, v227, v136
v_exp_f32 v138, v138
v_mfma_f32_16x16x32_bf16 v[116:119], v[196:199], v[160:163], v[116:119]
ds_read_b128 v[192:195], v242 offset:22528
buffer_load_dwordx4 v236, s[36:39], s9 offen offset:1024 sc1 lds
v_mfma_f32_16x16x32_bf16 v[124:127], v[196:199], v[164:167], v[124:127]
v_add_f32 v227, v227, v137
s_waitcnt lgkmcnt(3)
v_mfma_f32_16x16x32_bf16 v[0:3], v[200:203], v[168:171], v[0:3]
ds_read_b128 v[196:199], v242 offset:23552
v_exp_f32 v139, v139
v_mfma_f32_16x16x32_bf16 v[8:11], v[200:203], v[172:175], v[8:11]
v_add_f32 v227, v227, v138
s_nop 0
v_add_f32 v227, v227, v139
v_mfma_f32_16x16x32_bf16 v[4:7], v[204:207], v[168:171], v[4:7]
ds_read_b128 v[200:203], v242 offset:24576
v_exp_f32 v140, v140
v_mfma_f32_16x16x32_bf16 v[12:15], v[204:207], v[172:175], v[12:15]
buffer_load_dwordx4 v236, s[36:39], s9 offen offset:2048 sc1 lds
v_mfma_f32_16x16x32_bf16 v[16:19], v[208:211], v[168:171], v[16:19]
ds_read_b128 v[204:207], v242 offset:25600
v_cvt_pk_bf16_f32 v160, v128, v129
v_mfma_f32_16x16x32_bf16 v[24:27], v[208:211], v[172:175], v[24:27]
v_exp_f32 v141, v141
v_mfma_f32_16x16x32_bf16 v[20:23], v[212:215], v[168:171], v[20:23]
ds_read_b128 v[208:211], v242 offset:26624
v_add_f32 v227, v227, v140
v_mfma_f32_16x16x32_bf16 v[28:31], v[212:215], v[172:175], v[28:31]
v_cvt_pk_bf16_f32 v161, v130, v131
s_waitcnt lgkmcnt(3)
v_mfma_f32_16x16x32_bf16 v[32:35], v[216:219], v[168:171], v[32:35]
ds_read_b128 v[212:215], v242 offset:27648
buffer_load_dwordx4 v236, s[36:39], s9 offen offset:3072 sc1 lds
v_mfma_f32_16x16x32_bf16 v[40:43], v[216:219], v[172:175], v[40:43]
v_exp_f32 v142, v142
v_mfma_f32_16x16x32_bf16 v[36:39], v[220:223], v[168:171], v[36:39]
ds_read_b128 v[216:219], v242 offset:28672
v_add_f32 v227, v227, v141
v_mfma_f32_16x16x32_bf16 v[44:47], v[220:223], v[172:175], v[44:47]
v_cvt_pk_bf16_f32 v162, v132, v133
v_exp_f32 v143, v143
v_mfma_f32_16x16x32_bf16 v[48:51], v[192:195], v[168:171], v[48:51]
ds_read_b128 v[220:223], v242 offset:29696
v_add_f32 v227, v227, v142
v_mfma_f32_16x16x32_bf16 v[56:59], v[192:195], v[172:175], v[56:59]
s_add_u32 s8, s8, 32768
s_cmp_eq_u32 s8, 163840
s_cselect_b32 s8, 0, s8
v_mfma_f32_16x16x32_bf16 v[52:55], v[196:199], v[168:171], v[52:55]
ds_read_b128 v[192:195], v242 offset:30720
s_add_u32 s7, s7, 32768
s_cmp_eq_u32 s7, 163840
s_cselect_b32 s7, 0, s7
v_mfma_f32_16x16x32_bf16 v[60:63], v[196:199], v[172:175], v[60:63]
s_waitcnt lgkmcnt(3)
v_mfma_f32_16x16x32_bf16 v[64:67], v[200:203], v[168:171], v[64:67]
ds_read_b128 v[196:199], v242 offset:31744
v_cvt_pk_bf16_f32 v163, v134, v135
v_mfma_f32_16x16x32_bf16 v[72:75], v[200:203], v[172:175], v[72:75]
v_add_u32 v242, s7, v236
v_exp_f32 v144, v144
v_mfma_f32_16x16x32_bf16 v[68:71], v[204:207], v[168:171], v[68:71]
s_waitcnt vmcnt(16)
s_barrier
ds_read_b128 v[200:203], v242 offset:0
v_mfma_f32_16x16x32_bf16 v[76:79], v[204:207], v[172:175], v[76:79]
v_add_f32 v227, v227, v143
v_cvt_pk_bf16_f32 v164, v136, v137
v_mfma_f32_16x16x32_bf16 v[80:83], v[208:211], v[168:171], v[80:83]
ds_read_b128 v[204:207], v242 offset:1024
v_exp_f32 v145, v145
v_mfma_f32_16x16x32_bf16 v[88:91], v[208:211], v[172:175], v[88:91]
v_add_f32 v226, v226, v144
v_cvt_pk_bf16_f32 v165, v138, v139
v_mfma_f32_16x16x32_bf16 v[84:87], v[212:215], v[168:171], v[84:87]
ds_read_b128 v[208:211], v242 offset:2048
v_exp_f32 v146, v146
v_mfma_f32_16x16x32_bf16 v[92:95], v[212:215], v[172:175], v[92:95]
v_add_f32 v226, v226, v145
s_waitcnt lgkmcnt(3)
v_mfma_f32_16x16x32_bf16 v[96:99], v[216:219], v[168:171], v[96:99]
ds_read_b128 v[212:215], v242 offset:3072
v_cvt_pk_bf16_f32 v166, v140, v141
v_mfma_f32_16x16x32_bf16 v[104:107], v[216:219], v[172:175], v[104:107]
v_exp_f32 v147, v147
v_mfma_f32_16x16x32_bf16 v[100:103], v[220:223], v[168:171], v[100:103]
ds_read_b128 v[216:219], v242 offset:4096
)ASM"
R"ASM(
v_add_f32 v226, v226, v146
v_mfma_f32_16x16x32_bf16 v[108:111], v[220:223], v[172:175], v[108:111]
v_exp_f32 v148, v148
v_mfma_f32_16x16x32_bf16 v[112:115], v[192:195], v[168:171], v[112:115]
ds_read_b128 v[220:223], v242 offset:5120
v_add_f32 v226, v226, v147
v_mfma_f32_16x16x32_bf16 v[120:123], v[192:195], v[172:175], v[120:123]
v_exp_f32 v149, v149
v_mfma_f32_16x16x32_bf16 v[116:119], v[196:199], v[168:171], v[116:119]
ds_read_b128 v[192:195], v242 offset:6144
v_add_f32 v226, v226, v148
v_mfma_f32_16x16x32_bf16 v[124:127], v[196:199], v[172:175], v[124:127]
v_cvt_pk_bf16_f32 v167, v142, v143
v_add_f32 v226, v226, v149
s_bitcmp1_b32 s73, s6
s_cbranch_scc0 .Ls10
s_call_b64 s[24:25], .Lev2
.Ls10:
s_add_u32 s6, s6, 1
s_cmp_lt_u32 s6, 15
s_cbranch_scc1 .Lloop
s_waitcnt lgkmcnt(3)
v_mfma_f32_16x16x32_bf16 v[128:131], v[200:203], a[64:67], v[176:179]
ds_read_b128 v[196:199], v242 offset:7168
v_exp_f32 v150, v150
v_mfma_f32_16x16x32_bf16 v[136:139], v[200:203], a[68:71], v[180:183]
v_exp_f32 v151, v151
v_mfma_f32_16x16x32_bf16 v[128:131], v[204:207], a[72:75], v[128:131]
ds_read_b128 v[200:203], v242 offset:8192
v_add_f32 v226, v226, v150
v_cvt_pk_bf16_f32 v168, v144, v145
v_mfma_f32_16x16x32_bf16 v[136:139], v[204:207], a[76:79], v[136:139]
v_exp_f32 v152, v152
v_add_f32 v226, v226, v151
v_mfma_f32_16x16x32_bf16 v[128:131], v[208:211], a[80:83], v[128:131]
ds_read_b128 v[204:207], v242 offset:9216
s_add_u32 s5, s5, 65536
s_add_u32 s9, s64, 32768
s_cmp_eq_u32 s6, s66
s_cselect_b32 s5, s9, s5
v_mfma_f32_16x16x32_bf16 v[136:139], v[208:211], a[84:87], v[136:139]
v_cvt_pk_bf16_f32 v169, v146, v147
v_exp_f32 v153, v153
v_mfma_f32_16x16x32_bf16 v[128:131], v[212:215], a[88:91], v[128:131]
ds_read_b128 v[208:211], v242 offset:10240
v_add_f32 v227, v227, v152
v_mfma_f32_16x16x32_bf16 v[136:139], v[212:215], a[92:95], v[136:139]
v_cvt_pk_bf16_f32 v170, v148, v149
s_waitcnt lgkmcnt(3)
v_mfma_f32_16x16x32_bf16 v[128:131], v[216:219], a[96:99], v[128:131]
ds_read_b128 v[212:215], v242 offset:11264
s_add_u32 m0, s8, s68
s_nop 0
buffer_load_dwordx4 v236, s[36:39], s5 offen offset:0 sc1 lds
v_mfma_f32_16x16x32_bf16 v[136:139], v[216:219], a[100:103], v[136:139]
v_exp_f32 v154, v154
v_add_f32 v227, v227, v153
v_mfma_f32_16x16x32_bf16 v[128:131], v[220:223], a[104:107], v[128:131]
ds_read_b128 v[216:219], v242 offset:12288
v_cvt_pk_bf16_f32 v171, v150, v151
v_mfma_f32_16x16x32_bf16 v[136:139], v[220:223], a[108:111], v[136:139]
v_exp_f32 v155, v155
v_mfma_f32_16x16x32_bf16 v[128:131], v[192:195], a[112:115], v[128:131]
ds_read_b128 v[220:223], v242 offset:13312
v_add_f32 v227, v227, v154
v_mfma_f32_16x16x32_bf16 v[136:139], v[192:195], a[116:119], v[136:139]
buffer_load_dwordx4 v236, s[36:39], s5 offen offset:1024 sc1 lds
v_mfma_f32_16x16x32_bf16 v[128:131], v[196:199], a[120:123], v[128:131]
ds_read_b128 v[192:195], v242 offset:14336
v_cvt_pk_bf16_f32 v172, v152, v153
v_mfma_f32_16x16x32_bf16 v[136:139], v[196:199], a[124:127], v[136:139]
v_exp_f32 v156, v156
s_waitcnt lgkmcnt(3)
v_mfma_f32_16x16x32_bf16 v[132:135], v[200:203], a[64:67], v[176:179]
ds_read_b128 v[196:199], v242 offset:15360
v_add_f32 v227, v227, v155
v_mfma_f32_16x16x32_bf16 v[140:143], v[200:203], a[68:71], v[180:183]
buffer_load_dwordx4 v236, s[36:39], s5 offen offset:2048 sc1 lds
v_mfma_f32_16x16x32_bf16 v[132:135], v[204:207], a[72:75], v[132:135]
ds_read_b128 v[200:203], v242 offset:16384
v_cvt_pk_bf16_f32 v173, v154, v155
v_mfma_f32_16x16x32_bf16 v[140:143], v[204:207], a[76:79], v[140:143]
v_exp_f32 v157, v157
v_mfma_f32_16x16x32_bf16 v[132:135], v[208:211], a[80:83], v[132:135]
ds_read_b128 v[204:207], v242 offset:17408
v_add_f32 v227, v227, v156
v_mfma_f32_16x16x32_bf16 v[140:143], v[208:211], a[84:87], v[140:143]
v_exp_f32 v158, v158
v_mfma_f32_16x16x32_bf16 v[132:135], v[212:215], a[88:91], v[132:135]
ds_read_b128 v[208:211], v242 offset:18432
buffer_load_dwordx4 v236, s[36:39], s5 offen offset:3072 sc1 lds
v_mfma_f32_16x16x32_bf16 v[140:143], v[212:215], a[92:95], v[140:143]
s_waitcnt lgkmcnt(3)
v_mfma_f32_16x16x32_bf16 v[132:135], v[216:219], a[96:99], v[132:135]
ds_read_b128 v[212:215], v242 offset:19456
v_add_f32 v227, v227, v157
v_mfma_f32_16x16x32_bf16 v[140:143], v[216:219], a[100:103], v[140:143]
v_exp_f32 v159, v159
v_mfma_f32_16x16x32_bf16 v[132:135], v[220:223], a[104:107], v[132:135]
ds_read_b128 v[216:219], v242 offset:20480
v_add_f32 v227, v227, v158
v_mfma_f32_16x16x32_bf16 v[140:143], v[220:223], a[108:111], v[140:143]
s_add_u32 m0, m0, 4096
s_add_u32 s9, s5, 4096
buffer_load_dwordx4 v236, s[36:39], s9 offen offset:0 sc1 lds
v_mfma_f32_16x16x32_bf16 v[132:135], v[192:195], a[112:115], v[132:135]
ds_read_b128 v[220:223], v242 offset:21504
v_cvt_pk_bf16_f32 v174, v156, v157
v_mfma_f32_16x16x32_bf16 v[140:143], v[192:195], a[116:119], v[140:143]
v_add_f32 v227, v227, v159
v_cvt_pk_bf16_f32 v175, v158, v159
v_mfma_f32_16x16x32_bf16 v[132:135], v[196:199], a[120:123], v[132:135]
ds_read_b128 v[192:195], v242 offset:22528
v_mfma_f32_16x16x32_bf16 v[140:143], v[196:199], a[124:127], v[140:143]
buffer_load_dwordx4 v236, s[36:39], s9 offen offset:1024 sc1 lds
s_waitcnt lgkmcnt(3)
v_mfma_f32_16x16x32_bf16 v[144:147], v[200:203], a[64:67], v[176:179]
ds_read_b128 v[196:199], v242 offset:23552
v_mfma_f32_16x16x32_bf16 v[152:155], v[200:203], a[68:71], v[180:183]
v_mfma_f32_16x16x32_bf16 v[144:147], v[204:207], a[72:75], v[144:147]
ds_read_b128 v[200:203], v242 offset:24576
v_mfma_f32_16x16x32_bf16 v[152:155], v[204:207], a[76:79], v[152:155]
v_mfma_f32_16x16x32_bf16 v[144:147], v[208:211], a[80:83], v[144:147]
ds_read_b128 v[204:207], v242 offset:25600
buffer_load_dwordx4 v236, s[36:39], s9 offen offset:2048 sc1 lds
v_mfma_f32_16x16x32_bf16 v[152:155], v[208:211], a[84:87], v[152:155]
v_mfma_f32_16x16x32_bf16 v[144:147], v[212:215], a[88:91], v[144:147]
ds_read_b128 v[208:211], v242 offset:26624
v_mfma_f32_16x16x32_bf16 v[152:155], v[212:215], a[92:95], v[152:155]
s_waitcnt lgkmcnt(3)
v_mfma_f32_16x16x32_bf16 v[144:147], v[216:219], a[96:99], v[144:147]
ds_read_b128 v[212:215], v242 offset:27648
buffer_load_dwordx4 v236, s[36:39], s9 offen offset:3072 sc1 lds
v_mfma_f32_16x16x32_bf16 v[152:155], v[216:219], a[100:103], v[152:155]
v_max3_f32 v186, v128, v129, v130
v_mfma_f32_16x16x32_bf16 v[144:147], v[220:223], a[104:107], v[144:147]
ds_read_b128 v[216:219], v242 offset:28672
v_mfma_f32_16x16x32_bf16 v[152:155], v[220:223], a[108:111], v[152:155]
v_max3_f32 v186, v186, v131, v132
v_mfma_f32_16x16x32_bf16 v[144:147], v[192:195], a[112:115], v[144:147]
ds_read_b128 v[220:223], v242 offset:29696
v_mfma_f32_16x16x32_bf16 v[152:155], v[192:195], a[116:119], v[152:155]
s_add_u32 s8, s8, 32768
s_cmp_eq_u32 s8, 163840
s_cselect_b32 s8, 0, s8
v_mfma_f32_16x16x32_bf16 v[144:147], v[196:199], a[120:123], v[144:147]
ds_read_b128 v[192:195], v242 offset:30720
s_add_u32 s7, s7, 32768
s_cmp_eq_u32 s7, 163840
s_cselect_b32 s7, 0, s7
v_mfma_f32_16x16x32_bf16 v[152:155], v[196:199], a[124:127], v[152:155]
s_waitcnt lgkmcnt(3)
v_mfma_f32_16x16x32_bf16 v[148:151], v[200:203], a[64:67], v[176:179]
ds_read_b128 v[196:199], v242 offset:31744
v_mfma_f32_16x16x32_bf16 v[156:159], v[200:203], a[68:71], v[180:183]
v_add_u32 v242, s7, v236
v_mfma_f32_16x16x32_bf16 v[148:151], v[204:207], a[72:75], v[148:151]
s_waitcnt vmcnt(16)
s_barrier
ds_read_b128 v[200:203], v242 offset:0
v_mfma_f32_16x16x32_bf16 v[156:159], v[204:207], a[76:79], v[156:159]
v_max3_f32 v186, v186, v133, v134
v_mfma_f32_16x16x32_bf16 v[148:151], v[208:211], a[80:83], v[148:151]
ds_read_b128 v[204:207], v242 offset:1024
v_mfma_f32_16x16x32_bf16 v[156:159], v[208:211], a[84:87], v[156:159]
v_max3_f32 v186, v186, v135, v136
v_mfma_f32_16x16x32_bf16 v[148:151], v[212:215], a[88:91], v[148:151]
ds_read_b128 v[208:211], v242 offset:2048
v_mfma_f32_16x16x32_bf16 v[156:159], v[212:215], a[92:95], v[156:159]
s_waitcnt lgkmcnt(3)
v_mfma_f32_16x16x32_bf16 v[148:151], v[216:219], a[96:99], v[148:151]
ds_read_b128 v[212:215], v242 offset:3072
v_mfma_f32_16x16x32_bf16 v[156:159], v[216:219], a[100:103], v[156:159]
v_max3_f32 v186, v186, v137, v138
v_mfma_f32_16x16x32_bf16 v[148:151], v[220:223], a[104:107], v[148:151]
ds_read_b128 v[216:219], v242 offset:4096
v_mfma_f32_16x16x32_bf16 v[156:159], v[220:223], a[108:111], v[156:159]
v_max3_f32 v186, v186, v139, v140
v_mfma_f32_16x16x32_bf16 v[148:151], v[192:195], a[112:115], v[148:151]
ds_read_b128 v[220:223], v242 offset:5120
v_mfma_f32_16x16x32_bf16 v[156:159], v[192:195], a[116:119], v[156:159]
v_max3_f32 v186, v186, v141, v142
v_mfma_f32_16x16x32_bf16 v[148:151], v[196:199], a[120:123], v[148:151]
ds_read_b128 v[192:195], v242 offset:6144
v_mfma_f32_16x16x32_bf16 v[156:159], v[196:199], a[124:127], v[156:159]
v_max_f32 v186, v186, v143
s_waitcnt lgkmcnt(3)
v_mfma_f32_16x16x32_bf16 v[0:3], v[200:203], v[160:163], v[0:3]
ds_read_b128 v[196:199], v242 offset:7168
s_add_u32 m0, s8, s71
s_mov_b32 s9, 0
buffer_load_dwordx4 v245, s[56:59], s9 offen offset:0 lds
v_mfma_f32_16x16x32_bf16 v[8:11], v[200:203], v[164:167], v[8:11]
v_mfma_f32_16x16x32_bf16 v[4:7], v[204:207], v[160:163], v[4:7]
ds_read_b128 v[200:203], v242 offset:8192
s_bitcmp1_b32 s72, s6
s_cbranch_scc0 .Ls11
s_call_b64 s[24:25], .Lmask
.Ls11:
v_mfma_f32_16x16x32_bf16 v[12:15], v[204:207], v[164:167], v[12:15]
)ASM"
R"ASM(
v_mfma_f32_16x16x32_bf16 v[16:19], v[208:211], v[160:163], v[16:19]
ds_read_b128 v[204:207], v242 offset:9216
v_max3_f32 v186, v186, v144, v145
v_mfma_f32_16x16x32_bf16 v[24:27], v[208:211], v[164:167], v[24:27]
v_max3_f32 v186, v186, v146, v147
v_max3_f32 v186, v186, v148, v149
v_mfma_f32_16x16x32_bf16 v[20:23], v[212:215], v[160:163], v[20:23]
ds_read_b128 v[208:211], v242 offset:10240
s_mov_b32 s9, 6144
buffer_load_dwordx4 v245, s[56:59], s9 offen offset:1024 lds
v_mfma_f32_16x16x32_bf16 v[28:31], v[212:215], v[164:167], v[28:31]
v_max3_f32 v186, v186, v150, v151
v_max3_f32 v186, v186, v152, v153
s_waitcnt lgkmcnt(3)
v_mfma_f32_16x16x32_bf16 v[32:35], v[216:219], v[160:163], v[32:35]
ds_read_b128 v[212:215], v242 offset:11264
v_max3_f32 v186, v186, v154, v155
v_max3_f32 v186, v186, v156, v157
v_mfma_f32_16x16x32_bf16 v[40:43], v[216:219], v[164:167], v[40:43]
v_max3_f32 v186, v186, v158, v159
v_cmp_lt_f32 vcc, s13, v186
v_mfma_f32_16x16x32_bf16 v[36:39], v[220:223], v[160:163], v[36:39]
ds_read_b128 v[216:219], v242 offset:12288
s_nop 0
s_cbranch_vccz .Ls12
s_call_b64 s[24:25], .Lslow
.Ls12:
v_mfma_f32_16x16x32_bf16 v[44:47], v[220:223], v[164:167], v[44:47]
v_mfma_f32_16x16x32_bf16 v[48:51], v[192:195], v[160:163], v[48:51]
ds_read_b128 v[220:223], v242 offset:13312
s_mov_b32 s9, 12288
buffer_load_dwordx4 v245, s[56:59], s9 offen offset:2048 lds
v_mfma_f32_16x16x32_bf16 v[56:59], v[192:195], v[164:167], v[56:59]
v_exp_f32 v128, v128
v_mfma_f32_16x16x32_bf16 v[52:55], v[196:199], v[160:163], v[52:55]
ds_read_b128 v[192:195], v242 offset:14336
v_exp_f32 v129, v129
v_mfma_f32_16x16x32_bf16 v[60:63], v[196:199], v[164:167], v[60:63]
v_add_f32 v226, v226, v128
s_waitcnt lgkmcnt(3)
v_mfma_f32_16x16x32_bf16 v[64:67], v[200:203], v[160:163], v[64:67]
ds_read_b128 v[196:199], v242 offset:15360
v_exp_f32 v130, v130
v_mfma_f32_16x16x32_bf16 v[72:75], v[200:203], v[164:167], v[72:75]
v_add_f32 v226, v226, v129
v_exp_f32 v131, v131
v_mfma_f32_16x16x32_bf16 v[68:71], v[204:207], v[160:163], v[68:71]
ds_read_b128 v[200:203], v242 offset:16384
s_mov_b32 s9, 18432
buffer_load_dwordx4 v245, s[56:59], s9 offen offset:3072 lds
v_mfma_f32_16x16x32_bf16 v[76:79], v[204:207], v[164:167], v[76:79]
v_add_f32 v226, v226, v130
v_exp_f32 v132, v132
v_mfma_f32_16x16x32_bf16 v[80:83], v[208:211], v[160:163], v[80:83]
ds_read_b128 v[204:207], v242 offset:17408
v_add_f32 v226, v226, v131
v_mfma_f32_16x16x32_bf16 v[88:91], v[208:211], v[164:167], v[88:91]
v_exp_f32 v133, v133
v_add_f32 v226, v226, v132
v_mfma_f32_16x16x32_bf16 v[84:87], v[212:215], v[160:163], v[84:87]
ds_read_b128 v[208:211], v242 offset:18432
v_exp_f32 v134, v134
v_mfma_f32_16x16x32_bf16 v[92:95], v[212:215], v[164:167], v[92:95]
v_add_f32 v226, v226, v133
s_waitcnt lgkmcnt(3)
v_mfma_f32_16x16x32_bf16 v[96:99], v[216:219], v[160:163], v[96:99]
ds_read_b128 v[212:215], v242 offset:19456
v_exp_f32 v135, v135
v_mfma_f32_16x16x32_bf16 v[104:107], v[216:219], v[164:167], v[104:107]
s_add_u32 m0, m0, 4096
s_mov_b32 s9, 28672
buffer_load_dwordx4 v245, s[56:59], s9 offen offset:0 lds
v_mfma_f32_16x16x32_bf16 v[100:103], v[220:223], v[160:163], v[100:103]
ds_read_b128 v[216:219], v242 offset:20480
v_add_f32 v226, v226, v134
v_mfma_f32_16x16x32_bf16 v[108:111], v[220:223], v[164:167], v[108:111]
v_exp_f32 v136, v136
v_add_f32 v226, v226, v135
v_mfma_f32_16x16x32_bf16 v[112:115], v[192:195], v[160:163], v[112:115]
ds_read_b128 v[220:223], v242 offset:21504
v_exp_f32 v137, v137
v_mfma_f32_16x16x32_bf16 v[120:123], v[192:195], v[164:167], v[120:123]
v_add_f32 v227, v227, v136
v_exp_f32 v138, v138
v_mfma_f32_16x16x32_bf16 v[116:119], v[196:199], v[160:163], v[116:119]
ds_read_b128 v[192:195], v242 offset:22528
v_add_f32 v227, v227, v137
v_mfma_f32_16x16x32_bf16 v[124:127], v[196:199], v[164:167], v[124:127]
s_mov_b32 s9, 34816
buffer_load_dwordx4 v245, s[56:59], s9 offen offset:1024 lds
s_waitcnt lgkmcnt(3)
v_mfma_f32_16x16x32_bf16 v[0:3], v[200:203], v[168:171], v[0:3]
ds_read_b128 v[196:199], v242 offset:23552
v_exp_f32 v139, v139
v_mfma_f32_16x16x32_bf16 v[8:11], v[200:203], v[172:175], v[8:11]
v_add_f32 v227, v227, v138
s_nop 0
v_add_f32 v227, v227, v139
v_mfma_f32_16x16x32_bf16 v[4:7], v[204:207], v[168:171], v[4:7]
ds_read_b128 v[200:203], v242 offset:24576
v_mfma_f32_16x16x32_bf16 v[12:15], v[204:207], v[172:175], v[12:15]
v_exp_f32 v140, v140
v_mfma_f32_16x16x32_bf16 v[16:19], v[208:211], v[168:171], v[16:19]
ds_read_b128 v[204:207], v242 offset:25600
v_cvt_pk_bf16_f32 v160, v128, v129
v_mfma_f32_16x16x32_bf16 v[24:27], v[208:211], v[172:175], v[24:27]
s_mov_b32 s9, 40960
buffer_load_dwordx4 v245, s[56:59], s9 offen offset:2048 lds
v_mfma_f32_16x16x32_bf16 v[20:23], v[212:215], v[168:171], v[20:23]
ds_read_b128 v[208:211], v242 offset:26624
v_exp_f32 v141, v141
v_mfma_f32_16x16x32_bf16 v[28:31], v[212:215], v[172:175], v[28:31]
v_add_f32 v227, v227, v140
s_waitcnt lgkmcnt(3)
v_mfma_f32_16x16x32_bf16 v[32:35], v[216:219], v[168:171], v[32:35]
ds_read_b128 v[212:215], v242 offset:27648
v_cvt_pk_bf16_f32 v161, v130, v131
v_mfma_f32_16x16x32_bf16 v[40:43], v[216:219], v[172:175], v[40:43]
v_exp_f32 v142, v142
v_mfma_f32_16x16x32_bf16 v[36:39], v[220:223], v[168:171], v[36:39]
ds_read_b128 v[216:219], v242 offset:28672
v_add_f32 v227, v227, v141
v_mfma_f32_16x16x32_bf16 v[44:47], v[220:223], v[172:175], v[44:47]
s_add_u32 s8, s8, 32768
s_cmp_eq_u32 s8, 163840
s_cselect_b32 s8, 0, s8
v_mfma_f32_16x16x32_bf16 v[48:51], v[192:195], v[168:171], v[48:51]
ds_read_b128 v[220:223], v242 offset:29696
v_cvt_pk_bf16_f32 v162, v132, v133
v_mfma_f32_16x16x32_bf16 v[56:59], v[192:195], v[172:175], v[56:59]
v_exp_f32 v143, v143
v_mfma_f32_16x16x32_bf16 v[52:55], v[196:199], v[168:171], v[52:55]
ds_read_b128 v[192:195], v242 offset:30720
s_add_u32 s7, s7, 32768
s_cmp_eq_u32 s7, 163840
s_cselect_b32 s7, 0, s7
v_mfma_f32_16x16x32_bf16 v[60:63], v[196:199], v[172:175], v[60:63]
v_add_f32 v227, v227, v142
s_waitcnt lgkmcnt(3)
v_mfma_f32_16x16x32_bf16 v[64:67], v[200:203], v[168:171], v[64:67]
ds_read_b128 v[196:199], v242 offset:31744
v_cvt_pk_bf16_f32 v163, v134, v135
v_mfma_f32_16x16x32_bf16 v[72:75], v[200:203], v[172:175], v[72:75]
v_add_u32 v242, s7, v236
v_exp_f32 v144, v144
v_mfma_f32_16x16x32_bf16 v[68:71], v[204:207], v[168:171], v[68:71]
s_waitcnt vmcnt(15)
s_barrier
ds_read_b128 v[200:203], v242 offset:0
v_mfma_f32_16x16x32_bf16 v[76:79], v[204:207], v[172:175], v[76:79]
v_add_f32 v227, v227, v143
v_cvt_pk_bf16_f32 v164, v136, v137
v_mfma_f32_16x16x32_bf16 v[80:83], v[208:211], v[168:171], v[80:83]
ds_read_b128 v[204:207], v242 offset:1024
v_exp_f32 v145, v145
v_mfma_f32_16x16x32_bf16 v[88:91], v[208:211], v[172:175], v[88:91]
v_add_f32 v226, v226, v144
v_cvt_pk_bf16_f32 v165, v138, v139
v_mfma_f32_16x16x32_bf16 v[84:87], v[212:215], v[168:171], v[84:87]
ds_read_b128 v[208:211], v242 offset:2048
v_exp_f32 v146, v146
v_mfma_f32_16x16x32_bf16 v[92:95], v[212:215], v[172:175], v[92:95]
v_add_f32 v226, v226, v145
s_waitcnt lgkmcnt(3)
v_mfma_f32_16x16x32_bf16 v[96:99], v[216:219], v[168:171], v[96:99]
ds_read_b128 v[212:215], v242 offset:3072
v_cvt_pk_bf16_f32 v166, v140, v141
v_mfma_f32_16x16x32_bf16 v[104:107], v[216:219], v[172:175], v[104:107]
v_exp_f32 v147, v147
v_mfma_f32_16x16x32_bf16 v[100:103], v[220:223], v[168:171], v[100:103]
ds_read_b128 v[216:219], v242 offset:4096
v_add_f32 v226, v226, v146
v_mfma_f32_16x16x32_bf16 v[108:111], v[220:223], v[172:175], v[108:111]
v_exp_f32 v148, v148
v_mfma_f32_16x16x32_bf16 v[112:115], v[192:195], v[168:171], v[112:115]
ds_read_b128 v[220:223], v242 offset:5120
v_add_f32 v226, v226, v147
v_mfma_f32_16x16x32_bf16 v[120:123], v[192:195], v[172:175], v[120:123]
v_exp_f32 v149, v149
v_mfma_f32_16x16x32_bf16 v[116:119], v[196:199], v[168:171], v[116:119]
ds_read_b128 v[192:195], v242 offset:6144
v_add_f32 v226, v226, v148
v_mfma_f32_16x16x32_bf16 v[124:127], v[196:199], v[172:175], v[124:127]
v_cvt_pk_bf16_f32 v167, v142, v143
v_add_f32 v226, v226, v149
s_bitcmp1_b32 s73, s6
s_cbranch_scc0 .Ls13
s_call_b64 s[24:25], .Lev2
.Ls13:
s_add_u32 s6, s6, 1
s_waitcnt lgkmcnt(3)
v_mfma_f32_16x16x32_bf16 v[128:131], v[200:203], a[64:67], v[176:179]
ds_read_b128 v[196:199], v242 offset:7168
v_exp_f32 v150, v150
v_mfma_f32_16x16x32_bf16 v[136:139], v[200:203], a[68:71], v[180:183]
v_exp_f32 v151, v151
v_mfma_f32_16x16x32_bf16 v[128:131], v[204:207], a[72:75], v[128:131]
ds_read_b128 v[200:203], v242 offset:8192
v_add_f32 v226, v226, v150
v_cvt_pk_bf16_f32 v168, v144, v145
)ASM"
R"ASM(
v_mfma_f32_16x16x32_bf16 v[136:139], v[204:207], a[76:79], v[136:139]
v_exp_f32 v152, v152
v_add_f32 v226, v226, v151
v_mfma_f32_16x16x32_bf16 v[128:131], v[208:211], a[80:83], v[128:131]
ds_read_b128 v[204:207], v242 offset:9216
s_add_u32 s5, s5, 65536
s_add_u32 s9, s64, 32768
s_cmp_eq_u32 s6, s66
s_cselect_b32 s5, s9, s5
v_mfma_f32_16x16x32_bf16 v[136:139], v[208:211], a[84:87], v[136:139]
v_cvt_pk_bf16_f32 v169, v146, v147
v_exp_f32 v153, v153
v_mfma_f32_16x16x32_bf16 v[128:131], v[212:215], a[88:91], v[128:131]
ds_read_b128 v[208:211], v242 offset:10240
v_add_f32 v227, v227, v152
v_mfma_f32_16x16x32_bf16 v[136:139], v[212:215], a[92:95], v[136:139]
v_cvt_pk_bf16_f32 v170, v148, v149
s_waitcnt lgkmcnt(3)
v_mfma_f32_16x16x32_bf16 v[128:131], v[216:219], a[96:99], v[128:131]
ds_read_b128 v[212:215], v242 offset:11264
s_add_u32 m0, s8, s68
s_nop 0
buffer_load_dwordx4 v236, s[36:39], s5 offen offset:0 sc1 lds
v_mfma_f32_16x16x32_bf16 v[136:139], v[216:219], a[100:103], v[136:139]
v_exp_f32 v154, v154
v_add_f32 v227, v227, v153
v_mfma_f32_16x16x32_bf16 v[128:131], v[220:223], a[104:107], v[128:131]
ds_read_b128 v[216:219], v242 offset:12288
v_cvt_pk_bf16_f32 v171, v150, v151
v_mfma_f32_16x16x32_bf16 v[136:139], v[220:223], a[108:111], v[136:139]
v_exp_f32 v155, v155
v_mfma_f32_16x16x32_bf16 v[128:131], v[192:195], a[112:115], v[128:131]
ds_read_b128 v[220:223], v242 offset:13312
v_add_f32 v227, v227, v154
v_mfma_f32_16x16x32_bf16 v[136:139], v[192:195], a[116:119], v[136:139]
buffer_load_dwordx4 v236, s[36:39], s5 offen offset:1024 sc1 lds
v_mfma_f32_16x16x32_bf16 v[128:131], v[196:199], a[120:123], v[128:131]
ds_read_b128 v[192:195], v242 offset:14336
v_cvt_pk_bf16_f32 v172, v152, v153
v_mfma_f32_16x16x32_bf16 v[136:139], v[196:199], a[124:127], v[136:139]
v_exp_f32 v156, v156
s_waitcnt lgkmcnt(3)
v_mfma_f32_16x16x32_bf16 v[132:135], v[200:203], a[64:67], v[176:179]
ds_read_b128 v[196:199], v242 offset:15360
v_add_f32 v227, v227, v155
v_mfma_f32_16x16x32_bf16 v[140:143], v[200:203], a[68:71], v[180:183]
buffer_load_dwordx4 v236, s[36:39], s5 offen offset:2048 sc1 lds
v_mfma_f32_16x16x32_bf16 v[132:135], v[204:207], a[72:75], v[132:135]
ds_read_b128 v[200:203], v242 offset:16384
v_cvt_pk_bf16_f32 v173, v154, v155
v_mfma_f32_16x16x32_bf16 v[140:143], v[204:207], a[76:79], v[140:143]
v_exp_f32 v157, v157
v_mfma_f32_16x16x32_bf16 v[132:135], v[208:211], a[80:83], v[132:135]
ds_read_b128 v[204:207], v242 offset:17408
v_add_f32 v227, v227, v156
v_mfma_f32_16x16x32_bf16 v[140:143], v[208:211], a[84:87], v[140:143]
v_exp_f32 v158, v158
v_mfma_f32_16x16x32_bf16 v[132:135], v[212:215], a[88:91], v[132:135]
ds_read_b128 v[208:211], v242 offset:18432
buffer_load_dwordx4 v236, s[36:39], s5 offen offset:3072 sc1 lds
v_mfma_f32_16x16x32_bf16 v[140:143], v[212:215], a[92:95], v[140:143]
s_waitcnt lgkmcnt(3)
v_mfma_f32_16x16x32_bf16 v[132:135], v[216:219], a[96:99], v[132:135]
ds_read_b128 v[212:215], v242 offset:19456
v_add_f32 v227, v227, v157
v_mfma_f32_16x16x32_bf16 v[140:143], v[216:219], a[100:103], v[140:143]
v_exp_f32 v159, v159
v_mfma_f32_16x16x32_bf16 v[132:135], v[220:223], a[104:107], v[132:135]
ds_read_b128 v[216:219], v242 offset:20480
v_add_f32 v227, v227, v158
v_mfma_f32_16x16x32_bf16 v[140:143], v[220:223], a[108:111], v[140:143]
s_add_u32 m0, m0, 4096
s_add_u32 s9, s5, 4096
buffer_load_dwordx4 v236, s[36:39], s9 offen offset:0 sc1 lds
v_mfma_f32_16x16x32_bf16 v[132:135], v[192:195], a[112:115], v[132:135]
ds_read_b128 v[220:223], v242 offset:21504
v_cvt_pk_bf16_f32 v174, v156, v157
v_mfma_f32_16x16x32_bf16 v[140:143], v[192:195], a[116:119], v[140:143]
v_add_f32 v227, v227, v159
v_cvt_pk_bf16_f32 v175, v158, v159
v_mfma_f32_16x16x32_bf16 v[132:135], v[196:199], a[120:123], v[132:135]
ds_read_b128 v[192:195], v242 offset:22528
v_mfma_f32_16x16x32_bf16 v[140:143], v[196:199], a[124:127], v[140:143]
buffer_load_dwordx4 v236, s[36:39], s9 offen offset:1024 sc1 lds
s_waitcnt lgkmcnt(3)
v_mfma_f32_16x16x32_bf16 v[144:147], v[200:203], a[64:67], v[176:179]
ds_read_b128 v[196:199], v242 offset:23552
v_mfma_f32_16x16x32_bf16 v[152:155], v[200:203], a[68:71], v[180:183]
v_mfma_f32_16x16x32_bf16 v[144:147], v[204:207], a[72:75], v[144:147]
ds_read_b128 v[200:203], v242 offset:24576
v_mfma_f32_16x16x32_bf16 v[152:155], v[204:207], a[76:79], v[152:155]
v_mfma_f32_16x16x32_bf16 v[144:147], v[208:211], a[80:83], v[144:147]
ds_read_b128 v[204:207], v242 offset:25600
buffer_load_dwordx4 v236, s[36:39], s9 offen offset:2048 sc1 lds
v_mfma_f32_16x16x32_bf16 v[152:155], v[208:211], a[84:87], v[152:155]
v_mfma_f32_16x16x32_bf16 v[144:147], v[212:215], a[88:91], v[144:147]
ds_read_b128 v[208:211], v242 offset:26624
v_mfma_f32_16x16x32_bf16 v[152:155], v[212:215], a[92:95], v[152:155]
s_waitcnt lgkmcnt(3)
v_mfma_f32_16x16x32_bf16 v[144:147], v[216:219], a[96:99], v[144:147]
ds_read_b128 v[212:215], v242 offset:27648
buffer_load_dwordx4 v236, s[36:39], s9 offen offset:3072 sc1 lds
v_mfma_f32_16x16x32_bf16 v[152:155], v[216:219], a[100:103], v[152:155]
v_max3_f32 v186, v128, v129, v130
v_mfma_f32_16x16x32_bf16 v[144:147], v[220:223], a[104:107], v[144:147]
ds_read_b128 v[216:219], v242 offset:28672
v_mfma_f32_16x16x32_bf16 v[152:155], v[220:223], a[108:111], v[152:155]
v_max3_f32 v186, v186, v131, v132
v_mfma_f32_16x16x32_bf16 v[144:147], v[192:195], a[112:115], v[144:147]
ds_read_b128 v[220:223], v242 offset:29696
v_mfma_f32_16x16x32_bf16 v[152:155], v[192:195], a[116:119], v[152:155]
s_add_u32 s8, s8, 32768
s_cmp_eq_u32 s8, 163840
s_cselect_b32 s8, 0, s8
v_mfma_f32_16x16x32_bf16 v[144:147], v[196:199], a[120:123], v[144:147]
ds_read_b128 v[192:195], v242 offset:30720
s_add_u32 s7, s7, 32768
s_cmp_eq_u32 s7, 163840
s_cselect_b32 s7, 0, s7
v_mfma_f32_16x16x32_bf16 v[152:155], v[196:199], a[124:127], v[152:155]
s_waitcnt lgkmcnt(3)
v_mfma_f32_16x16x32_bf16 v[148:151], v[200:203], a[64:67], v[176:179]
ds_read_b128 v[196:199], v242 offset:31744
v_mfma_f32_16x16x32_bf16 v[156:159], v[200:203], a[68:71], v[180:183]
v_add_u32 v242, s7, v236
v_mfma_f32_16x16x32_bf16 v[148:151], v[204:207], a[72:75], v[148:151]
s_waitcnt vmcnt(15)
s_barrier
ds_read_b128 v[200:203], v242 offset:0
v_mfma_f32_16x16x32_bf16 v[156:159], v[204:207], a[76:79], v[156:159]
v_max3_f32 v186, v186, v133, v134
v_mfma_f32_16x16x32_bf16 v[148:151], v[208:211], a[80:83], v[148:151]
ds_read_b128 v[204:207], v242 offset:1024
v_mfma_f32_16x16x32_bf16 v[156:159], v[208:211], a[84:87], v[156:159]
v_max3_f32 v186, v186, v135, v136
v_mfma_f32_16x16x32_bf16 v[148:151], v[212:215], a[88:91], v[148:151]
ds_read_b128 v[208:211], v242 offset:2048
v_mfma_f32_16x16x32_bf16 v[156:159], v[212:215], a[92:95], v[156:159]
s_waitcnt lgkmcnt(3)
v_mfma_f32_16x16x32_bf16 v[148:151], v[216:219], a[96:99], v[148:151]
ds_read_b128 v[212:215], v242 offset:3072
v_mfma_f32_16x16x32_bf16 v[156:159], v[216:219], a[100:103], v[156:159]
v_max3_f32 v186, v186, v137, v138
v_mfma_f32_16x16x32_bf16 v[148:151], v[220:223], a[104:107], v[148:151]
ds_read_b128 v[216:219], v242 offset:4096
v_mfma_f32_16x16x32_bf16 v[156:159], v[220:223], a[108:111], v[156:159]
v_max3_f32 v186, v186, v139, v140
v_mfma_f32_16x16x32_bf16 v[148:151], v[192:195], a[112:115], v[148:151]
ds_read_b128 v[220:223], v242 offset:5120
v_mfma_f32_16x16x32_bf16 v[156:159], v[192:195], a[116:119], v[156:159]
v_max3_f32 v186, v186, v141, v142
v_mfma_f32_16x16x32_bf16 v[148:151], v[196:199], a[120:123], v[148:151]
ds_read_b128 v[192:195], v242 offset:6144
v_mfma_f32_16x16x32_bf16 v[156:159], v[196:199], a[124:127], v[156:159]
v_max_f32 v186, v186, v143
s_waitcnt lgkmcnt(3)
v_mfma_f32_16x16x32_bf16 v[0:3], v[200:203], v[160:163], v[0:3]
ds_read_b128 v[196:199], v242 offset:7168
s_add_u32 m0, s8, s71
s_mov_b32 s9, 256
buffer_load_dwordx4 v245, s[56:59], s9 offen offset:0 lds
v_mfma_f32_16x16x32_bf16 v[8:11], v[200:203], v[164:167], v[8:11]
v_mfma_f32_16x16x32_bf16 v[4:7], v[204:207], v[160:163], v[4:7]
ds_read_b128 v[200:203], v242 offset:8192
s_bitcmp1_b32 s72, s6
s_cbranch_scc0 .Ls14
s_call_b64 s[24:25], .Lmask
.Ls14:
v_mfma_f32_16x16x32_bf16 v[12:15], v[204:207], v[164:167], v[12:15]
v_mfma_f32_16x16x32_bf16 v[16:19], v[208:211], v[160:163], v[16:19]
ds_read_b128 v[204:207], v242 offset:9216
v_max3_f32 v186, v186, v144, v145
v_mfma_f32_16x16x32_bf16 v[24:27], v[208:211], v[164:167], v[24:27]
v_max3_f32 v186, v186, v146, v147
v_max3_f32 v186, v186, v148, v149
v_mfma_f32_16x16x32_bf16 v[20:23], v[212:215], v[160:163], v[20:23]
ds_read_b128 v[208:211], v242 offset:10240
s_mov_b32 s9, 6400
buffer_load_dwordx4 v245, s[56:59], s9 offen offset:1024 lds
v_mfma_f32_16x16x32_bf16 v[28:31], v[212:215], v[164:167], v[28:31]
v_max3_f32 v186, v186, v150, v151
v_max3_f32 v186, v186, v152, v153
s_waitcnt lgkmcnt(3)
v_mfma_f32_16x16x32_bf16 v[32:35], v[216:219], v[160:163], v[32:35]
ds_read_b128 v[212:215], v242 offset:11264
v_max3_f32 v186, v186, v154, v155
v_max3_f32 v186, v186, v156, v157
v_mfma_f32_16x16x32_bf16 v[40:43], v[216:219], v[164:167], v[40:43]
v_max3_f32 v186, v186, v158, v159
v_cmp_lt_f32 vcc, s13, v186
v_mfma_f32_16x16x32_bf16 v[36:39], v[220:223], v[160:163], v[36:39]
ds_read_b128 v[216:219], v242 offset:12288
s_nop 0
s_cbranch_vccz .Ls15
s_call_b64 s[24:25], .Lslow
.Ls15:
v_mfma_f32_16x16x32_bf16 v[44:47], v[220:223], v[164:167], v[44:47]
v_mfma_f32_16x16x32_bf16 v[48:51], v[192:195], v[160:163], v[48:51]
ds_read_b128 v[220:223], v242 offset:13312
s_mov_b32 s9, 12544
)ASM"
R"ASM(
buffer_load_dwordx4 v245, s[56:59], s9 offen offset:2048 lds
v_mfma_f32_16x16x32_bf16 v[56:59], v[192:195], v[164:167], v[56:59]
v_exp_f32 v128, v128
v_mfma_f32_16x16x32_bf16 v[52:55], v[196:199], v[160:163], v[52:55]
ds_read_b128 v[192:195], v242 offset:14336
v_exp_f32 v129, v129
v_mfma_f32_16x16x32_bf16 v[60:63], v[196:199], v[164:167], v[60:63]
v_add_f32 v226, v226, v128
s_waitcnt lgkmcnt(3)
v_mfma_f32_16x16x32_bf16 v[64:67], v[200:203], v[160:163], v[64:67]
ds_read_b128 v[196:199], v242 offset:15360
v_exp_f32 v130, v130
v_mfma_f32_16x16x32_bf16 v[72:75], v[200:203], v[164:167], v[72:75]
v_add_f32 v226, v226, v129
v_exp_f32 v131, v131
v_mfma_f32_16x16x32_bf16 v[68:71], v[204:207], v[160:163], v[68:71]
ds_read_b128 v[200:203], v242 offset:16384
s_mov_b32 s9, 18688
buffer_load_dwordx4 v245, s[56:59], s9 offen offset:3072 lds
v_mfma_f32_16x16x32_bf16 v[76:79], v[204:207], v[164:167], v[76:79]
v_add_f32 v226, v226, v130
v_exp_f32 v132, v132
v_mfma_f32_16x16x32_bf16 v[80:83], v[208:211], v[160:163], v[80:83]
ds_read_b128 v[204:207], v242 offset:17408
v_add_f32 v226, v226, v131
v_mfma_f32_16x16x32_bf16 v[88:91], v[208:211], v[164:167], v[88:91]
v_exp_f32 v133, v133
v_add_f32 v226, v226, v132
v_mfma_f32_16x16x32_bf16 v[84:87], v[212:215], v[160:163], v[84:87]
ds_read_b128 v[208:211], v242 offset:18432
v_exp_f32 v134, v134
v_mfma_f32_16x16x32_bf16 v[92:95], v[212:215], v[164:167], v[92:95]
v_add_f32 v226, v226, v133
s_waitcnt lgkmcnt(3)
v_mfma_f32_16x16x32_bf16 v[96:99], v[216:219], v[160:163], v[96:99]
ds_read_b128 v[212:215], v242 offset:19456
v_exp_f32 v135, v135
v_mfma_f32_16x16x32_bf16 v[104:107], v[216:219], v[164:167], v[104:107]
s_add_u32 m0, m0, 4096
s_mov_b32 s9, 28928
buffer_load_dwordx4 v245, s[56:59], s9 offen offset:0 lds
v_mfma_f32_16x16x32_bf16 v[100:103], v[220:223], v[160:163], v[100:103]
ds_read_b128 v[216:219], v242 offset:20480
v_add_f32 v226, v226, v134
v_mfma_f32_16x16x32_bf16 v[108:111], v[220:223], v[164:167], v[108:111]
v_exp_f32 v136, v136
v_add_f32 v226, v226, v135
v_mfma_f32_16x16x32_bf16 v[112:115], v[192:195], v[160:163], v[112:115]
ds_read_b128 v[220:223], v242 offset:21504
v_exp_f32 v137, v137
v_mfma_f32_16x16x32_bf16 v[120:123], v[192:195], v[164:167], v[120:123]
v_add_f32 v227, v227, v136
v_exp_f32 v138, v138
v_mfma_f32_16x16x32_bf16 v[116:119], v[196:199], v[160:163], v[116:119]
ds_read_b128 v[192:195], v242 offset:22528
v_add_f32 v227, v227, v137
v_mfma_f32_16x16x32_bf16 v[124:127], v[196:199], v[164:167], v[124:127]
s_mov_b32 s9, 35072
buffer_load_dwordx4 v245, s[56:59], s9 offen offset:1024 lds
s_waitcnt lgkmcnt(3)
v_mfma_f32_16x16x32_bf16 v[0:3], v[200:203], v[168:171], v[0:3]
ds_read_b128 v[196:199], v242 offset:23552
v_exp_f32 v139, v139
v_mfma_f32_16x16x32_bf16 v[8:11], v[200:203], v[172:175], v[8:11]
v_add_f32 v227, v227, v138
s_nop 0
v_add_f32 v227, v227, v139
v_mfma_f32_16x16x32_bf16 v[4:7], v[204:207], v[168:171], v[4:7]
ds_read_b128 v[200:203], v242 offset:24576
v_exp_f32 v140, v140
v_mfma_f32_16x16x32_bf16 v[12:15], v[204:207], v[172:175], v[12:15]
v_cvt_pk_bf16_f32 v160, v128, v129
v_exp_f32 v141, v141
v_mfma_f32_16x16x32_bf16 v[16:19], v[208:211], v[168:171], v[16:19]
ds_read_b128 v[204:207], v242 offset:25600
v_add_f32 v227, v227, v140
v_cvt_pk_bf16_f32 v161, v130, v131
v_mfma_f32_16x16x32_bf16 v[24:27], v[208:211], v[172:175], v[24:27]
s_mov_b32 s9, 41216
buffer_load_dwordx4 v245, s[56:59], s9 offen offset:2048 lds
v_mfma_f32_16x16x32_bf16 v[20:23], v[212:215], v[168:171], v[20:23]
ds_read_b128 v[208:211], v242 offset:26624
v_exp_f32 v142, v142
v_mfma_f32_16x16x32_bf16 v[28:31], v[212:215], v[172:175], v[28:31]
v_add_f32 v227, v227, v141
s_waitcnt lgkmcnt(3)
v_mfma_f32_16x16x32_bf16 v[32:35], v[216:219], v[168:171], v[32:35]
ds_read_b128 v[212:215], v242 offset:27648
v_cvt_pk_bf16_f32 v162, v132, v133
v_mfma_f32_16x16x32_bf16 v[40:43], v[216:219], v[172:175], v[40:43]
v_exp_f32 v143, v143
v_mfma_f32_16x16x32_bf16 v[36:39], v[220:223], v[168:171], v[36:39]
ds_read_b128 v[216:219], v242 offset:28672
v_add_f32 v227, v227, v142
v_mfma_f32_16x16x32_bf16 v[44:47], v[220:223], v[172:175], v[44:47]
s_add_u32 s8, s8, 32768
s_cmp_eq_u32 s8, 163840
s_cselect_b32 s8, 0, s8
v_mfma_f32_16x16x32_bf16 v[48:51], v[192:195], v[168:171], v[48:51]
ds_read_b128 v[220:223], v242 offset:29696
v_cvt_pk_bf16_f32 v163, v134, v135
v_mfma_f32_16x16x32_bf16 v[56:59], v[192:195], v[172:175], v[56:59]
v_exp_f32 v144, v144
v_mfma_f32_16x16x32_bf16 v[52:55], v[196:199], v[168:171], v[52:55]
ds_read_b128 v[192:195], v242 offset:30720
s_add_u32 s7, s7, 32768
s_cmp_eq_u32 s7, 163840
s_cselect_b32 s7, 0, s7
v_mfma_f32_16x16x32_bf16 v[60:63], v[196:199], v[172:175], v[60:63]
v_add_f32 v227, v227, v143
s_waitcnt lgkmcnt(3)
v_mfma_f32_16x16x32_bf16 v[64:67], v[200:203], v[168:171], v[64:67]
ds_read_b128 v[196:199], v242 offset:31744
v_cvt_pk_bf16_f32 v164, v136, v137
v_mfma_f32_16x16x32_bf16 v[72:75], v[200:203], v[172:175], v[72:75]
v_add_u32 v242, s7, v232
v_add_u32 v243, s7, v233
v_add_u32 v248, s7, v234
v_add_u32 v249, s7, v235
v_mfma_f32_16x16x32_bf16 v[68:71], v[204:207], v[168:171], v[68:71]
s_waitcnt vmcnt(15)
s_barrier
ds_read_b64_tr_b16 v[200:201], v242 offset:0
v_mfma_f32_16x16x32_bf16 v[76:79], v[204:207], v[172:175], v[76:79]
ds_read_b64_tr_b16 v[202:203], v242 offset:2048
v_exp_f32 v145, v145
v_mfma_f32_16x16x32_bf16 v[80:83], v[208:211], v[168:171], v[80:83]
ds_read_b64_tr_b16 v[204:205], v243 offset:0
v_add_f32 v226, v226, v144
v_mfma_f32_16x16x32_bf16 v[88:91], v[208:211], v[172:175], v[88:91]
ds_read_b64_tr_b16 v[206:207], v243 offset:2048
v_cvt_pk_bf16_f32 v165, v138, v139
v_mfma_f32_16x16x32_bf16 v[84:87], v[212:215], v[168:171], v[84:87]
ds_read_b64_tr_b16 v[208:209], v248 offset:0
v_exp_f32 v146, v146
v_mfma_f32_16x16x32_bf16 v[92:95], v[212:215], v[172:175], v[92:95]
ds_read_b64_tr_b16 v[210:211], v248 offset:2048
s_waitcnt lgkmcnt(6)
v_mfma_f32_16x16x32_bf16 v[96:99], v[216:219], v[168:171], v[96:99]
ds_read_b64_tr_b16 v[212:213], v249 offset:0
v_add_f32 v226, v226, v145
v_cvt_pk_bf16_f32 v166, v140, v141
v_mfma_f32_16x16x32_bf16 v[104:107], v[216:219], v[172:175], v[104:107]
ds_read_b64_tr_b16 v[214:215], v249 offset:2048
v_exp_f32 v147, v147
v_mfma_f32_16x16x32_bf16 v[100:103], v[220:223], v[168:171], v[100:103]
ds_read_b64_tr_b16 v[216:217], v242 offset:4096
v_add_f32 v226, v226, v146
v_mfma_f32_16x16x32_bf16 v[108:111], v[220:223], v[172:175], v[108:111]
ds_read_b64_tr_b16 v[218:219], v242 offset:6144
v_exp_f32 v148, v148
v_mfma_f32_16x16x32_bf16 v[112:115], v[192:195], v[168:171], v[112:115]
ds_read_b64_tr_b16 v[220:221], v243 offset:4096
v_add_f32 v226, v226, v147
v_mfma_f32_16x16x32_bf16 v[120:123], v[192:195], v[172:175], v[120:123]
ds_read_b64_tr_b16 v[222:223], v243 offset:6144
v_exp_f32 v149, v149
v_mfma_f32_16x16x32_bf16 v[116:119], v[196:199], v[168:171], v[116:119]
ds_read_b64_tr_b16 v[192:193], v248 offset:4096
v_add_f32 v226, v226, v148
v_mfma_f32_16x16x32_bf16 v[124:127], v[196:199], v[172:175], v[124:127]
ds_read_b64_tr_b16 v[194:195], v248 offset:6144
v_cvt_pk_bf16_f32 v167, v142, v143
v_add_f32 v226, v226, v149
s_waitcnt vmcnt(15)
s_bitcmp1_b32 s73, s6
s_cbranch_scc0 .Ls16
s_call_b64 s[24:25], .Lev2
.Ls16:
s_add_u32 s6, s6, 1
s_waitcnt lgkmcnt(6)
v_mfma_f32_16x16x32_bf16 a[0:3], v[200:203], a[224:227], 0
ds_read_b64_tr_b16 v[196:197], v249 offset:4096
v_exp_f32 v150, v150
v_mfma_f32_16x16x32_bf16 a[8:11], v[200:203], a[228:231], 0
ds_read_b64_tr_b16 v[198:199], v249 offset:6144
v_exp_f32 v151, v151
v_mfma_f32_16x16x32_bf16 a[4:7], v[204:207], a[224:227], 0
ds_read_b64_tr_b16 v[200:201], v242 offset:8192
v_add_f32 v226, v226, v150
v_mfma_f32_16x16x32_bf16 a[12:15], v[204:207], a[228:231], 0
ds_read_b64_tr_b16 v[202:203], v242 offset:10240
v_cvt_pk_bf16_f32 v168, v144, v145
v_mfma_f32_16x16x32_bf16 a[16:19], v[208:211], a[224:227], 0
ds_read_b64_tr_b16 v[204:205], v243 offset:8192
v_exp_f32 v152, v152
v_mfma_f32_16x16x32_bf16 a[24:27], v[208:211], a[228:231], 0
ds_read_b64_tr_b16 v[206:207], v243 offset:10240
v_add_f32 v226, v226, v151
v_mfma_f32_16x16x32_bf16 a[20:23], v[212:215], a[224:227], 0
ds_read_b64_tr_b16 v[208:209], v248 offset:8192
v_cvt_pk_bf16_f32 v169, v146, v147
v_mfma_f32_16x16x32_bf16 a[28:31], v[212:215], a[228:231], 0
ds_read_b64_tr_b16 v[210:211], v248 offset:10240
s_waitcnt lgkmcnt(6)
v_mfma_f32_16x16x32_bf16 a[0:3], v[216:219], a[232:235], a[0:3]
ds_read_b64_tr_b16 v[212:213], v249 offset:8192
v_exp_f32 v153, v153
v_mfma_f32_16x16x32_bf16 a[8:11], v[216:219], a[236:239], a[8:11]
ds_read_b64_tr_b16 v[214:215], v249 offset:10240
)ASM"
R"ASM(
v_add_f32 v227, v227, v152
v_mfma_f32_16x16x32_bf16 a[4:7], v[220:223], a[232:235], a[4:7]
ds_read_b64_tr_b16 v[216:217], v242 offset:12288
v_cvt_pk_bf16_f32 v170, v148, v149
v_mfma_f32_16x16x32_bf16 a[12:15], v[220:223], a[236:239], a[12:15]
v_exp_f32 v154, v154
v_mfma_f32_16x16x32_bf16 a[16:19], v[192:195], a[232:235], a[16:19]
ds_read_b64_tr_b16 v[220:221], v243 offset:12288
v_add_f32 v227, v227, v153
v_mfma_f32_16x16x32_bf16 a[24:27], v[192:195], a[236:239], a[24:27]
v_cvt_pk_bf16_f32 v171, v150, v151
v_exp_f32 v155, v155
v_mfma_f32_16x16x32_bf16 a[20:23], v[196:199], a[232:235], a[20:23]
ds_read_b64_tr_b16 v[192:193], v248 offset:12288
v_add_f32 v227, v227, v154
v_mfma_f32_16x16x32_bf16 a[28:31], v[196:199], a[236:239], a[28:31]
v_cvt_pk_bf16_f32 v172, v152, v153
s_waitcnt lgkmcnt(3)
v_mfma_f32_16x16x32_bf16 a[0:3], v[200:203], a[240:243], a[0:3]
ds_read_b64_tr_b16 v[196:197], v249 offset:12288
v_exp_f32 v156, v156
v_mfma_f32_16x16x32_bf16 a[8:11], v[200:203], a[244:247], a[8:11]
v_add_f32 v227, v227, v155
v_cvt_pk_bf16_f32 v173, v154, v155
v_mfma_f32_16x16x32_bf16 a[4:7], v[204:207], a[240:243], a[4:7]
ds_read_b64_tr_b16 v[200:201], v242 offset:14336
v_exp_f32 v157, v157
v_mfma_f32_16x16x32_bf16 a[12:15], v[204:207], a[244:247], a[12:15]
ds_read_b64_tr_b16 v[202:203], v242 offset:16384
v_add_f32 v227, v227, v156
v_mfma_f32_16x16x32_bf16 a[16:19], v[208:211], a[240:243], a[16:19]
ds_read_b64_tr_b16 v[204:205], v243 offset:14336
v_exp_f32 v158, v158
v_mfma_f32_16x16x32_bf16 a[24:27], v[208:211], a[244:247], a[24:27]
ds_read_b64_tr_b16 v[206:207], v243 offset:16384
v_add_f32 v227, v227, v157
v_mfma_f32_16x16x32_bf16 a[20:23], v[212:215], a[240:243], a[20:23]
ds_read_b64_tr_b16 v[208:209], v248 offset:14336
v_exp_f32 v159, v159
v_mfma_f32_16x16x32_bf16 a[28:31], v[212:215], a[244:247], a[28:31]
ds_read_b64_tr_b16 v[210:211], v248 offset:16384
s_waitcnt lgkmcnt(6)
v_mfma_f32_16x16x16_bf16 a[0:3], v[216:217], a[248:249], a[0:3]
ds_read_b64_tr_b16 v[212:213], v249 offset:14336
s_add_u32 m0, s8, s71
s_mov_b32 s9, 512
buffer_load_dwordx4 v245, s[56:59], s9 offen offset:0 lds
v_mfma_f32_16x16x16_bf16 a[8:11], v[216:217], a[250:251], a[8:11]
ds_read_b64_tr_b16 v[214:215], v249 offset:16384
v_add_f32 v227, v227, v158
v_mfma_f32_16x16x16_bf16 a[4:7], v[220:221], a[248:249], a[4:7]
ds_read_b64_tr_b16 v[216:217], v242 offset:18432
v_cvt_pk_bf16_f32 v174, v156, v157
v_mfma_f32_16x16x16_bf16 a[12:15], v[220:221], a[250:251], a[12:15]
ds_read_b64_tr_b16 v[218:219], v242 offset:20480
s_mov_b32 s9, 6656
buffer_load_dwordx4 v245, s[56:59], s9 offen offset:1024 lds
v_mfma_f32_16x16x16_bf16 a[16:19], v[192:193], a[248:249], a[16:19]
ds_read_b64_tr_b16 v[220:221], v243 offset:18432
v_add_f32 v227, v227, v159
v_mfma_f32_16x16x16_bf16 a[24:27], v[192:193], a[250:251], a[24:27]
ds_read_b64_tr_b16 v[222:223], v243 offset:20480
v_cvt_pk_bf16_f32 v175, v158, v159
v_mfma_f32_16x16x16_bf16 a[20:23], v[196:197], a[248:249], a[20:23]
ds_read_b64_tr_b16 v[192:193], v248 offset:18432
s_mov_b32 s9, 12800
buffer_load_dwordx4 v245, s[56:59], s9 offen offset:2048 lds
v_mfma_f32_16x16x16_bf16 a[28:31], v[196:197], a[250:251], a[28:31]
ds_read_b64_tr_b16 v[194:195], v248 offset:20480
s_waitcnt lgkmcnt(6)
v_mfma_f32_16x16x32_bf16 a[32:35], v[200:203], a[224:227], 0
ds_read_b64_tr_b16 v[196:197], v249 offset:18432
v_mfma_f32_16x16x32_bf16 a[40:43], v[200:203], a[228:231], 0
ds_read_b64_tr_b16 v[198:199], v249 offset:20480
s_mov_b32 s9, 18944
buffer_load_dwordx4 v245, s[56:59], s9 offen offset:3072 lds
v_mfma_f32_16x16x32_bf16 a[36:39], v[204:207], a[224:227], 0
ds_read_b64_tr_b16 v[200:201], v242 offset:22528
v_mfma_f32_16x16x32_bf16 a[44:47], v[204:207], a[228:231], 0
ds_read_b64_tr_b16 v[202:203], v242 offset:24576
v_mfma_f32_16x16x32_bf16 a[48:51], v[208:211], a[224:227], 0
ds_read_b64_tr_b16 v[204:205], v243 offset:22528
s_add_u32 m0, m0, 4096
s_mov_b32 s9, 29184
buffer_load_dwordx4 v245, s[56:59], s9 offen offset:0 lds
v_mfma_f32_16x16x32_bf16 a[56:59], v[208:211], a[228:231], 0
ds_read_b64_tr_b16 v[206:207], v243 offset:24576
v_mfma_f32_16x16x32_bf16 a[52:55], v[212:215], a[224:227], 0
ds_read_b64_tr_b16 v[208:209], v248 offset:22528
v_mfma_f32_16x16x32_bf16 a[60:63], v[212:215], a[228:231], 0
ds_read_b64_tr_b16 v[210:211], v248 offset:24576
s_mov_b32 s9, 35328
buffer_load_dwordx4 v245, s[56:59], s9 offen offset:1024 lds
s_waitcnt lgkmcnt(6)
v_mfma_f32_16x16x32_bf16 a[32:35], v[216:219], a[232:235], a[32:35]
ds_read_b64_tr_b16 v[212:213], v249 offset:22528
v_mfma_f32_16x16x32_bf16 a[40:43], v[216:219], a[236:239], a[40:43]
ds_read_b64_tr_b16 v[214:215], v249 offset:24576
v_mfma_f32_16x16x32_bf16 a[36:39], v[220:223], a[232:235], a[36:39]
ds_read_b64_tr_b16 v[216:217], v242 offset:26624
s_mov_b32 s9, 41472
buffer_load_dwordx4 v245, s[56:59], s9 offen offset:2048 lds
v_mfma_f32_16x16x32_bf16 a[44:47], v[220:223], a[236:239], a[44:47]
v_mfma_f32_16x16x32_bf16 a[48:51], v[192:195], a[232:235], a[48:51]
ds_read_b64_tr_b16 v[220:221], v243 offset:26624
v_mfma_f32_16x16x32_bf16 a[56:59], v[192:195], a[236:239], a[56:59]
s_add_u32 s8, s8, 32768
s_cmp_eq_u32 s8, 163840
s_cselect_b32 s8, 0, s8
v_mfma_f32_16x16x32_bf16 a[52:55], v[196:199], a[232:235], a[52:55]
ds_read_b64_tr_b16 v[192:193], v248 offset:26624
s_add_u32 s7, s7, 32768
s_cmp_eq_u32 s7, 163840
s_cselect_b32 s7, 0, s7
v_mfma_f32_16x16x32_bf16 a[60:63], v[196:199], a[236:239], a[60:63]
s_waitcnt lgkmcnt(3)
v_mfma_f32_16x16x32_bf16 a[32:35], v[200:203], a[240:243], a[32:35]
ds_read_b64_tr_b16 v[196:197], v249 offset:26624
v_mfma_f32_16x16x32_bf16 a[40:43], v[200:203], a[244:247], a[40:43]
v_add_u32 v242, s7, v236
v_mfma_f32_16x16x32_bf16 a[36:39], v[204:207], a[240:243], a[36:39]
s_waitcnt vmcnt(14)
s_barrier
ds_read_b128 v[200:203], v242 offset:0
v_mfma_f32_16x16x32_bf16 a[44:47], v[204:207], a[244:247], a[44:47]
v_mfma_f32_16x16x32_bf16 a[48:51], v[208:211], a[240:243], a[48:51]
ds_read_b128 v[204:207], v242 offset:1024
v_mfma_f32_16x16x32_bf16 a[56:59], v[208:211], a[244:247], a[56:59]
v_mfma_f32_16x16x32_bf16 a[52:55], v[212:215], a[240:243], a[52:55]
ds_read_b128 v[208:211], v242 offset:2048
v_mfma_f32_16x16x32_bf16 a[60:63], v[212:215], a[244:247], a[60:63]
s_waitcnt lgkmcnt(3)
v_mfma_f32_16x16x16_bf16 a[32:35], v[216:217], a[248:249], a[32:35]
ds_read_b128 v[212:215], v242 offset:3072
v_mfma_f32_16x16x16_bf16 a[40:43], v[216:217], a[250:251], a[40:43]
v_mfma_f32_16x16x16_bf16 a[36:39], v[220:221], a[248:249], a[36:39]
ds_read_b128 v[216:219], v242 offset:4096
v_mfma_f32_16x16x16_bf16 a[44:47], v[220:221], a[250:251], a[44:47]
v_mfma_f32_16x16x16_bf16 a[48:51], v[192:193], a[248:249], a[48:51]
ds_read_b128 v[220:223], v242 offset:5120
v_mfma_f32_16x16x16_bf16 a[56:59], v[192:193], a[250:251], a[56:59]
v_mfma_f32_16x16x16_bf16 a[52:55], v[196:197], a[248:249], a[52:55]
ds_read_b128 v[192:195], v242 offset:6144
v_mfma_f32_16x16x16_bf16 a[60:63], v[196:197], a[250:251], a[60:63]
s_waitcnt lgkmcnt(3)
v_mfma_f32_16x16x32_bf16 v[0:3], v[200:203], v[160:163], v[0:3]
ds_read_b128 v[196:199], v242 offset:7168
v_mov_b32 v184, 0
buffer_load_dword v239, v184, s[36:39], s75 offen offset:0 sc1
v_mfma_f32_16x16x32_bf16 v[8:11], v[200:203], v[164:167], v[8:11]
v_mul_f32 v254, -1.0, v176
v_mfma_f32_16x16x32_bf16 v[4:7], v[204:207], v[160:163], v[4:7]
ds_read_b128 v[200:203], v242 offset:8192
v_mfma_f32_16x16x32_bf16 v[12:15], v[204:207], v[164:167], v[12:15]
s_add_u32 m0, s8, s71
s_mov_b32 s9, 768
buffer_load_dwordx4 v245, s[56:59], s9 offen offset:0 lds
v_mfma_f32_16x16x32_bf16 v[16:19], v[208:211], v[160:163], v[16:19]
ds_read_b128 v[204:207], v242 offset:9216
v_mfma_f32_16x16x32_bf16 v[24:27], v[208:211], v[164:167], v[24:27]
v_mul_f32 v255, -1.0, v180
v_mfma_f32_16x16x32_bf16 v[20:23], v[212:215], v[160:163], v[20:23]
ds_read_b128 v[208:211], v242 offset:10240
v_mfma_f32_16x16x32_bf16 v[28:31], v[212:215], v[164:167], v[28:31]
s_mov_b32 s9, 6912
buffer_load_dwordx4 v245, s[56:59], s9 offen offset:1024 lds
s_waitcnt lgkmcnt(3)
v_mfma_f32_16x16x32_bf16 v[32:35], v[216:219], v[160:163], v[32:35]
ds_read_b128 v[212:215], v242 offset:11264
v_mov_b32 v251, v226
v_mfma_f32_16x16x32_bf16 v[40:43], v[216:219], v[164:167], v[40:43]
s_nop 1
v_mfma_f32_16x16x32_bf16 v[36:39], v[220:223], v[160:163], v[36:39]
ds_read_b128 v[216:219], v242 offset:12288
s_mov_b32 s9, 13056
buffer_load_dwordx4 v245, s[56:59], s9 offen offset:2048 lds
v_mfma_f32_16x16x32_bf16 v[44:47], v[220:223], v[164:167], v[44:47]
v_permlane32_swap_b32 v251, v226
v_mfma_f32_16x16x32_bf16 v[48:51], v[192:195], v[160:163], v[48:51]
ds_read_b128 v[220:223], v242 offset:13312
v_mfma_f32_16x16x32_bf16 v[56:59], v[192:195], v[164:167], v[56:59]
s_nop 1
v_mfma_f32_16x16x32_bf16 v[52:55], v[196:199], v[160:163], v[52:55]
ds_read_b128 v[192:195], v242 offset:14336
s_mov_b32 s9, 19200
buffer_load_dwordx4 v245, s[56:59], s9 offen offset:3072 lds
v_mfma_f32_16x16x32_bf16 v[60:63], v[196:199], v[164:167], v[60:63]
s_waitcnt lgkmcnt(3)
v_mfma_f32_16x16x32_bf16 v[64:67], v[200:203], v[160:163], v[64:67]
ds_read_b128 v[196:199], v242 offset:15360
v_mfma_f32_16x16x32_bf16 v[72:75], v[200:203], v[164:167], v[72:75]
s_add_u32 m0, m0, 4096
s_mov_b32 s9, 29440
buffer_load_dwordx4 v245, s[56:59], s9 offen offset:0 lds
v_mfma_f32_16x16x32_bf16 v[68:71], v[204:207], v[160:163], v[68:71]
ds_read_b128 v[200:203], v242 offset:16384
v_mfma_f32_16x16x32_bf16 v[76:79], v[204:207], v[164:167], v[76:79]
v_add_f32 v226, v226, v251
v_mfma_f32_16x16x32_bf16 v[80:83], v[208:211], v[160:163], v[80:83]
ds_read_b128 v[204:207], v242 offset:17408
)ASM"
R"ASM(
v_mfma_f32_16x16x32_bf16 v[88:91], v[208:211], v[164:167], v[88:91]
s_mov_b32 s9, 35584
buffer_load_dwordx4 v245, s[56:59], s9 offen offset:1024 lds
v_mfma_f32_16x16x32_bf16 v[84:87], v[212:215], v[160:163], v[84:87]
ds_read_b128 v[208:211], v242 offset:18432
v_mfma_f32_16x16x32_bf16 v[92:95], v[212:215], v[164:167], v[92:95]
v_mov_b32 v251, v226
s_waitcnt lgkmcnt(3)
v_mfma_f32_16x16x32_bf16 v[96:99], v[216:219], v[160:163], v[96:99]
ds_read_b128 v[212:215], v242 offset:19456
s_mov_b32 s9, 41728
buffer_load_dwordx4 v245, s[56:59], s9 offen offset:2048 lds
v_mfma_f32_16x16x32_bf16 v[104:107], v[216:219], v[164:167], v[104:107]
s_nop 1
v_mfma_f32_16x16x32_bf16 v[100:103], v[220:223], v[160:163], v[100:103]
ds_read_b128 v[216:219], v242 offset:20480
v_mfma_f32_16x16x32_bf16 v[108:111], v[220:223], v[164:167], v[108:111]
v_permlane16_swap_b32 v251, v226
v_mfma_f32_16x16x32_bf16 v[112:115], v[192:195], v[160:163], v[112:115]
ds_read_b128 v[220:223], v242 offset:21504
s_add_u32 s8, s8, 32768
s_cmp_eq_u32 s8, 163840
s_cselect_b32 s8, 0, s8
v_mfma_f32_16x16x32_bf16 v[120:123], v[192:195], v[164:167], v[120:123]
s_nop 1
v_mfma_f32_16x16x32_bf16 v[116:119], v[196:199], v[160:163], v[116:119]
ds_read_b128 v[192:195], v242 offset:22528
v_mfma_f32_16x16x32_bf16 v[124:127], v[196:199], v[164:167], v[124:127]
s_waitcnt lgkmcnt(3)
v_mfma_f32_16x16x32_bf16 v[0:3], v[200:203], v[168:171], v[0:3]
ds_read_b128 v[196:199], v242 offset:23552
v_mfma_f32_16x16x32_bf16 v[8:11], v[200:203], v[172:175], v[8:11]
v_add_f32 v226, v226, v251
v_mfma_f32_16x16x32_bf16 v[4:7], v[204:207], v[168:171], v[4:7]
ds_read_b128 v[200:203], v242 offset:24576
v_mov_b32 v251, v227
v_mfma_f32_16x16x32_bf16 v[12:15], v[204:207], v[172:175], v[12:15]
s_nop 1
v_mfma_f32_16x16x32_bf16 v[16:19], v[208:211], v[168:171], v[16:19]
ds_read_b128 v[204:207], v242 offset:25600
v_permlane32_swap_b32 v251, v227
v_mfma_f32_16x16x32_bf16 v[24:27], v[208:211], v[172:175], v[24:27]
s_nop 1
v_mfma_f32_16x16x32_bf16 v[20:23], v[212:215], v[168:171], v[20:23]
ds_read_b128 v[208:211], v242 offset:26624
v_mfma_f32_16x16x32_bf16 v[28:31], v[212:215], v[172:175], v[28:31]
s_waitcnt lgkmcnt(3)
v_mfma_f32_16x16x32_bf16 v[32:35], v[216:219], v[168:171], v[32:35]
ds_read_b128 v[212:215], v242 offset:27648
v_mfma_f32_16x16x32_bf16 v[40:43], v[216:219], v[172:175], v[40:43]
v_add_f32 v227, v227, v251
v_mfma_f32_16x16x32_bf16 v[36:39], v[220:223], v[168:171], v[36:39]
ds_read_b128 v[216:219], v242 offset:28672
v_mov_b32 v251, v227
v_mfma_f32_16x16x32_bf16 v[44:47], v[220:223], v[172:175], v[44:47]
s_nop 1
v_mfma_f32_16x16x32_bf16 v[48:51], v[192:195], v[168:171], v[48:51]
ds_read_b128 v[220:223], v242 offset:29696
v_permlane16_swap_b32 v251, v227
v_mfma_f32_16x16x32_bf16 v[56:59], v[192:195], v[172:175], v[56:59]
s_nop 1
v_mfma_f32_16x16x32_bf16 v[52:55], v[196:199], v[168:171], v[52:55]
ds_read_b128 v[192:195], v242 offset:30720
s_add_u32 s7, s7, 32768
s_cmp_eq_u32 s7, 163840
s_cselect_b32 s7, 0, s7
v_mfma_f32_16x16x32_bf16 v[60:63], v[196:199], v[172:175], v[60:63]
s_waitcnt lgkmcnt(3)
v_mfma_f32_16x16x32_bf16 v[64:67], v[200:203], v[168:171], v[64:67]
ds_read_b128 v[196:199], v242 offset:31744
v_mfma_f32_16x16x32_bf16 v[72:75], v[200:203], v[172:175], v[72:75]
v_add_u32 v242, s7, v232
v_add_u32 v243, s7, v233
v_add_u32 v248, s7, v234
v_add_u32 v249, s7, v235
v_mfma_f32_16x16x32_bf16 v[68:71], v[204:207], v[168:171], v[68:71]
s_waitcnt vmcnt(15)
s_barrier
ds_read_b64_tr_b16 v[200:201], v242 offset:0
v_mfma_f32_16x16x32_bf16 v[76:79], v[204:207], v[172:175], v[76:79]
ds_read_b64_tr_b16 v[202:203], v242 offset:2048
v_mfma_f32_16x16x32_bf16 v[80:83], v[208:211], v[168:171], v[80:83]
ds_read_b64_tr_b16 v[204:205], v243 offset:0
v_mfma_f32_16x16x32_bf16 v[88:91], v[208:211], v[172:175], v[88:91]
ds_read_b64_tr_b16 v[206:207], v243 offset:2048
v_mfma_f32_16x16x32_bf16 v[84:87], v[212:215], v[168:171], v[84:87]
ds_read_b64_tr_b16 v[208:209], v248 offset:0
v_mfma_f32_16x16x32_bf16 v[92:95], v[212:215], v[172:175], v[92:95]
ds_read_b64_tr_b16 v[210:211], v248 offset:2048
s_waitcnt lgkmcnt(6)
v_mfma_f32_16x16x32_bf16 v[96:99], v[216:219], v[168:171], v[96:99]
ds_read_b64_tr_b16 v[212:213], v249 offset:0
v_mfma_f32_16x16x32_bf16 v[104:107], v[216:219], v[172:175], v[104:107]
ds_read_b64_tr_b16 v[214:215], v249 offset:2048
v_mfma_f32_16x16x32_bf16 v[100:103], v[220:223], v[168:171], v[100:103]
ds_read_b64_tr_b16 v[216:217], v242 offset:4096
v_mfma_f32_16x16x32_bf16 v[108:111], v[220:223], v[172:175], v[108:111]
ds_read_b64_tr_b16 v[218:219], v242 offset:6144
v_mfma_f32_16x16x32_bf16 v[112:115], v[192:195], v[168:171], v[112:115]
ds_read_b64_tr_b16 v[220:221], v243 offset:4096
v_mfma_f32_16x16x32_bf16 v[120:123], v[192:195], v[172:175], v[120:123]
ds_read_b64_tr_b16 v[222:223], v243 offset:6144
v_mfma_f32_16x16x32_bf16 v[116:119], v[196:199], v[168:171], v[116:119]
ds_read_b64_tr_b16 v[192:193], v248 offset:4096
v_mfma_f32_16x16x32_bf16 v[124:127], v[196:199], v[172:175], v[124:127]
ds_read_b64_tr_b16 v[194:195], v248 offset:6144
v_add_f32 v227, v227, v251
s_waitcnt vmcnt(7)
s_cmp_eq_u32 s81, 0
s_cbranch_scc1 .Lnopoll
s_mov_b32 s97, 0
.Lp17:
v_cmp_ne_u32 vcc, s29, v239
s_add_u32 s97, s97, 1
s_nop 2
s_cbranch_vccz .Lq17
s_cmp_ge_u32 s97, 400000
s_cbranch_scc1 .Lq17
s_sleep 1
buffer_load_dword v239, v184, s[36:39], s75 offen offset:0 sc1
s_waitcnt vmcnt(0)
s_branch .Lp17
.Lq17:
.Lnopoll:
s_waitcnt lgkmcnt(6)
v_mfma_f32_16x16x32_bf16 a[64:67], v[200:203], a[224:227], 0
ds_read_b64_tr_b16 v[196:197], v249 offset:4096
buffer_load_dwordx2 v[224:225], v236, s[60:63], s79 offen offset:0 sc1
v_mfma_f32_16x16x32_bf16 a[72:75], v[200:203], a[228:231], 0
ds_read_b64_tr_b16 v[198:199], v249 offset:6144
buffer_load_dwordx2 v[228:229], v236, s[60:63], s79 offen offset:8 sc1
v_mfma_f32_16x16x32_bf16 a[68:71], v[204:207], a[224:227], 0
ds_read_b64_tr_b16 v[200:201], v242 offset:8192
s_add_u32 s28, s78, 0
buffer_load_dwordx4 v[128:131], v236, s[60:63], s28 offen offset:0 sc1
v_mfma_f32_16x16x32_bf16 a[76:79], v[204:207], a[228:231], 0
ds_read_b64_tr_b16 v[202:203], v242 offset:10240
v_mfma_f32_16x16x32_bf16 a[80:83], v[208:211], a[224:227], 0
ds_read_b64_tr_b16 v[204:205], v243 offset:8192
buffer_load_dwordx4 v[132:135], v236, s[60:63], s28 offen offset:1024 sc1
v_mfma_f32_16x16x32_bf16 a[88:91], v[208:211], a[228:231], 0
ds_read_b64_tr_b16 v[206:207], v243 offset:10240
buffer_load_dwordx4 v[136:139], v236, s[60:63], s28 offen offset:2048 sc1
v_mfma_f32_16x16x32_bf16 a[84:87], v[212:215], a[224:227], 0
ds_read_b64_tr_b16 v[208:209], v248 offset:8192
buffer_load_dwordx4 v[140:143], v236, s[60:63], s28 offen offset:3072 sc1
v_mfma_f32_16x16x32_bf16 a[92:95], v[212:215], a[228:231], 0
ds_read_b64_tr_b16 v[210:211], v248 offset:10240
s_waitcnt lgkmcnt(6)
v_mfma_f32_16x16x32_bf16 a[64:67], v[216:219], a[232:235], a[64:67]
ds_read_b64_tr_b16 v[212:213], v249 offset:8192
s_add_u32 s28, s78, 4096
buffer_load_dwordx4 v[144:147], v236, s[60:63], s28 offen offset:0 sc1
v_mfma_f32_16x16x32_bf16 a[72:75], v[216:219], a[236:239], a[72:75]
ds_read_b64_tr_b16 v[214:215], v249 offset:10240
buffer_load_dwordx4 v[148:151], v236, s[60:63], s28 offen offset:1024 sc1
v_mfma_f32_16x16x32_bf16 a[68:71], v[220:223], a[232:235], a[68:71]
ds_read_b64_tr_b16 v[216:217], v242 offset:12288
buffer_load_dwordx4 v[152:155], v236, s[60:63], s28 offen offset:2048 sc1
v_mfma_f32_16x16x32_bf16 a[76:79], v[220:223], a[236:239], a[76:79]
v_mfma_f32_16x16x32_bf16 a[80:83], v[192:195], a[232:235], a[80:83]
ds_read_b64_tr_b16 v[220:221], v243 offset:12288
buffer_load_dwordx4 v[156:159], v236, s[60:63], s28 offen offset:3072 sc1
v_mfma_f32_16x16x32_bf16 a[88:91], v[192:195], a[236:239], a[88:91]
s_add_u32 s28, s78, 8192
buffer_load_dwordx4 v[160:163], v236, s[60:63], s28 offen offset:0 sc1
v_mfma_f32_16x16x32_bf16 a[84:87], v[196:199], a[232:235], a[84:87]
ds_read_b64_tr_b16 v[192:193], v248 offset:12288
buffer_load_dwordx4 v[164:167], v236, s[60:63], s28 offen offset:1024 sc1
v_mfma_f32_16x16x32_bf16 a[92:95], v[196:199], a[236:239], a[92:95]
s_waitcnt lgkmcnt(3)
v_mfma_f32_16x16x32_bf16 a[64:67], v[200:203], a[240:243], a[64:67]
ds_read_b64_tr_b16 v[196:197], v249 offset:12288
buffer_load_dwordx4 v[168:171], v236, s[60:63], s28 offen offset:2048 sc1
v_mfma_f32_16x16x32_bf16 a[72:75], v[200:203], a[244:247], a[72:75]
buffer_load_dwordx4 v[172:175], v236, s[60:63], s28 offen offset:3072 sc1
v_mfma_f32_16x16x32_bf16 a[68:71], v[204:207], a[240:243], a[68:71]
ds_read_b64_tr_b16 v[200:201], v242 offset:14336
s_add_u32 s28, s78, 12288
buffer_load_dwordx4 v[176:179], v236, s[60:63], s28 offen offset:0 sc1
v_mfma_f32_16x16x32_bf16 a[76:79], v[204:207], a[244:247], a[76:79]
ds_read_b64_tr_b16 v[202:203], v242 offset:16384
v_mfma_f32_16x16x32_bf16 a[80:83], v[208:211], a[240:243], a[80:83]
ds_read_b64_tr_b16 v[204:205], v243 offset:14336
buffer_load_dwordx4 v[180:183], v236, s[60:63], s28 offen offset:1024 sc1
v_mfma_f32_16x16x32_bf16 a[88:91], v[208:211], a[244:247], a[88:91]
ds_read_b64_tr_b16 v[206:207], v243 offset:16384
buffer_load_dwordx4 v[184:187], v236, s[60:63], s28 offen offset:2048 sc1
v_mfma_f32_16x16x32_bf16 a[84:87], v[212:215], a[240:243], a[84:87]
ds_read_b64_tr_b16 v[208:209], v248 offset:14336
buffer_load_dwordx4 v[188:191], v236, s[60:63], s28 offen offset:3072 sc1
v_mfma_f32_16x16x32_bf16 a[92:95], v[212:215], a[244:247], a[92:95]
ds_read_b64_tr_b16 v[210:211], v248 offset:16384
s_waitcnt lgkmcnt(6)
v_mfma_f32_16x16x16_bf16 a[64:67], v[216:217], a[248:249], a[64:67]
ds_read_b64_tr_b16 v[212:213], v249 offset:14336
s_add_u32 m0, s8, s68
s_mov_b32 s9, 0
buffer_load_dwordx4 v244, s[44:47], s9 offen offset:0 lds
v_mfma_f32_16x16x16_bf16 a[72:75], v[216:217], a[250:251], a[72:75]
)ASM"
R"ASM(
ds_read_b64_tr_b16 v[214:215], v249 offset:16384
v_mfma_f32_16x16x16_bf16 a[68:71], v[220:221], a[248:249], a[68:71]
ds_read_b64_tr_b16 v[216:217], v242 offset:18432
s_mov_b32 s9, 6144
buffer_load_dwordx4 v244, s[44:47], s9 offen offset:1024 lds
v_mfma_f32_16x16x16_bf16 a[76:79], v[220:221], a[250:251], a[76:79]
ds_read_b64_tr_b16 v[218:219], v242 offset:20480
v_mfma_f32_16x16x16_bf16 a[80:83], v[192:193], a[248:249], a[80:83]
ds_read_b64_tr_b16 v[220:221], v243 offset:18432
v_mfma_f32_16x16x16_bf16 a[88:91], v[192:193], a[250:251], a[88:91]
ds_read_b64_tr_b16 v[222:223], v243 offset:20480
s_mov_b32 s9, 12288
buffer_load_dwordx4 v244, s[44:47], s9 offen offset:2048 lds
v_mfma_f32_16x16x16_bf16 a[84:87], v[196:197], a[248:249], a[84:87]
ds_read_b64_tr_b16 v[192:193], v248 offset:18432
v_mfma_f32_16x16x16_bf16 a[92:95], v[196:197], a[250:251], a[92:95]
ds_read_b64_tr_b16 v[194:195], v248 offset:20480
s_waitcnt lgkmcnt(6)
v_mfma_f32_16x16x32_bf16 a[96:99], v[200:203], a[224:227], 0
ds_read_b64_tr_b16 v[196:197], v249 offset:18432
s_mov_b32 s9, 18432
buffer_load_dwordx4 v244, s[44:47], s9 offen offset:3072 lds
v_mfma_f32_16x16x32_bf16 a[104:107], v[200:203], a[228:231], 0
ds_read_b64_tr_b16 v[198:199], v249 offset:20480
v_mfma_f32_16x16x32_bf16 a[100:103], v[204:207], a[224:227], 0
ds_read_b64_tr_b16 v[200:201], v242 offset:22528
s_add_u32 m0, m0, 4096
s_mov_b32 s9, 28672
buffer_load_dwordx4 v244, s[44:47], s9 offen offset:0 lds
v_mfma_f32_16x16x32_bf16 a[108:111], v[204:207], a[228:231], 0
ds_read_b64_tr_b16 v[202:203], v242 offset:24576
v_mfma_f32_16x16x32_bf16 a[112:115], v[208:211], a[224:227], 0
ds_read_b64_tr_b16 v[204:205], v243 offset:22528
v_mfma_f32_16x16x32_bf16 a[120:123], v[208:211], a[228:231], 0
ds_read_b64_tr_b16 v[206:207], v243 offset:24576
s_mov_b32 s9, 34816
buffer_load_dwordx4 v244, s[44:47], s9 offen offset:1024 lds
v_mfma_f32_16x16x32_bf16 a[116:119], v[212:215], a[224:227], 0
ds_read_b64_tr_b16 v[208:209], v248 offset:22528
v_mfma_f32_16x16x32_bf16 a[124:127], v[212:215], a[228:231], 0
ds_read_b64_tr_b16 v[210:211], v248 offset:24576
s_waitcnt lgkmcnt(6)
v_mfma_f32_16x16x32_bf16 a[96:99], v[216:219], a[232:235], a[96:99]
ds_read_b64_tr_b16 v[212:213], v249 offset:22528
s_mov_b32 s9, 40960
buffer_load_dwordx4 v244, s[44:47], s9 offen offset:2048 lds
v_mfma_f32_16x16x32_bf16 a[104:107], v[216:219], a[236:239], a[104:107]
ds_read_b64_tr_b16 v[214:215], v249 offset:24576
v_mfma_f32_16x16x32_bf16 a[100:103], v[220:223], a[232:235], a[100:103]
ds_read_b64_tr_b16 v[216:217], v242 offset:26624
s_mov_b32 s9, 47104
buffer_load_dwordx4 v244, s[44:47], s9 offen offset:3072 lds
v_mfma_f32_16x16x32_bf16 a[108:111], v[220:223], a[236:239], a[108:111]
v_mfma_f32_16x16x32_bf16 a[112:115], v[192:195], a[232:235], a[112:115]
ds_read_b64_tr_b16 v[220:221], v243 offset:26624
v_mfma_f32_16x16x32_bf16 a[120:123], v[192:195], a[236:239], a[120:123]
s_add_u32 s8, s8, 32768
s_cmp_eq_u32 s8, 163840
s_cselect_b32 s8, 0, s8
v_mfma_f32_16x16x32_bf16 a[116:119], v[196:199], a[232:235], a[116:119]
ds_read_b64_tr_b16 v[192:193], v248 offset:26624
s_add_u32 s7, s7, 32768
s_cmp_eq_u32 s7, 163840
s_cselect_b32 s7, 0, s7
v_mfma_f32_16x16x32_bf16 a[124:127], v[196:199], a[236:239], a[124:127]
s_waitcnt lgkmcnt(3)
v_mfma_f32_16x16x32_bf16 a[96:99], v[200:203], a[240:243], a[96:99]
ds_read_b64_tr_b16 v[196:197], v249 offset:26624
v_mfma_f32_16x16x32_bf16 a[104:107], v[200:203], a[244:247], a[104:107]
v_add_u32 v242, s7, v232
v_add_u32 v243, s7, v233
v_add_u32 v248, s7, v234
v_add_u32 v249, s7, v235
v_mfma_f32_16x16x32_bf16 a[100:103], v[204:207], a[240:243], a[100:103]
s_barrier
ds_read_b64_tr_b16 v[200:201], v242 offset:0
v_mfma_f32_16x16x32_bf16 a[108:111], v[204:207], a[244:247], a[108:111]
ds_read_b64_tr_b16 v[202:203], v242 offset:2048
v_mfma_f32_16x16x32_bf16 a[112:115], v[208:211], a[240:243], a[112:115]
ds_read_b64_tr_b16 v[204:205], v243 offset:0
v_mfma_f32_16x16x32_bf16 a[120:123], v[208:211], a[244:247], a[120:123]
ds_read_b64_tr_b16 v[206:207], v243 offset:2048
v_mfma_f32_16x16x32_bf16 a[116:119], v[212:215], a[240:243], a[116:119]
ds_read_b64_tr_b16 v[208:209], v248 offset:0
v_mfma_f32_16x16x32_bf16 a[124:127], v[212:215], a[244:247], a[124:127]
ds_read_b64_tr_b16 v[210:211], v248 offset:2048
s_waitcnt lgkmcnt(6)
v_mfma_f32_16x16x16_bf16 a[96:99], v[216:217], a[248:249], a[96:99]
ds_read_b64_tr_b16 v[212:213], v249 offset:0
v_mfma_f32_16x16x16_bf16 a[104:107], v[216:217], a[250:251], a[104:107]
ds_read_b64_tr_b16 v[214:215], v249 offset:2048
v_mfma_f32_16x16x16_bf16 a[100:103], v[220:221], a[248:249], a[100:103]
ds_read_b64_tr_b16 v[216:217], v242 offset:4096
v_mfma_f32_16x16x16_bf16 a[108:111], v[220:221], a[250:251], a[108:111]
ds_read_b64_tr_b16 v[218:219], v242 offset:6144
v_mfma_f32_16x16x16_bf16 a[112:115], v[192:193], a[248:249], a[112:115]
ds_read_b64_tr_b16 v[220:221], v243 offset:4096
v_mfma_f32_16x16x16_bf16 a[120:123], v[192:193], a[250:251], a[120:123]
ds_read_b64_tr_b16 v[222:223], v243 offset:6144
v_mfma_f32_16x16x16_bf16 a[116:119], v[196:197], a[248:249], a[116:119]
ds_read_b64_tr_b16 v[192:193], v248 offset:4096
v_mfma_f32_16x16x16_bf16 a[124:127], v[196:197], a[250:251], a[124:127]
ds_read_b64_tr_b16 v[194:195], v248 offset:6144
s_waitcnt lgkmcnt(6)
v_mfma_f32_16x16x32_bf16 a[128:131], v[200:203], a[224:227], 0
ds_read_b64_tr_b16 v[196:197], v249 offset:4096
s_add_u32 m0, s8, s68
s_mov_b32 s9, 128
buffer_load_dwordx4 v244, s[44:47], s9 offen offset:0 lds
v_mfma_f32_16x16x32_bf16 a[136:139], v[200:203], a[228:231], 0
ds_read_b64_tr_b16 v[198:199], v249 offset:6144
s_waitcnt vmcnt(9)
v_cmp_lt_f32 vcc, 0, v228
s_nop 1
v_mfma_f32_16x16x32_bf16 a[132:135], v[204:207], a[224:227], 0
ds_read_b64_tr_b16 v[200:201], v242 offset:8192
v_cndmask_b32 v224, v254, v224, vcc
v_max_f32 v251, v254, v224
v_sub_f32 v230, v254, v251
v_sub_f32 v252, v224, v251
v_mfma_f32_16x16x32_bf16 a[140:143], v[204:207], a[228:231], 0
ds_read_b64_tr_b16 v[202:203], v242 offset:10240
v_exp_f32 v230, v230
v_exp_f32 v252, v252
v_mfma_f32_16x16x32_bf16 a[144:147], v[208:211], a[224:227], 0
ds_read_b64_tr_b16 v[204:205], v243 offset:8192
s_mov_b32 s9, 6272
buffer_load_dwordx4 v244, s[44:47], s9 offen offset:1024 lds
s_nop 0
v_mfma_f32_16x16x32_bf16 a[152:155], v[208:211], a[228:231], 0
ds_read_b64_tr_b16 v[206:207], v243 offset:10240
v_mul_f32 v251, v252, v228
v_fmac_f32 v251, v230, v226
v_rcp_f32 v251, v251
v_mfma_f32_16x16x32_bf16 a[148:151], v[212:215], a[224:227], 0
ds_read_b64_tr_b16 v[208:209], v248 offset:8192
s_nop 0
v_mul_f32 v251, v251, v240
v_mul_f32 v230, v230, v251
v_mul_f32 v252, v252, v251
v_mfma_f32_16x16x32_bf16 a[156:159], v[212:215], a[228:231], 0
ds_read_b64_tr_b16 v[210:211], v248 offset:10240
v_cmp_lt_f32 vcc, 0, v229
s_nop 1
s_waitcnt lgkmcnt(6)
v_mfma_f32_16x16x32_bf16 a[128:131], v[216:219], a[232:235], a[128:131]
ds_read_b64_tr_b16 v[212:213], v249 offset:8192
s_mov_b32 s9, 12416
buffer_load_dwordx4 v244, s[44:47], s9 offen offset:2048 lds
v_cndmask_b32 v225, v255, v225, vcc
v_mfma_f32_16x16x32_bf16 a[136:139], v[216:219], a[236:239], a[136:139]
ds_read_b64_tr_b16 v[214:215], v249 offset:10240
v_max_f32 v251, v255, v225
v_sub_f32 v231, v255, v251
v_sub_f32 v253, v225, v251
v_exp_f32 v231, v231
v_mfma_f32_16x16x32_bf16 a[132:135], v[220:223], a[232:235], a[132:135]
ds_read_b64_tr_b16 v[216:217], v242 offset:12288
v_exp_f32 v253, v253
s_nop 0
v_mul_f32 v251, v253, v229
v_mfma_f32_16x16x32_bf16 a[140:143], v[220:223], a[236:239], a[140:143]
v_fmac_f32 v251, v231, v227
v_rcp_f32 v251, v251
s_nop 0
v_mul_f32 v251, v251, v241
v_mfma_f32_16x16x32_bf16 a[144:147], v[192:195], a[232:235], a[144:147]
ds_read_b64_tr_b16 v[220:221], v243 offset:12288
v_mul_f32 v231, v231, v251
v_mul_f32 v253, v253, v251
v_lshlrev_b32 v226, 16, v128
v_and_b32 v227, 0xffff0000, v128
v_mfma_f32_16x16x32_bf16 a[152:155], v[192:195], a[236:239], a[152:155]
s_mov_b32 s9, 18560
buffer_load_dwordx4 v244, s[44:47], s9 offen offset:3072 lds
v_mul_f32 v0, v0, v230
v_mul_f32 v1, v1, v230
v_mfma_f32_16x16x32_bf16 a[148:151], v[196:199], a[232:235], a[148:151]
ds_read_b64_tr_b16 v[192:193], v248 offset:12288
v_fmac_f32 v0, v226, v252
v_fmac_f32 v1, v227, v252
v_cvt_pk_bf16_f32 v128, v0, v1
v_lshlrev_b32 v228, 16, v129
v_mfma_f32_16x16x32_bf16 a[156:159], v[196:199], a[236:239], a[156:159]
v_and_b32 v229, 0xffff0000, v129
v_mul_f32 v2, v2, v230
v_mul_f32 v3, v3, v230
v_fmac_f32 v2, v228, v252
s_waitcnt lgkmcnt(3)
v_mfma_f32_16x16x32_bf16 a[128:131], v[200:203], a[240:243], a[128:131]
ds_read_b64_tr_b16 v[196:197], v249 offset:12288
v_fmac_f32 v3, v229, v252
v_cvt_pk_bf16_f32 v129, v2, v3
v_lshlrev_b32 v226, 16, v130
v_and_b32 v227, 0xffff0000, v130
v_mfma_f32_16x16x32_bf16 a[136:139], v[200:203], a[244:247], a[136:139]
s_add_u32 m0, m0, 4096
s_mov_b32 s9, 28800
buffer_load_dwordx4 v244, s[44:47], s9 offen offset:0 lds
v_mul_f32 v4, v4, v230
)ASM"
R"ASM(
v_mfma_f32_16x16x32_bf16 a[132:135], v[204:207], a[240:243], a[132:135]
ds_read_b64_tr_b16 v[200:201], v242 offset:14336
v_mul_f32 v5, v5, v230
v_fmac_f32 v4, v226, v252
v_fmac_f32 v5, v227, v252
v_cvt_pk_bf16_f32 v130, v4, v5
v_mfma_f32_16x16x32_bf16 a[140:143], v[204:207], a[244:247], a[140:143]
ds_read_b64_tr_b16 v[202:203], v242 offset:16384
v_lshlrev_b32 v228, 16, v131
v_and_b32 v229, 0xffff0000, v131
v_mul_f32 v6, v6, v230
v_mul_f32 v7, v7, v230
v_mfma_f32_16x16x32_bf16 a[144:147], v[208:211], a[240:243], a[144:147]
ds_read_b64_tr_b16 v[204:205], v243 offset:14336
v_fmac_f32 v6, v228, v252
v_fmac_f32 v7, v229, v252
v_cvt_pk_bf16_f32 v131, v6, v7
v_lshlrev_b32 v226, 16, v132
v_mfma_f32_16x16x32_bf16 a[152:155], v[208:211], a[244:247], a[152:155]
ds_read_b64_tr_b16 v[206:207], v243 offset:16384
v_and_b32 v227, 0xffff0000, v132
v_mul_f32 v8, v8, v231
v_mul_f32 v9, v9, v231
v_fmac_f32 v8, v226, v253
v_mfma_f32_16x16x32_bf16 a[148:151], v[212:215], a[240:243], a[148:151]
ds_read_b64_tr_b16 v[208:209], v248 offset:14336
s_mov_b32 s9, 34944
buffer_load_dwordx4 v244, s[44:47], s9 offen offset:1024 lds
v_fmac_f32 v9, v227, v253
v_mfma_f32_16x16x32_bf16 a[156:159], v[212:215], a[244:247], a[156:159]
ds_read_b64_tr_b16 v[210:211], v248 offset:16384
v_cvt_pk_bf16_f32 v132, v8, v9
v_lshlrev_b32 v228, 16, v133
v_and_b32 v229, 0xffff0000, v133
s_waitcnt lgkmcnt(6)
v_mfma_f32_16x16x16_bf16 a[128:131], v[216:217], a[248:249], a[128:131]
ds_read_b64_tr_b16 v[212:213], v249 offset:14336
v_mul_f32 v10, v10, v231
v_mul_f32 v11, v11, v231
v_fmac_f32 v10, v228, v253
v_fmac_f32 v11, v229, v253
v_mfma_f32_16x16x16_bf16 a[136:139], v[216:217], a[250:251], a[136:139]
ds_read_b64_tr_b16 v[214:215], v249 offset:16384
v_cvt_pk_bf16_f32 v133, v10, v11
v_lshlrev_b32 v226, 16, v134
v_and_b32 v227, 0xffff0000, v134
v_mul_f32 v12, v12, v231
v_mfma_f32_16x16x16_bf16 a[132:135], v[220:221], a[248:249], a[132:135]
ds_read_b64_tr_b16 v[216:217], v242 offset:18432
s_mov_b32 s9, 41088
buffer_load_dwordx4 v244, s[44:47], s9 offen offset:2048 lds
v_mul_f32 v13, v13, v231
v_mfma_f32_16x16x16_bf16 a[140:143], v[220:221], a[250:251], a[140:143]
ds_read_b64_tr_b16 v[218:219], v242 offset:20480
v_fmac_f32 v12, v226, v253
v_fmac_f32 v13, v227, v253
v_cvt_pk_bf16_f32 v134, v12, v13
v_lshlrev_b32 v228, 16, v135
v_mfma_f32_16x16x16_bf16 a[144:147], v[192:193], a[248:249], a[144:147]
ds_read_b64_tr_b16 v[220:221], v243 offset:18432
v_and_b32 v229, 0xffff0000, v135
v_mul_f32 v14, v14, v231
v_mul_f32 v15, v15, v231
v_fmac_f32 v14, v228, v253
v_mfma_f32_16x16x16_bf16 a[152:155], v[192:193], a[250:251], a[152:155]
ds_read_b64_tr_b16 v[222:223], v243 offset:20480
v_fmac_f32 v15, v229, v253
v_cvt_pk_bf16_f32 v135, v14, v15
v_lshlrev_b32 v226, 16, v136
v_and_b32 v227, 0xffff0000, v136
v_mfma_f32_16x16x16_bf16 a[148:151], v[196:197], a[248:249], a[148:151]
ds_read_b64_tr_b16 v[192:193], v248 offset:18432
v_mul_f32 v16, v16, v230
v_mul_f32 v17, v17, v230
v_fmac_f32 v16, v226, v252
v_fmac_f32 v17, v227, v252
v_mfma_f32_16x16x16_bf16 a[156:159], v[196:197], a[250:251], a[156:159]
ds_read_b64_tr_b16 v[194:195], v248 offset:20480
s_mov_b32 s9, 47232
buffer_load_dwordx4 v244, s[44:47], s9 offen offset:3072 lds
s_waitcnt lgkmcnt(6)
v_mfma_f32_16x16x32_bf16 a[160:163], v[200:203], a[224:227], 0
ds_read_b64_tr_b16 v[196:197], v249 offset:18432
v_cvt_pk_bf16_f32 v136, v16, v17
v_lshlrev_b32 v228, 16, v137
v_and_b32 v229, 0xffff0000, v137
v_mul_f32 v18, v18, v230
v_mfma_f32_16x16x32_bf16 a[168:171], v[200:203], a[228:231], 0
ds_read_b64_tr_b16 v[198:199], v249 offset:20480
v_mul_f32 v19, v19, v230
v_fmac_f32 v18, v228, v252
v_fmac_f32 v19, v229, v252
v_cvt_pk_bf16_f32 v137, v18, v19
v_mfma_f32_16x16x32_bf16 a[164:167], v[204:207], a[224:227], 0
ds_read_b64_tr_b16 v[200:201], v242 offset:22528
v_lshlrev_b32 v226, 16, v138
v_and_b32 v227, 0xffff0000, v138
v_mul_f32 v20, v20, v230
v_mul_f32 v21, v21, v230
v_mfma_f32_16x16x32_bf16 a[172:175], v[204:207], a[228:231], 0
ds_read_b64_tr_b16 v[202:203], v242 offset:24576
s_add_u32 s8, s8, 32768
s_cmp_eq_u32 s8, 163840
s_cselect_b32 s8, 0, s8
v_fmac_f32 v20, v226, v252
v_mfma_f32_16x16x32_bf16 a[176:179], v[208:211], a[224:227], 0
ds_read_b64_tr_b16 v[204:205], v243 offset:22528
v_fmac_f32 v21, v227, v252
v_cvt_pk_bf16_f32 v138, v20, v21
v_lshlrev_b32 v228, 16, v139
v_and_b32 v229, 0xffff0000, v139
v_mfma_f32_16x16x32_bf16 a[184:187], v[208:211], a[228:231], 0
ds_read_b64_tr_b16 v[206:207], v243 offset:24576
v_mul_f32 v22, v22, v230
v_mul_f32 v23, v23, v230
v_fmac_f32 v22, v228, v252
v_fmac_f32 v23, v229, v252
v_mfma_f32_16x16x32_bf16 a[180:183], v[212:215], a[224:227], 0
ds_read_b64_tr_b16 v[208:209], v248 offset:22528
v_cvt_pk_bf16_f32 v139, v22, v23
v_lshlrev_b32 v226, 16, v140
v_and_b32 v227, 0xffff0000, v140
v_mul_f32 v24, v24, v231
v_mfma_f32_16x16x32_bf16 a[188:191], v[212:215], a[228:231], 0
ds_read_b64_tr_b16 v[210:211], v248 offset:24576
v_mul_f32 v25, v25, v231
v_fmac_f32 v24, v226, v253
v_fmac_f32 v25, v227, v253
s_waitcnt lgkmcnt(6)
v_mfma_f32_16x16x32_bf16 a[160:163], v[216:219], a[232:235], a[160:163]
ds_read_b64_tr_b16 v[212:213], v249 offset:22528
v_cvt_pk_bf16_f32 v140, v24, v25
v_lshlrev_b32 v228, 16, v141
v_and_b32 v229, 0xffff0000, v141
v_mul_f32 v26, v26, v231
v_mfma_f32_16x16x32_bf16 a[168:171], v[216:219], a[236:239], a[168:171]
ds_read_b64_tr_b16 v[214:215], v249 offset:24576
v_mul_f32 v27, v27, v231
v_fmac_f32 v26, v228, v253
v_fmac_f32 v27, v229, v253
v_cvt_pk_bf16_f32 v141, v26, v27
v_mfma_f32_16x16x32_bf16 a[164:167], v[220:223], a[232:235], a[164:167]
ds_read_b64_tr_b16 v[216:217], v242 offset:26624
v_lshlrev_b32 v226, 16, v142
v_and_b32 v227, 0xffff0000, v142
v_mul_f32 v28, v28, v231
v_mul_f32 v29, v29, v231
v_mfma_f32_16x16x32_bf16 a[172:175], v[220:223], a[236:239], a[172:175]
v_fmac_f32 v28, v226, v253
v_fmac_f32 v29, v227, v253
v_cvt_pk_bf16_f32 v142, v28, v29
v_lshlrev_b32 v228, 16, v143
v_and_b32 v229, 0xffff0000, v143
v_mfma_f32_16x16x32_bf16 a[176:179], v[192:195], a[232:235], a[176:179]
ds_read_b64_tr_b16 v[220:221], v243 offset:26624
v_mul_f32 v30, v30, v231
v_mul_f32 v31, v31, v231
v_fmac_f32 v30, v228, v253
v_fmac_f32 v31, v229, v253
v_mfma_f32_16x16x32_bf16 a[184:187], v[192:195], a[236:239], a[184:187]
v_cvt_pk_bf16_f32 v143, v30, v31
v_lshlrev_b32 v226, 16, v144
v_and_b32 v227, 0xffff0000, v144
v_mul_f32 v32, v32, v230
v_mul_f32 v33, v33, v230
v_mfma_f32_16x16x32_bf16 a[180:183], v[196:199], a[232:235], a[180:183]
ds_read_b64_tr_b16 v[192:193], v248 offset:26624
s_add_u32 s7, s7, 32768
s_cmp_eq_u32 s7, 163840
s_cselect_b32 s7, 0, s7
v_fmac_f32 v32, v226, v252
v_mfma_f32_16x16x32_bf16 a[188:191], v[196:199], a[236:239], a[188:191]
v_fmac_f32 v33, v227, v252
v_cvt_pk_bf16_f32 v144, v32, v33
v_lshlrev_b32 v228, 16, v145
v_and_b32 v229, 0xffff0000, v145
s_waitcnt lgkmcnt(3)
v_mfma_f32_16x16x32_bf16 a[160:163], v[200:203], a[240:243], a[160:163]
ds_read_b64_tr_b16 v[196:197], v249 offset:26624
v_mul_f32 v34, v34, v230
v_mul_f32 v35, v35, v230
v_fmac_f32 v34, v228, v252
v_fmac_f32 v35, v229, v252
v_mfma_f32_16x16x32_bf16 a[168:171], v[200:203], a[244:247], a[168:171]
v_add_u32 v242, s7, v232
v_add_u32 v243, s7, v233
v_add_u32 v248, s7, v234
v_add_u32 v249, s7, v235
v_cvt_pk_bf16_f32 v145, v34, v35
v_mfma_f32_16x16x32_bf16 a[164:167], v[204:207], a[240:243], a[164:167]
s_barrier
ds_read_b64_tr_b16 v[200:201], v242 offset:0
v_lshlrev_b32 v226, 16, v146
v_and_b32 v227, 0xffff0000, v146
v_mfma_f32_16x16x32_bf16 a[172:175], v[204:207], a[244:247], a[172:175]
ds_read_b64_tr_b16 v[202:203], v242 offset:2048
v_mul_f32 v36, v36, v230
v_mul_f32 v37, v37, v230
v_fmac_f32 v36, v226, v252
v_fmac_f32 v37, v227, v252
)ASM"
R"ASM(
v_mfma_f32_16x16x32_bf16 a[176:179], v[208:211], a[240:243], a[176:179]
ds_read_b64_tr_b16 v[204:205], v243 offset:0
v_cvt_pk_bf16_f32 v146, v36, v37
v_lshlrev_b32 v228, 16, v147
v_and_b32 v229, 0xffff0000, v147
v_mul_f32 v38, v38, v230
v_mfma_f32_16x16x32_bf16 a[184:187], v[208:211], a[244:247], a[184:187]
ds_read_b64_tr_b16 v[206:207], v243 offset:2048
v_mul_f32 v39, v39, v230
v_fmac_f32 v38, v228, v252
v_fmac_f32 v39, v229, v252
v_cvt_pk_bf16_f32 v147, v38, v39
v_mfma_f32_16x16x32_bf16 a[180:183], v[212:215], a[240:243], a[180:183]
ds_read_b64_tr_b16 v[208:209], v248 offset:0
v_lshlrev_b32 v226, 16, v148
v_and_b32 v227, 0xffff0000, v148
v_mul_f32 v40, v40, v231
v_mul_f32 v41, v41, v231
v_mfma_f32_16x16x32_bf16 a[188:191], v[212:215], a[244:247], a[188:191]
ds_read_b64_tr_b16 v[210:211], v248 offset:2048
v_fmac_f32 v40, v226, v253
v_fmac_f32 v41, v227, v253
v_cvt_pk_bf16_f32 v148, v40, v41
s_waitcnt lgkmcnt(6)
v_mfma_f32_16x16x16_bf16 a[160:163], v[216:217], a[248:249], a[160:163]
ds_read_b64_tr_b16 v[212:213], v249 offset:0
v_lshlrev_b32 v228, 16, v149
v_and_b32 v229, 0xffff0000, v149
v_mul_f32 v42, v42, v231
v_mul_f32 v43, v43, v231
v_mfma_f32_16x16x16_bf16 a[168:171], v[216:217], a[250:251], a[168:171]
ds_read_b64_tr_b16 v[214:215], v249 offset:2048
v_fmac_f32 v42, v228, v253
v_fmac_f32 v43, v229, v253
v_cvt_pk_bf16_f32 v149, v42, v43
v_lshlrev_b32 v226, 16, v150
v_mfma_f32_16x16x16_bf16 a[164:167], v[220:221], a[248:249], a[164:167]
ds_read_b64_tr_b16 v[216:217], v242 offset:4096
v_and_b32 v227, 0xffff0000, v150
v_mul_f32 v44, v44, v231
v_mul_f32 v45, v45, v231
v_fmac_f32 v44, v226, v253
v_mfma_f32_16x16x16_bf16 a[172:175], v[220:221], a[250:251], a[172:175]
ds_read_b64_tr_b16 v[218:219], v242 offset:6144
v_fmac_f32 v45, v227, v253
v_cvt_pk_bf16_f32 v150, v44, v45
v_lshlrev_b32 v228, 16, v151
v_and_b32 v229, 0xffff0000, v151
v_mfma_f32_16x16x16_bf16 a[176:179], v[192:193], a[248:249], a[176:179]
ds_read_b64_tr_b16 v[220:221], v243 offset:4096
v_mul_f32 v46, v46, v231
v_mul_f32 v47, v47, v231
v_fmac_f32 v46, v228, v253
v_fmac_f32 v47, v229, v253
v_mfma_f32_16x16x16_bf16 a[184:187], v[192:193], a[250:251], a[184:187]
ds_read_b64_tr_b16 v[222:223], v243 offset:6144
v_cvt_pk_bf16_f32 v151, v46, v47
v_lshlrev_b32 v226, 16, v152
v_and_b32 v227, 0xffff0000, v152
v_mul_f32 v48, v48, v230
v_mul_f32 v49, v49, v230
v_mfma_f32_16x16x16_bf16 a[180:183], v[196:197], a[248:249], a[180:183]
ds_read_b64_tr_b16 v[192:193], v248 offset:4096
v_fmac_f32 v48, v226, v252
v_fmac_f32 v49, v227, v252
v_cvt_pk_bf16_f32 v152, v48, v49
v_lshlrev_b32 v228, 16, v153
v_and_b32 v229, 0xffff0000, v153
v_mfma_f32_16x16x16_bf16 a[188:191], v[196:197], a[250:251], a[188:191]
ds_read_b64_tr_b16 v[194:195], v248 offset:6144
v_mul_f32 v50, v50, v230
v_mul_f32 v51, v51, v230
v_fmac_f32 v50, v228, v252
v_fmac_f32 v51, v229, v252
v_cvt_pk_bf16_f32 v153, v50, v51
s_waitcnt lgkmcnt(6)
v_mfma_f32_16x16x32_bf16 a[192:195], v[200:203], a[224:227], 0
ds_read_b64_tr_b16 v[196:197], v249 offset:4096
s_add_u32 m0, s8, s68
s_mov_b32 s9, 256
buffer_load_dwordx4 v244, s[44:47], s9 offen offset:0 lds
v_mfma_f32_16x16x32_bf16 a[200:203], v[200:203], a[228:231], 0
ds_read_b64_tr_b16 v[198:199], v249 offset:6144
v_lshlrev_b32 v226, 16, v154
v_and_b32 v227, 0xffff0000, v154
v_mul_f32 v52, v52, v230
v_mul_f32 v53, v53, v230
v_mfma_f32_16x16x32_bf16 a[196:199], v[204:207], a[224:227], 0
ds_read_b64_tr_b16 v[200:201], v242 offset:8192
s_mov_b32 s9, 6400
buffer_load_dwordx4 v244, s[44:47], s9 offen offset:1024 lds
v_fmac_f32 v52, v226, v252
v_fmac_f32 v53, v227, v252
v_mfma_f32_16x16x32_bf16 a[204:207], v[204:207], a[228:231], 0
ds_read_b64_tr_b16 v[202:203], v242 offset:10240
v_cvt_pk_bf16_f32 v154, v52, v53
v_lshlrev_b32 v228, 16, v155
v_and_b32 v229, 0xffff0000, v155
v_mul_f32 v54, v54, v230
v_mul_f32 v55, v55, v230
v_mfma_f32_16x16x32_bf16 a[208:211], v[208:211], a[224:227], 0
ds_read_b64_tr_b16 v[204:205], v243 offset:8192
s_mov_b32 s9, 12544
buffer_load_dwordx4 v244, s[44:47], s9 offen offset:2048 lds
v_fmac_f32 v54, v228, v252
v_fmac_f32 v55, v229, v252
v_mfma_f32_16x16x32_bf16 a[216:219], v[208:211], a[228:231], 0
ds_read_b64_tr_b16 v[206:207], v243 offset:10240
v_cvt_pk_bf16_f32 v155, v54, v55
v_lshlrev_b32 v226, 16, v156
v_and_b32 v227, 0xffff0000, v156
v_mul_f32 v56, v56, v231
v_mul_f32 v57, v57, v231
v_mfma_f32_16x16x32_bf16 a[212:215], v[212:215], a[224:227], 0
ds_read_b64_tr_b16 v[208:209], v248 offset:8192
s_mov_b32 s9, 18688
buffer_load_dwordx4 v244, s[44:47], s9 offen offset:3072 lds
v_fmac_f32 v56, v226, v253
v_fmac_f32 v57, v227, v253
v_mfma_f32_16x16x32_bf16 a[220:223], v[212:215], a[228:231], 0
ds_read_b64_tr_b16 v[210:211], v248 offset:10240
v_cvt_pk_bf16_f32 v156, v56, v57
v_lshlrev_b32 v228, 16, v157
v_and_b32 v229, 0xffff0000, v157
v_mul_f32 v58, v58, v231
s_waitcnt lgkmcnt(6)
v_mfma_f32_16x16x32_bf16 a[192:195], v[216:219], a[232:235], a[192:195]
ds_read_b64_tr_b16 v[212:213], v249 offset:8192
s_add_u32 m0, m0, 4096
s_mov_b32 s9, 28928
buffer_load_dwordx4 v244, s[44:47], s9 offen offset:0 lds
v_mul_f32 v59, v59, v231
v_mfma_f32_16x16x32_bf16 a[200:203], v[216:219], a[236:239], a[200:203]
ds_read_b64_tr_b16 v[214:215], v249 offset:10240
v_fmac_f32 v58, v228, v253
v_fmac_f32 v59, v229, v253
v_cvt_pk_bf16_f32 v157, v58, v59
v_lshlrev_b32 v226, 16, v158
v_and_b32 v227, 0xffff0000, v158
v_mfma_f32_16x16x32_bf16 a[196:199], v[220:223], a[232:235], a[196:199]
ds_read_b64_tr_b16 v[216:217], v242 offset:12288
s_mov_b32 s9, 35072
buffer_load_dwordx4 v244, s[44:47], s9 offen offset:1024 lds
v_mul_f32 v60, v60, v231
v_mfma_f32_16x16x32_bf16 a[204:207], v[220:223], a[236:239], a[204:207]
v_mul_f32 v61, v61, v231
v_fmac_f32 v60, v226, v253
v_fmac_f32 v61, v227, v253
v_cvt_pk_bf16_f32 v158, v60, v61
v_lshlrev_b32 v228, 16, v159
v_mfma_f32_16x16x32_bf16 a[208:211], v[192:195], a[232:235], a[208:211]
ds_read_b64_tr_b16 v[220:221], v243 offset:12288
s_mov_b32 s9, 41216
buffer_load_dwordx4 v244, s[44:47], s9 offen offset:2048 lds
v_and_b32 v229, 0xffff0000, v159
v_mfma_f32_16x16x32_bf16 a[216:219], v[192:195], a[236:239], a[216:219]
v_mul_f32 v62, v62, v231
v_mul_f32 v63, v63, v231
v_fmac_f32 v62, v228, v253
v_fmac_f32 v63, v229, v253
v_cvt_pk_bf16_f32 v159, v62, v63
v_mfma_f32_16x16x32_bf16 a[212:215], v[196:199], a[232:235], a[212:215]
ds_read_b64_tr_b16 v[192:193], v248 offset:12288
s_add_u32 s7, s7, 32768
s_cmp_eq_u32 s7, 163840
s_cselect_b32 s7, 0, s7
s_mov_b32 s9, 47360
buffer_load_dwordx4 v244, s[44:47], s9 offen offset:3072 lds
v_mfma_f32_16x16x32_bf16 a[220:223], v[196:199], a[236:239], a[220:223]
v_lshlrev_b32 v226, 16, v160
v_and_b32 v227, 0xffff0000, v160
v_mul_f32 v64, v64, v230
v_mul_f32 v65, v65, v230
s_waitcnt lgkmcnt(3)
v_mfma_f32_16x16x32_bf16 a[192:195], v[200:203], a[240:243], a[192:195]
ds_read_b64_tr_b16 v[196:197], v249 offset:12288
s_add_u32 s8, s8, 32768
s_cmp_eq_u32 s8, 163840
s_cselect_b32 s8, 0, s8
v_fmac_f32 v64, v226, v252
v_mfma_f32_16x16x32_bf16 a[200:203], v[200:203], a[244:247], a[200:203]
v_add_u32 v242, s7, v232
v_add_u32 v243, s7, v233
v_add_u32 v248, s7, v234
v_add_u32 v249, s7, v235
v_fmac_f32 v65, v227, v252
v_mfma_f32_16x16x32_bf16 a[196:199], v[204:207], a[240:243], a[196:199]
s_waitcnt vmcnt(16)
s_barrier
ds_read_b64_tr_b16 v[200:201], v242 offset:0
v_cvt_pk_bf16_f32 v160, v64, v65
v_lshlrev_b32 v228, 16, v161
v_mfma_f32_16x16x32_bf16 a[204:207], v[204:207], a[244:247], a[204:207]
ds_read_b64_tr_b16 v[202:203], v242 offset:2048
v_and_b32 v229, 0xffff0000, v161
v_mul_f32 v66, v66, v230
v_mul_f32 v67, v67, v230
v_fmac_f32 v66, v228, v252
v_mfma_f32_16x16x32_bf16 a[208:211], v[208:211], a[240:243], a[208:211]
ds_read_b64_tr_b16 v[204:205], v243 offset:0
)ASM"
R"ASM(
v_fmac_f32 v67, v229, v252
v_cvt_pk_bf16_f32 v161, v66, v67
v_lshlrev_b32 v226, 16, v162
v_and_b32 v227, 0xffff0000, v162
v_mfma_f32_16x16x32_bf16 a[216:219], v[208:211], a[244:247], a[216:219]
ds_read_b64_tr_b16 v[206:207], v243 offset:2048
v_mul_f32 v68, v68, v230
v_mul_f32 v69, v69, v230
v_fmac_f32 v68, v226, v252
v_fmac_f32 v69, v227, v252
v_mfma_f32_16x16x32_bf16 a[212:215], v[212:215], a[240:243], a[212:215]
ds_read_b64_tr_b16 v[208:209], v248 offset:0
v_cvt_pk_bf16_f32 v162, v68, v69
v_lshlrev_b32 v228, 16, v163
v_and_b32 v229, 0xffff0000, v163
v_mul_f32 v70, v70, v230
v_mfma_f32_16x16x32_bf16 a[220:223], v[212:215], a[244:247], a[220:223]
ds_read_b64_tr_b16 v[210:211], v248 offset:2048
v_mul_f32 v71, v71, v230
v_fmac_f32 v70, v228, v252
v_fmac_f32 v71, v229, v252
s_waitcnt lgkmcnt(6)
v_mfma_f32_16x16x16_bf16 a[192:195], v[216:217], a[248:249], a[192:195]
ds_read_b64_tr_b16 v[212:213], v249 offset:0
v_cvt_pk_bf16_f32 v163, v70, v71
v_lshlrev_b32 v226, 16, v164
v_and_b32 v227, 0xffff0000, v164
v_mul_f32 v72, v72, v231
v_mfma_f32_16x16x16_bf16 a[200:203], v[216:217], a[250:251], a[200:203]
ds_read_b64_tr_b16 v[214:215], v249 offset:2048
v_mul_f32 v73, v73, v231
v_fmac_f32 v72, v226, v253
v_fmac_f32 v73, v227, v253
v_cvt_pk_bf16_f32 v164, v72, v73
v_lshlrev_b32 v228, 16, v165
v_mfma_f32_16x16x16_bf16 a[196:199], v[220:221], a[248:249], a[196:199]
ds_read_b64_tr_b16 v[216:217], v242 offset:4096
v_and_b32 v229, 0xffff0000, v165
v_mul_f32 v74, v74, v231
v_mul_f32 v75, v75, v231
v_fmac_f32 v74, v228, v253
v_fmac_f32 v75, v229, v253
v_mfma_f32_16x16x16_bf16 a[204:207], v[220:221], a[250:251], a[204:207]
ds_read_b64_tr_b16 v[218:219], v242 offset:6144
v_cvt_pk_bf16_f32 v165, v74, v75
v_lshlrev_b32 v226, 16, v166
v_and_b32 v227, 0xffff0000, v166
v_mul_f32 v76, v76, v231
v_mul_f32 v77, v77, v231
v_mfma_f32_16x16x16_bf16 a[208:211], v[192:193], a[248:249], a[208:211]
ds_read_b64_tr_b16 v[220:221], v243 offset:4096
v_fmac_f32 v76, v226, v253
v_fmac_f32 v77, v227, v253
v_cvt_pk_bf16_f32 v166, v76, v77
v_lshlrev_b32 v228, 16, v167
v_and_b32 v229, 0xffff0000, v167
v_mfma_f32_16x16x16_bf16 a[216:219], v[192:193], a[250:251], a[216:219]
ds_read_b64_tr_b16 v[222:223], v243 offset:6144
v_mul_f32 v78, v78, v231
v_mul_f32 v79, v79, v231
v_fmac_f32 v78, v228, v253
v_fmac_f32 v79, v229, v253
v_cvt_pk_bf16_f32 v167, v78, v79
v_mfma_f32_16x16x16_bf16 a[212:215], v[196:197], a[248:249], a[212:215]
ds_read_b64_tr_b16 v[192:193], v248 offset:4096
v_mfma_f32_16x16x16_bf16 a[220:223], v[196:197], a[250:251], a[220:223]
ds_read_b64_tr_b16 v[194:195], v248 offset:6144
s_waitcnt lgkmcnt(6)
v_mfma_f32_16x16x32_bf16 a[0:3], v[200:203], v[128:131], a[0:3]
ds_read_b64_tr_b16 v[196:197], v249 offset:4096
s_add_u32 m0, s8, s68
s_mov_b32 s9, 384
buffer_load_dwordx4 v244, s[44:47], s9 offen offset:0 lds
v_mfma_f32_16x16x32_bf16 a[8:11], v[200:203], v[132:135], a[8:11]
ds_read_b64_tr_b16 v[198:199], v249 offset:6144
v_lshlrev_b32 v226, 16, v168
v_and_b32 v227, 0xffff0000, v168
v_mul_f32 v80, v80, v230
v_mul_f32 v81, v81, v230
v_mfma_f32_16x16x32_bf16 a[4:7], v[204:207], v[128:131], a[4:7]
ds_read_b64_tr_b16 v[200:201], v242 offset:8192
v_fmac_f32 v80, v226, v252
v_fmac_f32 v81, v227, v252
v_cvt_pk_bf16_f32 v168, v80, v81
v_lshlrev_b32 v228, 16, v169
v_mfma_f32_16x16x32_bf16 a[12:15], v[204:207], v[132:135], a[12:15]
ds_read_b64_tr_b16 v[202:203], v242 offset:10240
v_and_b32 v229, 0xffff0000, v169
v_mul_f32 v82, v82, v230
v_mul_f32 v83, v83, v230
v_mfma_f32_16x16x32_bf16 a[16:19], v[208:211], v[128:131], a[16:19]
ds_read_b64_tr_b16 v[204:205], v243 offset:8192
v_fmac_f32 v82, v228, v252
v_fmac_f32 v83, v229, v252
v_cvt_pk_bf16_f32 v169, v82, v83
v_lshlrev_b32 v226, 16, v170
v_mfma_f32_16x16x32_bf16 a[24:27], v[208:211], v[132:135], a[24:27]
ds_read_b64_tr_b16 v[206:207], v243 offset:10240
s_mov_b32 s9, 6528
buffer_load_dwordx4 v244, s[44:47], s9 offen offset:1024 lds
v_and_b32 v227, 0xffff0000, v170
v_mfma_f32_16x16x32_bf16 a[20:23], v[212:215], v[128:131], a[20:23]
ds_read_b64_tr_b16 v[208:209], v248 offset:8192
v_mul_f32 v84, v84, v230
v_mul_f32 v85, v85, v230
v_fmac_f32 v84, v226, v252
v_fmac_f32 v85, v227, v252
v_mfma_f32_16x16x32_bf16 a[28:31], v[212:215], v[132:135], a[28:31]
ds_read_b64_tr_b16 v[210:211], v248 offset:10240
v_cvt_pk_bf16_f32 v170, v84, v85
v_lshlrev_b32 v228, 16, v171
v_and_b32 v229, 0xffff0000, v171
s_waitcnt lgkmcnt(6)
v_mfma_f32_16x16x32_bf16 a[0:3], v[216:219], v[136:139], a[0:3]
ds_read_b64_tr_b16 v[212:213], v249 offset:8192
v_mul_f32 v86, v86, v230
v_mul_f32 v87, v87, v230
v_fmac_f32 v86, v228, v252
v_fmac_f32 v87, v229, v252
v_mfma_f32_16x16x32_bf16 a[8:11], v[216:219], v[140:143], a[8:11]
ds_read_b64_tr_b16 v[214:215], v249 offset:10240
v_cvt_pk_bf16_f32 v171, v86, v87
v_lshlrev_b32 v226, 16, v172
v_and_b32 v227, 0xffff0000, v172
v_mul_f32 v88, v88, v231
v_mfma_f32_16x16x32_bf16 a[4:7], v[220:223], v[136:139], a[4:7]
ds_read_b64_tr_b16 v[216:217], v242 offset:12288
s_mov_b32 s9, 12672
buffer_load_dwordx4 v244, s[44:47], s9 offen offset:2048 lds
v_mul_f32 v89, v89, v231
v_mfma_f32_16x16x32_bf16 a[12:15], v[220:223], v[140:143], a[12:15]
ds_read_b64_tr_b16 v[218:219], v242 offset:14336
v_fmac_f32 v88, v226, v253
v_fmac_f32 v89, v227, v253
v_cvt_pk_bf16_f32 v172, v88, v89
v_lshlrev_b32 v228, 16, v173
v_mfma_f32_16x16x32_bf16 a[16:19], v[192:195], v[136:139], a[16:19]
ds_read_b64_tr_b16 v[220:221], v243 offset:12288
v_and_b32 v229, 0xffff0000, v173
v_mul_f32 v90, v90, v231
v_mul_f32 v91, v91, v231
v_fmac_f32 v90, v228, v253
v_mfma_f32_16x16x32_bf16 a[24:27], v[192:195], v[140:143], a[24:27]
ds_read_b64_tr_b16 v[222:223], v243 offset:14336
v_fmac_f32 v91, v229, v253
v_cvt_pk_bf16_f32 v173, v90, v91
v_lshlrev_b32 v226, 16, v174
v_and_b32 v227, 0xffff0000, v174
v_mfma_f32_16x16x32_bf16 a[20:23], v[196:199], v[136:139], a[20:23]
ds_read_b64_tr_b16 v[192:193], v248 offset:12288
v_mul_f32 v92, v92, v231
v_mul_f32 v93, v93, v231
v_fmac_f32 v92, v226, v253
v_fmac_f32 v93, v227, v253
v_mfma_f32_16x16x32_bf16 a[28:31], v[196:199], v[140:143], a[28:31]
ds_read_b64_tr_b16 v[194:195], v248 offset:14336
v_cvt_pk_bf16_f32 v174, v92, v93
v_lshlrev_b32 v228, 16, v175
v_and_b32 v229, 0xffff0000, v175
s_waitcnt lgkmcnt(6)
v_mfma_f32_16x16x32_bf16 a[0:3], v[200:203], v[144:147], a[0:3]
ds_read_b64_tr_b16 v[196:197], v249 offset:12288
s_mov_b32 s9, 18816
buffer_load_dwordx4 v244, s[44:47], s9 offen offset:3072 lds
v_mul_f32 v94, v94, v231
v_mfma_f32_16x16x32_bf16 a[8:11], v[200:203], v[148:151], a[8:11]
ds_read_b64_tr_b16 v[198:199], v249 offset:14336
v_mul_f32 v95, v95, v231
v_fmac_f32 v94, v228, v253
v_fmac_f32 v95, v229, v253
v_cvt_pk_bf16_f32 v175, v94, v95
v_mfma_f32_16x16x32_bf16 a[4:7], v[204:207], v[144:147], a[4:7]
ds_read_b64_tr_b16 v[200:201], v242 offset:16384
v_lshlrev_b32 v226, 16, v176
v_and_b32 v227, 0xffff0000, v176
v_mul_f32 v96, v96, v230
v_mfma_f32_16x16x32_bf16 a[12:15], v[204:207], v[148:151], a[12:15]
ds_read_b64_tr_b16 v[202:203], v242 offset:18432
v_mul_f32 v97, v97, v230
v_fmac_f32 v96, v226, v252
v_fmac_f32 v97, v227, v252
v_cvt_pk_bf16_f32 v176, v96, v97
v_mfma_f32_16x16x32_bf16 a[16:19], v[208:211], v[144:147], a[16:19]
ds_read_b64_tr_b16 v[204:205], v243 offset:16384
v_lshlrev_b32 v228, 16, v177
v_and_b32 v229, 0xffff0000, v177
v_mul_f32 v98, v98, v230
v_mul_f32 v99, v99, v230
v_mfma_f32_16x16x32_bf16 a[24:27], v[208:211], v[148:151], a[24:27]
ds_read_b64_tr_b16 v[206:207], v243 offset:18432
s_add_u32 m0, m0, 4096
s_mov_b32 s9, 29056
buffer_load_dwordx4 v244, s[44:47], s9 offen offset:0 lds
v_mfma_f32_16x16x32_bf16 a[20:23], v[212:215], v[144:147], a[20:23]
ds_read_b64_tr_b16 v[208:209], v248 offset:16384
v_fmac_f32 v98, v228, v252
v_fmac_f32 v99, v229, v252
v_cvt_pk_bf16_f32 v177, v98, v99
v_mfma_f32_16x16x32_bf16 a[28:31], v[212:215], v[148:151], a[28:31]
ds_read_b64_tr_b16 v[210:211], v248 offset:18432
)ASM"
R"ASM(
v_lshlrev_b32 v226, 16, v178
v_and_b32 v227, 0xffff0000, v178
s_waitcnt lgkmcnt(6)
v_mfma_f32_16x16x32_bf16 a[0:3], v[216:219], v[152:155], a[0:3]
ds_read_b64_tr_b16 v[212:213], v249 offset:16384
v_mul_f32 v100, v100, v230
v_mul_f32 v101, v101, v230
v_fmac_f32 v100, v226, v252
v_mfma_f32_16x16x32_bf16 a[8:11], v[216:219], v[156:159], a[8:11]
ds_read_b64_tr_b16 v[214:215], v249 offset:18432
v_fmac_f32 v101, v227, v252
v_cvt_pk_bf16_f32 v178, v100, v101
v_lshlrev_b32 v228, 16, v179
v_and_b32 v229, 0xffff0000, v179
v_mfma_f32_16x16x32_bf16 a[4:7], v[220:223], v[152:155], a[4:7]
ds_read_b64_tr_b16 v[216:217], v242 offset:20480
s_mov_b32 s9, 35200
buffer_load_dwordx4 v244, s[44:47], s9 offen offset:1024 lds
v_mul_f32 v102, v102, v230
v_mfma_f32_16x16x32_bf16 a[12:15], v[220:223], v[156:159], a[12:15]
ds_read_b64_tr_b16 v[218:219], v242 offset:22528
v_mul_f32 v103, v103, v230
v_fmac_f32 v102, v228, v252
v_fmac_f32 v103, v229, v252
v_cvt_pk_bf16_f32 v179, v102, v103
v_mfma_f32_16x16x32_bf16 a[16:19], v[192:195], v[152:155], a[16:19]
ds_read_b64_tr_b16 v[220:221], v243 offset:20480
v_lshlrev_b32 v226, 16, v180
v_and_b32 v227, 0xffff0000, v180
v_mul_f32 v104, v104, v231
v_mul_f32 v105, v105, v231
v_mfma_f32_16x16x32_bf16 a[24:27], v[192:195], v[156:159], a[24:27]
ds_read_b64_tr_b16 v[222:223], v243 offset:22528
v_fmac_f32 v104, v226, v253
v_fmac_f32 v105, v227, v253
v_cvt_pk_bf16_f32 v180, v104, v105
v_lshlrev_b32 v228, 16, v181
v_mfma_f32_16x16x32_bf16 a[20:23], v[196:199], v[152:155], a[20:23]
ds_read_b64_tr_b16 v[192:193], v248 offset:20480
v_and_b32 v229, 0xffff0000, v181
v_mul_f32 v106, v106, v231
v_mul_f32 v107, v107, v231
v_fmac_f32 v106, v228, v253
v_mfma_f32_16x16x32_bf16 a[28:31], v[196:199], v[156:159], a[28:31]
ds_read_b64_tr_b16 v[194:195], v248 offset:22528
v_fmac_f32 v107, v229, v253
v_cvt_pk_bf16_f32 v181, v106, v107
v_lshlrev_b32 v226, 16, v182
s_waitcnt lgkmcnt(6)
v_mfma_f32_16x16x32_bf16 a[0:3], v[200:203], v[160:163], a[0:3]
ds_read_b64_tr_b16 v[196:197], v249 offset:20480
s_mov_b32 s9, 41344
buffer_load_dwordx4 v244, s[44:47], s9 offen offset:2048 lds
v_and_b32 v227, 0xffff0000, v182
v_mfma_f32_16x16x32_bf16 a[8:11], v[200:203], v[164:167], a[8:11]
ds_read_b64_tr_b16 v[198:199], v249 offset:22528
v_mul_f32 v108, v108, v231
v_mul_f32 v109, v109, v231
v_fmac_f32 v108, v226, v253
v_fmac_f32 v109, v227, v253
v_mfma_f32_16x16x32_bf16 a[4:7], v[204:207], v[160:163], a[4:7]
ds_read_b64_tr_b16 v[200:201], v242 offset:24576
v_cvt_pk_bf16_f32 v182, v108, v109
v_lshlrev_b32 v228, 16, v183
v_and_b32 v229, 0xffff0000, v183
v_mul_f32 v110, v110, v231
v_mfma_f32_16x16x32_bf16 a[12:15], v[204:207], v[164:167], a[12:15]
ds_read_b64_tr_b16 v[202:203], v242 offset:26624
v_mul_f32 v111, v111, v231
v_fmac_f32 v110, v228, v253
v_fmac_f32 v111, v229, v253
v_cvt_pk_bf16_f32 v183, v110, v111
v_mfma_f32_16x16x32_bf16 a[16:19], v[208:211], v[160:163], a[16:19]
ds_read_b64_tr_b16 v[204:205], v243 offset:24576
v_lshlrev_b32 v226, 16, v184
v_and_b32 v227, 0xffff0000, v184
v_mul_f32 v112, v112, v230
v_mul_f32 v113, v113, v230
v_mfma_f32_16x16x32_bf16 a[24:27], v[208:211], v[164:167], a[24:27]
ds_read_b64_tr_b16 v[206:207], v243 offset:26624
s_mov_b32 s9, 47488
buffer_load_dwordx4 v244, s[44:47], s9 offen offset:3072 lds
v_fmac_f32 v112, v226, v252
v_mfma_f32_16x16x32_bf16 a[20:23], v[212:215], v[160:163], a[20:23]
ds_read_b64_tr_b16 v[208:209], v248 offset:24576
v_fmac_f32 v113, v227, v252
v_cvt_pk_bf16_f32 v184, v112, v113
v_lshlrev_b32 v228, 16, v185
v_and_b32 v229, 0xffff0000, v185
v_mfma_f32_16x16x32_bf16 a[28:31], v[212:215], v[164:167], a[28:31]
ds_read_b64_tr_b16 v[210:211], v248 offset:26624
v_mul_f32 v114, v114, v230
v_mul_f32 v115, v115, v230
v_fmac_f32 v114, v228, v252
s_waitcnt lgkmcnt(6)
v_mfma_f32_16x16x32_bf16 a[0:3], v[216:219], v[168:171], a[0:3]
ds_read_b64_tr_b16 v[212:213], v249 offset:24576
v_fmac_f32 v115, v229, v252
v_cvt_pk_bf16_f32 v185, v114, v115
v_lshlrev_b32 v226, 16, v186
v_and_b32 v227, 0xffff0000, v186
v_mfma_f32_16x16x32_bf16 a[8:11], v[216:219], v[172:175], a[8:11]
ds_read_b64_tr_b16 v[214:215], v249 offset:26624
v_mul_f32 v116, v116, v230
v_mul_f32 v117, v117, v230
v_fmac_f32 v116, v226, v252
v_fmac_f32 v117, v227, v252
v_mfma_f32_16x16x32_bf16 a[4:7], v[220:223], v[168:171], a[4:7]
ds_read_b64_tr_b16 v[216:217], v242 offset:28672
s_add_u32 s8, s8, 32768
s_cmp_eq_u32 s8, 163840
s_cselect_b32 s8, 0, s8
v_cvt_pk_bf16_f32 v186, v116, v117
v_mfma_f32_16x16x32_bf16 a[12:15], v[220:223], v[172:175], a[12:15]
ds_read_b64_tr_b16 v[218:219], v242 offset:30720
v_lshlrev_b32 v228, 16, v187
v_and_b32 v229, 0xffff0000, v187
v_mul_f32 v118, v118, v230
v_mul_f32 v119, v119, v230
v_mfma_f32_16x16x32_bf16 a[16:19], v[192:195], v[168:171], a[16:19]
ds_read_b64_tr_b16 v[220:221], v243 offset:28672
v_fmac_f32 v118, v228, v252
v_fmac_f32 v119, v229, v252
v_cvt_pk_bf16_f32 v187, v118, v119
v_lshlrev_b32 v226, 16, v188
v_mfma_f32_16x16x32_bf16 a[24:27], v[192:195], v[172:175], a[24:27]
ds_read_b64_tr_b16 v[222:223], v243 offset:30720
v_and_b32 v227, 0xffff0000, v188
v_mul_f32 v120, v120, v231
v_mul_f32 v121, v121, v231
v_fmac_f32 v120, v226, v253
v_mfma_f32_16x16x32_bf16 a[20:23], v[196:199], v[168:171], a[20:23]
ds_read_b64_tr_b16 v[192:193], v248 offset:28672
s_add_u32 s7, s7, 32768
s_cmp_eq_u32 s7, 163840
s_cselect_b32 s7, 0, s7
v_fmac_f32 v121, v227, v253
v_mfma_f32_16x16x32_bf16 a[28:31], v[196:199], v[172:175], a[28:31]
ds_read_b64_tr_b16 v[194:195], v248 offset:30720
v_cvt_pk_bf16_f32 v188, v120, v121
v_lshlrev_b32 v228, 16, v189
v_and_b32 v229, 0xffff0000, v189
v_mul_f32 v122, v122, v231
s_waitcnt lgkmcnt(6)
v_mfma_f32_16x16x32_bf16 a[0:3], v[200:203], v[176:179], a[0:3]
ds_read_b64_tr_b16 v[196:197], v249 offset:28672
v_mul_f32 v123, v123, v231
v_fmac_f32 v122, v228, v253
v_fmac_f32 v123, v229, v253
v_cvt_pk_bf16_f32 v189, v122, v123
v_mfma_f32_16x16x32_bf16 a[8:11], v[200:203], v[180:183], a[8:11]
ds_read_b64_tr_b16 v[198:199], v249 offset:30720
v_add_u32 v242, s7, v232
v_add_u32 v243, s7, v233
v_add_u32 v248, s7, v234
v_add_u32 v249, s7, v235
v_mfma_f32_16x16x32_bf16 a[4:7], v[204:207], v[176:179], a[4:7]
s_waitcnt vmcnt(16)
s_barrier
ds_read_b64_tr_b16 v[200:201], v242 offset:0
v_lshlrev_b32 v226, 16, v190
v_and_b32 v227, 0xffff0000, v190
v_mfma_f32_16x16x32_bf16 a[12:15], v[204:207], v[180:183], a[12:15]
ds_read_b64_tr_b16 v[202:203], v242 offset:2048
v_mul_f32 v124, v124, v231
v_mul_f32 v125, v125, v231
v_fmac_f32 v124, v226, v253
v_fmac_f32 v125, v227, v253
v_mfma_f32_16x16x32_bf16 a[16:19], v[208:211], v[176:179], a[16:19]
ds_read_b64_tr_b16 v[204:205], v243 offset:0
v_cvt_pk_bf16_f32 v190, v124, v125
v_lshlrev_b32 v228, 16, v191
v_and_b32 v229, 0xffff0000, v191
v_mul_f32 v126, v126, v231
v_mfma_f32_16x16x32_bf16 a[24:27], v[208:211], v[180:183], a[24:27]
ds_read_b64_tr_b16 v[206:207], v243 offset:2048
v_mul_f32 v127, v127, v231
v_fmac_f32 v126, v228, v253
v_fmac_f32 v127, v229, v253
v_cvt_pk_bf16_f32 v191, v126, v127
v_mfma_f32_16x16x32_bf16 a[20:23], v[212:215], v[176:179], a[20:23]
ds_read_b64_tr_b16 v[208:209], v248 offset:0
v_mfma_f32_16x16x32_bf16 a[28:31], v[212:215], v[180:183], a[28:31]
ds_read_b64_tr_b16 v[210:211], v248 offset:2048
s_waitcnt lgkmcnt(6)
v_mfma_f32_16x16x32_bf16 a[0:3], v[216:219], v[184:187], a[0:3]
ds_read_b64_tr_b16 v[212:213], v249 offset:0
v_mfma_f32_16x16x32_bf16 a[8:11], v[216:219], v[188:191], a[8:11]
ds_read_b64_tr_b16 v[214:215], v249 offset:2048
v_mfma_f32_16x16x32_bf16 a[4:7], v[220:223], v[184:187], a[4:7]
ds_read_b64_tr_b16 v[216:217], v242 offset:4096
v_mfma_f32_16x16x32_bf16 a[12:15], v[220:223], v[188:191], a[12:15]
ds_read_b64_tr_b16 v[218:219], v242 offset:6144
v_mfma_f32_16x16x32_bf16 a[16:19], v[192:195], v[184:187], a[16:19]
ds_read_b64_tr_b16 v[220:221], v243 offset:4096
v_mfma_f32_16x16x32_bf16 a[24:27], v[192:195], v[188:191], a[24:27]
ds_read_b64_tr_b16 v[222:223], v243 offset:6144
v_mfma_f32_16x16x32_bf16 a[20:23], v[196:199], v[184:187], a[20:23]
ds_read_b64_tr_b16 v[192:193], v248 offset:4096
v_mfma_f32_16x16x32_bf16 a[28:31], v[196:199], v[188:191], a[28:31]
)ASM"
R"ASM(
ds_read_b64_tr_b16 v[194:195], v248 offset:6144
s_waitcnt lgkmcnt(6)
v_mfma_f32_16x16x32_bf16 a[32:35], v[200:203], v[128:131], a[32:35]
ds_read_b64_tr_b16 v[196:197], v249 offset:4096
s_add_u32 m0, s8, s68
s_mov_b32 s9, 512
buffer_load_dwordx4 v244, s[44:47], s9 offen offset:0 lds
v_mfma_f32_16x16x32_bf16 a[40:43], v[200:203], v[132:135], a[40:43]
ds_read_b64_tr_b16 v[198:199], v249 offset:6144
v_mfma_f32_16x16x32_bf16 a[36:39], v[204:207], v[128:131], a[36:39]
ds_read_b64_tr_b16 v[200:201], v242 offset:8192
s_add_u32 s28, s80, 0
v_mfma_f32_16x16x32_bf16 a[44:47], v[204:207], v[132:135], a[44:47]
ds_read_b64_tr_b16 v[202:203], v242 offset:10240
v_accvgpr_read_b32 v0, a0
v_mfma_f32_16x16x32_bf16 a[48:51], v[208:211], v[128:131], a[48:51]
ds_read_b64_tr_b16 v[204:205], v243 offset:8192
v_accvgpr_read_b32 v1, a1
v_mfma_f32_16x16x32_bf16 a[56:59], v[208:211], v[132:135], a[56:59]
ds_read_b64_tr_b16 v[206:207], v243 offset:10240
s_mov_b32 s9, 6656
buffer_load_dwordx4 v244, s[44:47], s9 offen offset:1024 lds
v_mfma_f32_16x16x32_bf16 a[52:55], v[212:215], v[128:131], a[52:55]
ds_read_b64_tr_b16 v[208:209], v248 offset:8192
v_accvgpr_read_b32 v2, a2
v_mfma_f32_16x16x32_bf16 a[60:63], v[212:215], v[132:135], a[60:63]
ds_read_b64_tr_b16 v[210:211], v248 offset:10240
s_waitcnt lgkmcnt(6)
v_mfma_f32_16x16x32_bf16 a[32:35], v[216:219], v[136:139], a[32:35]
ds_read_b64_tr_b16 v[212:213], v249 offset:8192
v_accvgpr_read_b32 v3, a3
v_mfma_f32_16x16x32_bf16 a[40:43], v[216:219], v[140:143], a[40:43]
ds_read_b64_tr_b16 v[214:215], v249 offset:10240
v_accvgpr_read_b32 v4, a4
v_mfma_f32_16x16x32_bf16 a[36:39], v[220:223], v[136:139], a[36:39]
ds_read_b64_tr_b16 v[216:217], v242 offset:12288
s_mov_b32 s9, 12800
buffer_load_dwordx4 v244, s[44:47], s9 offen offset:2048 lds
v_mfma_f32_16x16x32_bf16 a[44:47], v[220:223], v[140:143], a[44:47]
ds_read_b64_tr_b16 v[218:219], v242 offset:14336
v_accvgpr_read_b32 v5, a5
v_mfma_f32_16x16x32_bf16 a[48:51], v[192:195], v[136:139], a[48:51]
ds_read_b64_tr_b16 v[220:221], v243 offset:12288
v_accvgpr_read_b32 v6, a6
v_mfma_f32_16x16x32_bf16 a[56:59], v[192:195], v[140:143], a[56:59]
ds_read_b64_tr_b16 v[222:223], v243 offset:14336
v_accvgpr_read_b32 v7, a7
v_mfma_f32_16x16x32_bf16 a[52:55], v[196:199], v[136:139], a[52:55]
ds_read_b64_tr_b16 v[192:193], v248 offset:12288
v_cvt_pk_bf16_f32 v0, v0, v1
v_mfma_f32_16x16x32_bf16 a[60:63], v[196:199], v[140:143], a[60:63]
ds_read_b64_tr_b16 v[194:195], v248 offset:14336
s_waitcnt lgkmcnt(6)
v_mfma_f32_16x16x32_bf16 a[32:35], v[200:203], v[144:147], a[32:35]
ds_read_b64_tr_b16 v[196:197], v249 offset:12288
s_mov_b32 s9, 18944
buffer_load_dwordx4 v244, s[44:47], s9 offen offset:3072 lds
v_mfma_f32_16x16x32_bf16 a[40:43], v[200:203], v[148:151], a[40:43]
ds_read_b64_tr_b16 v[198:199], v249 offset:14336
v_cvt_pk_bf16_f32 v1, v2, v3
v_mfma_f32_16x16x32_bf16 a[36:39], v[204:207], v[144:147], a[36:39]
ds_read_b64_tr_b16 v[200:201], v242 offset:16384
v_cvt_pk_bf16_f32 v2, v4, v5
v_mfma_f32_16x16x32_bf16 a[44:47], v[204:207], v[148:151], a[44:47]
ds_read_b64_tr_b16 v[202:203], v242 offset:18432
v_cvt_pk_bf16_f32 v3, v6, v7
v_mfma_f32_16x16x32_bf16 a[48:51], v[208:211], v[144:147], a[48:51]
ds_read_b64_tr_b16 v[204:205], v243 offset:16384
buffer_store_dwordx4 v[0:3], v236, s[36:39], s28 offen offset:0 sc1
v_mfma_f32_16x16x32_bf16 a[56:59], v[208:211], v[148:151], a[56:59]
ds_read_b64_tr_b16 v[206:207], v243 offset:18432
s_add_u32 m0, m0, 4096
s_mov_b32 s9, 29184
buffer_load_dwordx4 v244, s[44:47], s9 offen offset:0 lds
v_mfma_f32_16x16x32_bf16 a[52:55], v[212:215], v[144:147], a[52:55]
ds_read_b64_tr_b16 v[208:209], v248 offset:16384
v_accvgpr_read_b32 v8, a8
v_mfma_f32_16x16x32_bf16 a[60:63], v[212:215], v[148:151], a[60:63]
ds_read_b64_tr_b16 v[210:211], v248 offset:18432
s_waitcnt lgkmcnt(6)
v_mfma_f32_16x16x32_bf16 a[32:35], v[216:219], v[152:155], a[32:35]
ds_read_b64_tr_b16 v[212:213], v249 offset:16384
v_accvgpr_read_b32 v9, a9
v_mfma_f32_16x16x32_bf16 a[40:43], v[216:219], v[156:159], a[40:43]
ds_read_b64_tr_b16 v[214:215], v249 offset:18432
v_accvgpr_read_b32 v10, a10
v_mfma_f32_16x16x32_bf16 a[36:39], v[220:223], v[152:155], a[36:39]
ds_read_b64_tr_b16 v[216:217], v242 offset:20480
s_mov_b32 s9, 35328
buffer_load_dwordx4 v244, s[44:47], s9 offen offset:1024 lds
v_mfma_f32_16x16x32_bf16 a[44:47], v[220:223], v[156:159], a[44:47]
ds_read_b64_tr_b16 v[218:219], v242 offset:22528
v_accvgpr_read_b32 v11, a11
v_mfma_f32_16x16x32_bf16 a[48:51], v[192:195], v[152:155], a[48:51]
ds_read_b64_tr_b16 v[220:221], v243 offset:20480
v_accvgpr_read_b32 v12, a12
v_mfma_f32_16x16x32_bf16 a[56:59], v[192:195], v[156:159], a[56:59]
ds_read_b64_tr_b16 v[222:223], v243 offset:22528
v_accvgpr_read_b32 v13, a13
v_mfma_f32_16x16x32_bf16 a[52:55], v[196:199], v[152:155], a[52:55]
ds_read_b64_tr_b16 v[192:193], v248 offset:20480
v_accvgpr_read_b32 v14, a14
v_mfma_f32_16x16x32_bf16 a[60:63], v[196:199], v[156:159], a[60:63]
ds_read_b64_tr_b16 v[194:195], v248 offset:22528
v_accvgpr_read_b32 v15, a15
s_waitcnt lgkmcnt(6)
v_mfma_f32_16x16x32_bf16 a[32:35], v[200:203], v[160:163], a[32:35]
ds_read_b64_tr_b16 v[196:197], v249 offset:20480
s_mov_b32 s9, 41472
buffer_load_dwordx4 v244, s[44:47], s9 offen offset:2048 lds
v_mfma_f32_16x16x32_bf16 a[40:43], v[200:203], v[164:167], a[40:43]
ds_read_b64_tr_b16 v[198:199], v249 offset:22528
v_cvt_pk_bf16_f32 v8, v8, v9
v_cvt_pk_bf16_f32 v9, v10, v11
v_mfma_f32_16x16x32_bf16 a[36:39], v[204:207], v[160:163], a[36:39]
ds_read_b64_tr_b16 v[200:201], v242 offset:24576
v_cvt_pk_bf16_f32 v10, v12, v13
v_cvt_pk_bf16_f32 v11, v14, v15
v_mfma_f32_16x16x32_bf16 a[44:47], v[204:207], v[164:167], a[44:47]
ds_read_b64_tr_b16 v[202:203], v242 offset:26624
buffer_store_dwordx4 v[8:11], v236, s[36:39], s28 offen offset:1024 sc1
v_mfma_f32_16x16x32_bf16 a[48:51], v[208:211], v[160:163], a[48:51]
ds_read_b64_tr_b16 v[204:205], v243 offset:24576
v_accvgpr_read_b32 v16, a16
v_mfma_f32_16x16x32_bf16 a[56:59], v[208:211], v[164:167], a[56:59]
ds_read_b64_tr_b16 v[206:207], v243 offset:26624
s_mov_b32 s9, 47616
buffer_load_dwordx4 v244, s[44:47], s9 offen offset:3072 lds
v_mfma_f32_16x16x32_bf16 a[52:55], v[212:215], v[160:163], a[52:55]
ds_read_b64_tr_b16 v[208:209], v248 offset:24576
v_accvgpr_read_b32 v17, a17
v_accvgpr_read_b32 v18, a18
v_mfma_f32_16x16x32_bf16 a[60:63], v[212:215], v[164:167], a[60:63]
ds_read_b64_tr_b16 v[210:211], v248 offset:26624
v_accvgpr_read_b32 v19, a19
s_waitcnt lgkmcnt(6)
v_mfma_f32_16x16x32_bf16 a[32:35], v[216:219], v[168:171], a[32:35]
ds_read_b64_tr_b16 v[212:213], v249 offset:24576
v_accvgpr_read_b32 v20, a20
v_accvgpr_read_b32 v21, a21
v_mfma_f32_16x16x32_bf16 a[40:43], v[216:219], v[172:175], a[40:43]
ds_read_b64_tr_b16 v[214:215], v249 offset:26624
v_accvgpr_read_b32 v22, a22
v_accvgpr_read_b32 v23, a23
v_mfma_f32_16x16x32_bf16 a[36:39], v[220:223], v[168:171], a[36:39]
ds_read_b64_tr_b16 v[216:217], v242 offset:28672
s_add_u32 s8, s8, 32768
s_cmp_eq_u32 s8, 163840
s_cselect_b32 s8, 0, s8
v_mfma_f32_16x16x32_bf16 a[44:47], v[220:223], v[172:175], a[44:47]
ds_read_b64_tr_b16 v[218:219], v242 offset:30720
v_cvt_pk_bf16_f32 v16, v16, v17
v_cvt_pk_bf16_f32 v17, v18, v19
v_mfma_f32_16x16x32_bf16 a[48:51], v[192:195], v[168:171], a[48:51]
ds_read_b64_tr_b16 v[220:221], v243 offset:28672
v_cvt_pk_bf16_f32 v18, v20, v21
v_cvt_pk_bf16_f32 v19, v22, v23
v_mfma_f32_16x16x32_bf16 a[56:59], v[192:195], v[172:175], a[56:59]
ds_read_b64_tr_b16 v[222:223], v243 offset:30720
buffer_store_dwordx4 v[16:19], v236, s[36:39], s28 offen offset:2048 sc1
v_mfma_f32_16x16x32_bf16 a[52:55], v[196:199], v[168:171], a[52:55]
ds_read_b64_tr_b16 v[192:193], v248 offset:28672
s_add_u32 s7, s7, 32768
s_cmp_eq_u32 s7, 163840
s_cselect_b32 s7, 0, s7
v_mfma_f32_16x16x32_bf16 a[60:63], v[196:199], v[172:175], a[60:63]
ds_read_b64_tr_b16 v[194:195], v248 offset:30720
s_waitcnt lgkmcnt(6)
v_mfma_f32_16x16x32_bf16 a[32:35], v[200:203], v[176:179], a[32:35]
ds_read_b64_tr_b16 v[196:197], v249 offset:28672
v_accvgpr_read_b32 v24, a24
v_mfma_f32_16x16x32_bf16 a[40:43], v[200:203], v[180:183], a[40:43]
ds_read_b64_tr_b16 v[198:199], v249 offset:30720
v_add_u32 v242, s7, v232
v_add_u32 v243, s7, v233
v_add_u32 v248, s7, v234
v_add_u32 v249, s7, v235
v_mfma_f32_16x16x32_bf16 a[36:39], v[204:207], v[176:179], a[36:39]
s_waitcnt vmcnt(19)
s_barrier
ds_read_b64_tr_b16 v[200:201], v242 offset:0
v_mfma_f32_16x16x32_bf16 a[44:47], v[204:207], v[180:183], a[44:47]
ds_read_b64_tr_b16 v[202:203], v242 offset:2048
v_accvgpr_read_b32 v25, a25
v_mfma_f32_16x16x32_bf16 a[48:51], v[208:211], v[176:179], a[48:51]
ds_read_b64_tr_b16 v[204:205], v243 offset:0
v_accvgpr_read_b32 v26, a26
v_mfma_f32_16x16x32_bf16 a[56:59], v[208:211], v[180:183], a[56:59]
ds_read_b64_tr_b16 v[206:207], v243 offset:2048
v_accvgpr_read_b32 v27, a27
v_mfma_f32_16x16x32_bf16 a[52:55], v[212:215], v[176:179], a[52:55]
ds_read_b64_tr_b16 v[208:209], v248 offset:0
v_accvgpr_read_b32 v28, a28
v_mfma_f32_16x16x32_bf16 a[60:63], v[212:215], v[180:183], a[60:63]
ds_read_b64_tr_b16 v[210:211], v248 offset:2048
s_waitcnt lgkmcnt(6)
v_mfma_f32_16x16x32_bf16 a[32:35], v[216:219], v[184:187], a[32:35]
ds_read_b64_tr_b16 v[212:213], v249 offset:0
v_accvgpr_read_b32 v29, a29
v_mfma_f32_16x16x32_bf16 a[40:43], v[216:219], v[188:191], a[40:43]
)ASM"
R"ASM(
ds_read_b64_tr_b16 v[214:215], v249 offset:2048
v_accvgpr_read_b32 v30, a30
v_mfma_f32_16x16x32_bf16 a[36:39], v[220:223], v[184:187], a[36:39]
ds_read_b64_tr_b16 v[216:217], v242 offset:4096
v_accvgpr_read_b32 v31, a31
v_mfma_f32_16x16x32_bf16 a[44:47], v[220:223], v[188:191], a[44:47]
ds_read_b64_tr_b16 v[218:219], v242 offset:6144
v_cvt_pk_bf16_f32 v24, v24, v25
v_mfma_f32_16x16x32_bf16 a[48:51], v[192:195], v[184:187], a[48:51]
ds_read_b64_tr_b16 v[220:221], v243 offset:4096
v_cvt_pk_bf16_f32 v25, v26, v27
v_mfma_f32_16x16x32_bf16 a[56:59], v[192:195], v[188:191], a[56:59]
ds_read_b64_tr_b16 v[222:223], v243 offset:6144
v_cvt_pk_bf16_f32 v26, v28, v29
v_mfma_f32_16x16x32_bf16 a[52:55], v[196:199], v[184:187], a[52:55]
ds_read_b64_tr_b16 v[192:193], v248 offset:4096
v_cvt_pk_bf16_f32 v27, v30, v31
v_mfma_f32_16x16x32_bf16 a[60:63], v[196:199], v[188:191], a[60:63]
ds_read_b64_tr_b16 v[194:195], v248 offset:6144
buffer_store_dwordx4 v[24:27], v236, s[36:39], s28 offen offset:3072 sc1
s_waitcnt lgkmcnt(6)
v_mfma_f32_16x16x32_bf16 a[64:67], v[200:203], v[128:131], a[64:67]
ds_read_b64_tr_b16 v[196:197], v249 offset:4096
s_add_u32 m0, s8, s68
s_mov_b32 s9, 640
buffer_load_dwordx4 v244, s[44:47], s9 offen offset:0 lds
v_mfma_f32_16x16x32_bf16 a[72:75], v[200:203], v[132:135], a[72:75]
ds_read_b64_tr_b16 v[198:199], v249 offset:6144
v_mfma_f32_16x16x32_bf16 a[68:71], v[204:207], v[128:131], a[68:71]
ds_read_b64_tr_b16 v[200:201], v242 offset:8192
s_add_u32 s28, s80, 4096
v_mfma_f32_16x16x32_bf16 a[76:79], v[204:207], v[132:135], a[76:79]
ds_read_b64_tr_b16 v[202:203], v242 offset:10240
v_accvgpr_read_b32 v32, a32
v_mfma_f32_16x16x32_bf16 a[80:83], v[208:211], v[128:131], a[80:83]
ds_read_b64_tr_b16 v[204:205], v243 offset:8192
v_accvgpr_read_b32 v33, a33
v_mfma_f32_16x16x32_bf16 a[88:91], v[208:211], v[132:135], a[88:91]
ds_read_b64_tr_b16 v[206:207], v243 offset:10240
s_mov_b32 s9, 6784
buffer_load_dwordx4 v244, s[44:47], s9 offen offset:1024 lds
v_mfma_f32_16x16x32_bf16 a[84:87], v[212:215], v[128:131], a[84:87]
ds_read_b64_tr_b16 v[208:209], v248 offset:8192
v_accvgpr_read_b32 v34, a34
v_mfma_f32_16x16x32_bf16 a[92:95], v[212:215], v[132:135], a[92:95]
ds_read_b64_tr_b16 v[210:211], v248 offset:10240
s_waitcnt lgkmcnt(6)
v_mfma_f32_16x16x32_bf16 a[64:67], v[216:219], v[136:139], a[64:67]
ds_read_b64_tr_b16 v[212:213], v249 offset:8192
v_accvgpr_read_b32 v35, a35
v_mfma_f32_16x16x32_bf16 a[72:75], v[216:219], v[140:143], a[72:75]
ds_read_b64_tr_b16 v[214:215], v249 offset:10240
v_accvgpr_read_b32 v36, a36
v_mfma_f32_16x16x32_bf16 a[68:71], v[220:223], v[136:139], a[68:71]
ds_read_b64_tr_b16 v[216:217], v242 offset:12288
s_mov_b32 s9, 12928
buffer_load_dwordx4 v244, s[44:47], s9 offen offset:2048 lds
v_mfma_f32_16x16x32_bf16 a[76:79], v[220:223], v[140:143], a[76:79]
ds_read_b64_tr_b16 v[218:219], v242 offset:14336
v_accvgpr_read_b32 v37, a37
v_mfma_f32_16x16x32_bf16 a[80:83], v[192:195], v[136:139], a[80:83]
ds_read_b64_tr_b16 v[220:221], v243 offset:12288
v_accvgpr_read_b32 v38, a38
v_mfma_f32_16x16x32_bf16 a[88:91], v[192:195], v[140:143], a[88:91]
ds_read_b64_tr_b16 v[222:223], v243 offset:14336
v_accvgpr_read_b32 v39, a39
v_mfma_f32_16x16x32_bf16 a[84:87], v[196:199], v[136:139], a[84:87]
ds_read_b64_tr_b16 v[192:193], v248 offset:12288
v_cvt_pk_bf16_f32 v32, v32, v33
v_mfma_f32_16x16x32_bf16 a[92:95], v[196:199], v[140:143], a[92:95]
ds_read_b64_tr_b16 v[194:195], v248 offset:14336
s_waitcnt lgkmcnt(6)
v_mfma_f32_16x16x32_bf16 a[64:67], v[200:203], v[144:147], a[64:67]
ds_read_b64_tr_b16 v[196:197], v249 offset:12288
s_mov_b32 s9, 19072
buffer_load_dwordx4 v244, s[44:47], s9 offen offset:3072 lds
v_mfma_f32_16x16x32_bf16 a[72:75], v[200:203], v[148:151], a[72:75]
ds_read_b64_tr_b16 v[198:199], v249 offset:14336
v_cvt_pk_bf16_f32 v33, v34, v35
v_mfma_f32_16x16x32_bf16 a[68:71], v[204:207], v[144:147], a[68:71]
ds_read_b64_tr_b16 v[200:201], v242 offset:16384
v_cvt_pk_bf16_f32 v34, v36, v37
v_mfma_f32_16x16x32_bf16 a[76:79], v[204:207], v[148:151], a[76:79]
ds_read_b64_tr_b16 v[202:203], v242 offset:18432
v_cvt_pk_bf16_f32 v35, v38, v39
v_mfma_f32_16x16x32_bf16 a[80:83], v[208:211], v[144:147], a[80:83]
ds_read_b64_tr_b16 v[204:205], v243 offset:16384
buffer_store_dwordx4 v[32:35], v236, s[36:39], s28 offen offset:0 sc1
v_mfma_f32_16x16x32_bf16 a[88:91], v[208:211], v[148:151], a[88:91]
ds_read_b64_tr_b16 v[206:207], v243 offset:18432
s_add_u32 m0, m0, 4096
s_mov_b32 s9, 29312
buffer_load_dwordx4 v244, s[44:47], s9 offen offset:0 lds
v_mfma_f32_16x16x32_bf16 a[84:87], v[212:215], v[144:147], a[84:87]
ds_read_b64_tr_b16 v[208:209], v248 offset:16384
v_accvgpr_read_b32 v40, a40
v_mfma_f32_16x16x32_bf16 a[92:95], v[212:215], v[148:151], a[92:95]
ds_read_b64_tr_b16 v[210:211], v248 offset:18432
s_waitcnt lgkmcnt(6)
v_mfma_f32_16x16x32_bf16 a[64:67], v[216:219], v[152:155], a[64:67]
ds_read_b64_tr_b16 v[212:213], v249 offset:16384
v_accvgpr_read_b32 v41, a41
v_mfma_f32_16x16x32_bf16 a[72:75], v[216:219], v[156:159], a[72:75]
ds_read_b64_tr_b16 v[214:215], v249 offset:18432
v_accvgpr_read_b32 v42, a42
v_mfma_f32_16x16x32_bf16 a[68:71], v[220:223], v[152:155], a[68:71]
ds_read_b64_tr_b16 v[216:217], v242 offset:20480
s_mov_b32 s9, 35456
buffer_load_dwordx4 v244, s[44:47], s9 offen offset:1024 lds
v_mfma_f32_16x16x32_bf16 a[76:79], v[220:223], v[156:159], a[76:79]
ds_read_b64_tr_b16 v[218:219], v242 offset:22528
v_accvgpr_read_b32 v43, a43
v_mfma_f32_16x16x32_bf16 a[80:83], v[192:195], v[152:155], a[80:83]
ds_read_b64_tr_b16 v[220:221], v243 offset:20480
v_accvgpr_read_b32 v44, a44
v_mfma_f32_16x16x32_bf16 a[88:91], v[192:195], v[156:159], a[88:91]
ds_read_b64_tr_b16 v[222:223], v243 offset:22528
v_accvgpr_read_b32 v45, a45
v_mfma_f32_16x16x32_bf16 a[84:87], v[196:199], v[152:155], a[84:87]
ds_read_b64_tr_b16 v[192:193], v248 offset:20480
v_accvgpr_read_b32 v46, a46
v_mfma_f32_16x16x32_bf16 a[92:95], v[196:199], v[156:159], a[92:95]
ds_read_b64_tr_b16 v[194:195], v248 offset:22528
v_accvgpr_read_b32 v47, a47
s_waitcnt lgkmcnt(6)
v_mfma_f32_16x16x32_bf16 a[64:67], v[200:203], v[160:163], a[64:67]
ds_read_b64_tr_b16 v[196:197], v249 offset:20480
s_mov_b32 s9, 41600
buffer_load_dwordx4 v244, s[44:47], s9 offen offset:2048 lds
v_mfma_f32_16x16x32_bf16 a[72:75], v[200:203], v[164:167], a[72:75]
ds_read_b64_tr_b16 v[198:199], v249 offset:22528
v_cvt_pk_bf16_f32 v40, v40, v41
v_cvt_pk_bf16_f32 v41, v42, v43
v_mfma_f32_16x16x32_bf16 a[68:71], v[204:207], v[160:163], a[68:71]
ds_read_b64_tr_b16 v[200:201], v242 offset:24576
v_cvt_pk_bf16_f32 v42, v44, v45
v_cvt_pk_bf16_f32 v43, v46, v47
v_mfma_f32_16x16x32_bf16 a[76:79], v[204:207], v[164:167], a[76:79]
ds_read_b64_tr_b16 v[202:203], v242 offset:26624
buffer_store_dwordx4 v[40:43], v236, s[36:39], s28 offen offset:1024 sc1
v_mfma_f32_16x16x32_bf16 a[80:83], v[208:211], v[160:163], a[80:83]
ds_read_b64_tr_b16 v[204:205], v243 offset:24576
v_accvgpr_read_b32 v48, a48
v_mfma_f32_16x16x32_bf16 a[88:91], v[208:211], v[164:167], a[88:91]
ds_read_b64_tr_b16 v[206:207], v243 offset:26624
s_mov_b32 s9, 47744
buffer_load_dwordx4 v244, s[44:47], s9 offen offset:3072 lds
v_mfma_f32_16x16x32_bf16 a[84:87], v[212:215], v[160:163], a[84:87]
ds_read_b64_tr_b16 v[208:209], v248 offset:24576
v_accvgpr_read_b32 v49, a49
v_accvgpr_read_b32 v50, a50
v_mfma_f32_16x16x32_bf16 a[92:95], v[212:215], v[164:167], a[92:95]
ds_read_b64_tr_b16 v[210:211], v248 offset:26624
v_accvgpr_read_b32 v51, a51
s_waitcnt lgkmcnt(6)
v_mfma_f32_16x16x32_bf16 a[64:67], v[216:219], v[168:171], a[64:67]
ds_read_b64_tr_b16 v[212:213], v249 offset:24576
v_accvgpr_read_b32 v52, a52
v_accvgpr_read_b32 v53, a53
v_mfma_f32_16x16x32_bf16 a[72:75], v[216:219], v[172:175], a[72:75]
ds_read_b64_tr_b16 v[214:215], v249 offset:26624
v_accvgpr_read_b32 v54, a54
v_accvgpr_read_b32 v55, a55
v_mfma_f32_16x16x32_bf16 a[68:71], v[220:223], v[168:171], a[68:71]
ds_read_b64_tr_b16 v[216:217], v242 offset:28672
s_add_u32 s8, s8, 32768
s_cmp_eq_u32 s8, 163840
s_cselect_b32 s8, 0, s8
v_mfma_f32_16x16x32_bf16 a[76:79], v[220:223], v[172:175], a[76:79]
ds_read_b64_tr_b16 v[218:219], v242 offset:30720
v_cvt_pk_bf16_f32 v48, v48, v49
v_cvt_pk_bf16_f32 v49, v50, v51
v_mfma_f32_16x16x32_bf16 a[80:83], v[192:195], v[168:171], a[80:83]
ds_read_b64_tr_b16 v[220:221], v243 offset:28672
v_cvt_pk_bf16_f32 v50, v52, v53
v_cvt_pk_bf16_f32 v51, v54, v55
v_mfma_f32_16x16x32_bf16 a[88:91], v[192:195], v[172:175], a[88:91]
ds_read_b64_tr_b16 v[222:223], v243 offset:30720
buffer_store_dwordx4 v[48:51], v236, s[36:39], s28 offen offset:2048 sc1
v_mfma_f32_16x16x32_bf16 a[84:87], v[196:199], v[168:171], a[84:87]
ds_read_b64_tr_b16 v[192:193], v248 offset:28672
s_add_u32 s7, s7, 32768
s_cmp_eq_u32 s7, 163840
s_cselect_b32 s7, 0, s7
v_mfma_f32_16x16x32_bf16 a[92:95], v[196:199], v[172:175], a[92:95]
ds_read_b64_tr_b16 v[194:195], v248 offset:30720
s_waitcnt lgkmcnt(6)
v_mfma_f32_16x16x32_bf16 a[64:67], v[200:203], v[176:179], a[64:67]
ds_read_b64_tr_b16 v[196:197], v249 offset:28672
v_accvgpr_read_b32 v56, a56
v_mfma_f32_16x16x32_bf16 a[72:75], v[200:203], v[180:183], a[72:75]
ds_read_b64_tr_b16 v[198:199], v249 offset:30720
v_add_u32 v242, s7, v232
v_add_u32 v243, s7, v233
v_add_u32 v248, s7, v234
v_add_u32 v249, s7, v235
v_mfma_f32_16x16x32_bf16 a[68:71], v[204:207], v[176:179], a[68:71]
s_waitcnt vmcnt(23)
s_barrier
ds_read_b64_tr_b16 v[200:201], v242 offset:0
)ASM"
R"ASM(
v_mfma_f32_16x16x32_bf16 a[76:79], v[204:207], v[180:183], a[76:79]
ds_read_b64_tr_b16 v[202:203], v242 offset:2048
v_accvgpr_read_b32 v57, a57
v_mfma_f32_16x16x32_bf16 a[80:83], v[208:211], v[176:179], a[80:83]
ds_read_b64_tr_b16 v[204:205], v243 offset:0
v_accvgpr_read_b32 v58, a58
v_mfma_f32_16x16x32_bf16 a[88:91], v[208:211], v[180:183], a[88:91]
ds_read_b64_tr_b16 v[206:207], v243 offset:2048
v_accvgpr_read_b32 v59, a59
v_mfma_f32_16x16x32_bf16 a[84:87], v[212:215], v[176:179], a[84:87]
ds_read_b64_tr_b16 v[208:209], v248 offset:0
v_accvgpr_read_b32 v60, a60
v_mfma_f32_16x16x32_bf16 a[92:95], v[212:215], v[180:183], a[92:95]
ds_read_b64_tr_b16 v[210:211], v248 offset:2048
s_waitcnt lgkmcnt(6)
v_mfma_f32_16x16x32_bf16 a[64:67], v[216:219], v[184:187], a[64:67]
ds_read_b64_tr_b16 v[212:213], v249 offset:0
v_accvgpr_read_b32 v61, a61
v_mfma_f32_16x16x32_bf16 a[72:75], v[216:219], v[188:191], a[72:75]
ds_read_b64_tr_b16 v[214:215], v249 offset:2048
v_accvgpr_read_b32 v62, a62
v_mfma_f32_16x16x32_bf16 a[68:71], v[220:223], v[184:187], a[68:71]
ds_read_b64_tr_b16 v[216:217], v242 offset:4096
v_accvgpr_read_b32 v63, a63
v_mfma_f32_16x16x32_bf16 a[76:79], v[220:223], v[188:191], a[76:79]
ds_read_b64_tr_b16 v[218:219], v242 offset:6144
v_cvt_pk_bf16_f32 v56, v56, v57
v_mfma_f32_16x16x32_bf16 a[80:83], v[192:195], v[184:187], a[80:83]
ds_read_b64_tr_b16 v[220:221], v243 offset:4096
v_cvt_pk_bf16_f32 v57, v58, v59
v_mfma_f32_16x16x32_bf16 a[88:91], v[192:195], v[188:191], a[88:91]
ds_read_b64_tr_b16 v[222:223], v243 offset:6144
v_cvt_pk_bf16_f32 v58, v60, v61
v_mfma_f32_16x16x32_bf16 a[84:87], v[196:199], v[184:187], a[84:87]
ds_read_b64_tr_b16 v[192:193], v248 offset:4096
v_cvt_pk_bf16_f32 v59, v62, v63
v_mfma_f32_16x16x32_bf16 a[92:95], v[196:199], v[188:191], a[92:95]
ds_read_b64_tr_b16 v[194:195], v248 offset:6144
buffer_store_dwordx4 v[56:59], v236, s[36:39], s28 offen offset:3072 sc1
s_waitcnt lgkmcnt(6)
v_mfma_f32_16x16x32_bf16 a[96:99], v[200:203], v[128:131], a[96:99]
ds_read_b64_tr_b16 v[196:197], v249 offset:4096
s_add_u32 m0, s8, s68
s_mov_b32 s9, 768
buffer_load_dwordx4 v244, s[44:47], s9 offen offset:0 lds
v_mfma_f32_16x16x32_bf16 a[104:107], v[200:203], v[132:135], a[104:107]
ds_read_b64_tr_b16 v[198:199], v249 offset:6144
v_mfma_f32_16x16x32_bf16 a[100:103], v[204:207], v[128:131], a[100:103]
ds_read_b64_tr_b16 v[200:201], v242 offset:8192
s_add_u32 s28, s80, 8192
v_mfma_f32_16x16x32_bf16 a[108:111], v[204:207], v[132:135], a[108:111]
ds_read_b64_tr_b16 v[202:203], v242 offset:10240
v_accvgpr_read_b32 v64, a64
v_mfma_f32_16x16x32_bf16 a[112:115], v[208:211], v[128:131], a[112:115]
ds_read_b64_tr_b16 v[204:205], v243 offset:8192
v_accvgpr_read_b32 v65, a65
v_mfma_f32_16x16x32_bf16 a[120:123], v[208:211], v[132:135], a[120:123]
ds_read_b64_tr_b16 v[206:207], v243 offset:10240
s_mov_b32 s9, 6912
buffer_load_dwordx4 v244, s[44:47], s9 offen offset:1024 lds
v_mfma_f32_16x16x32_bf16 a[116:119], v[212:215], v[128:131], a[116:119]
ds_read_b64_tr_b16 v[208:209], v248 offset:8192
v_accvgpr_read_b32 v66, a66
v_mfma_f32_16x16x32_bf16 a[124:127], v[212:215], v[132:135], a[124:127]
ds_read_b64_tr_b16 v[210:211], v248 offset:10240
s_waitcnt lgkmcnt(6)
v_mfma_f32_16x16x32_bf16 a[96:99], v[216:219], v[136:139], a[96:99]
ds_read_b64_tr_b16 v[212:213], v249 offset:8192
v_accvgpr_read_b32 v67, a67
v_mfma_f32_16x16x32_bf16 a[104:107], v[216:219], v[140:143], a[104:107]
ds_read_b64_tr_b16 v[214:215], v249 offset:10240
v_accvgpr_read_b32 v68, a68
v_mfma_f32_16x16x32_bf16 a[100:103], v[220:223], v[136:139], a[100:103]
ds_read_b64_tr_b16 v[216:217], v242 offset:12288
s_mov_b32 s9, 13056
buffer_load_dwordx4 v244, s[44:47], s9 offen offset:2048 lds
v_mfma_f32_16x16x32_bf16 a[108:111], v[220:223], v[140:143], a[108:111]
ds_read_b64_tr_b16 v[218:219], v242 offset:14336
v_accvgpr_read_b32 v69, a69
v_mfma_f32_16x16x32_bf16 a[112:115], v[192:195], v[136:139], a[112:115]
ds_read_b64_tr_b16 v[220:221], v243 offset:12288
v_accvgpr_read_b32 v70, a70
v_mfma_f32_16x16x32_bf16 a[120:123], v[192:195], v[140:143], a[120:123]
ds_read_b64_tr_b16 v[222:223], v243 offset:14336
v_accvgpr_read_b32 v71, a71
v_mfma_f32_16x16x32_bf16 a[116:119], v[196:199], v[136:139], a[116:119]
ds_read_b64_tr_b16 v[192:193], v248 offset:12288
v_cvt_pk_bf16_f32 v64, v64, v65
v_mfma_f32_16x16x32_bf16 a[124:127], v[196:199], v[140:143], a[124:127]
ds_read_b64_tr_b16 v[194:195], v248 offset:14336
s_waitcnt lgkmcnt(6)
v_mfma_f32_16x16x32_bf16 a[96:99], v[200:203], v[144:147], a[96:99]
ds_read_b64_tr_b16 v[196:197], v249 offset:12288
s_mov_b32 s9, 19200
buffer_load_dwordx4 v244, s[44:47], s9 offen offset:3072 lds
v_mfma_f32_16x16x32_bf16 a[104:107], v[200:203], v[148:151], a[104:107]
ds_read_b64_tr_b16 v[198:199], v249 offset:14336
v_cvt_pk_bf16_f32 v65, v66, v67
v_mfma_f32_16x16x32_bf16 a[100:103], v[204:207], v[144:147], a[100:103]
ds_read_b64_tr_b16 v[200:201], v242 offset:16384
v_cvt_pk_bf16_f32 v66, v68, v69
v_mfma_f32_16x16x32_bf16 a[108:111], v[204:207], v[148:151], a[108:111]
ds_read_b64_tr_b16 v[202:203], v242 offset:18432
v_cvt_pk_bf16_f32 v67, v70, v71
v_mfma_f32_16x16x32_bf16 a[112:115], v[208:211], v[144:147], a[112:115]
ds_read_b64_tr_b16 v[204:205], v243 offset:16384
buffer_store_dwordx4 v[64:67], v236, s[36:39], s28 offen offset:0 sc1
v_mfma_f32_16x16x32_bf16 a[120:123], v[208:211], v[148:151], a[120:123]
ds_read_b64_tr_b16 v[206:207], v243 offset:18432
s_add_u32 m0, m0, 4096
s_mov_b32 s9, 29440
buffer_load_dwordx4 v244, s[44:47], s9 offen offset:0 lds
v_mfma_f32_16x16x32_bf16 a[116:119], v[212:215], v[144:147], a[116:119]
ds_read_b64_tr_b16 v[208:209], v248 offset:16384
v_accvgpr_read_b32 v72, a72
v_mfma_f32_16x16x32_bf16 a[124:127], v[212:215], v[148:151], a[124:127]
ds_read_b64_tr_b16 v[210:211], v248 offset:18432
s_waitcnt lgkmcnt(6)
v_mfma_f32_16x16x32_bf16 a[96:99], v[216:219], v[152:155], a[96:99]
ds_read_b64_tr_b16 v[212:213], v249 offset:16384
v_accvgpr_read_b32 v73, a73
v_mfma_f32_16x16x32_bf16 a[104:107], v[216:219], v[156:159], a[104:107]
ds_read_b64_tr_b16 v[214:215], v249 offset:18432
v_accvgpr_read_b32 v74, a74
v_mfma_f32_16x16x32_bf16 a[100:103], v[220:223], v[152:155], a[100:103]
ds_read_b64_tr_b16 v[216:217], v242 offset:20480
s_mov_b32 s9, 35584
buffer_load_dwordx4 v244, s[44:47], s9 offen offset:1024 lds
v_mfma_f32_16x16x32_bf16 a[108:111], v[220:223], v[156:159], a[108:111]
ds_read_b64_tr_b16 v[218:219], v242 offset:22528
v_accvgpr_read_b32 v75, a75
v_mfma_f32_16x16x32_bf16 a[112:115], v[192:195], v[152:155], a[112:115]
ds_read_b64_tr_b16 v[220:221], v243 offset:20480
v_accvgpr_read_b32 v76, a76
v_mfma_f32_16x16x32_bf16 a[120:123], v[192:195], v[156:159], a[120:123]
ds_read_b64_tr_b16 v[222:223], v243 offset:22528
v_accvgpr_read_b32 v77, a77
v_mfma_f32_16x16x32_bf16 a[116:119], v[196:199], v[152:155], a[116:119]
ds_read_b64_tr_b16 v[192:193], v248 offset:20480
v_accvgpr_read_b32 v78, a78
v_mfma_f32_16x16x32_bf16 a[124:127], v[196:199], v[156:159], a[124:127]
ds_read_b64_tr_b16 v[194:195], v248 offset:22528
v_accvgpr_read_b32 v79, a79
s_waitcnt lgkmcnt(6)
v_mfma_f32_16x16x32_bf16 a[96:99], v[200:203], v[160:163], a[96:99]
ds_read_b64_tr_b16 v[196:197], v249 offset:20480
s_mov_b32 s9, 41728
buffer_load_dwordx4 v244, s[44:47], s9 offen offset:2048 lds
v_mfma_f32_16x16x32_bf16 a[104:107], v[200:203], v[164:167], a[104:107]
ds_read_b64_tr_b16 v[198:199], v249 offset:22528
v_cvt_pk_bf16_f32 v72, v72, v73
v_cvt_pk_bf16_f32 v73, v74, v75
v_mfma_f32_16x16x32_bf16 a[100:103], v[204:207], v[160:163], a[100:103]
ds_read_b64_tr_b16 v[200:201], v242 offset:24576
v_cvt_pk_bf16_f32 v74, v76, v77
v_cvt_pk_bf16_f32 v75, v78, v79
v_mfma_f32_16x16x32_bf16 a[108:111], v[204:207], v[164:167], a[108:111]
ds_read_b64_tr_b16 v[202:203], v242 offset:26624
buffer_store_dwordx4 v[72:75], v236, s[36:39], s28 offen offset:1024 sc1
v_mfma_f32_16x16x32_bf16 a[112:115], v[208:211], v[160:163], a[112:115]
ds_read_b64_tr_b16 v[204:205], v243 offset:24576
v_accvgpr_read_b32 v80, a80
v_mfma_f32_16x16x32_bf16 a[120:123], v[208:211], v[164:167], a[120:123]
ds_read_b64_tr_b16 v[206:207], v243 offset:26624
s_mov_b32 s9, 47872
buffer_load_dwordx4 v244, s[44:47], s9 offen offset:3072 lds
v_mfma_f32_16x16x32_bf16 a[116:119], v[212:215], v[160:163], a[116:119]
ds_read_b64_tr_b16 v[208:209], v248 offset:24576
v_accvgpr_read_b32 v81, a81
v_accvgpr_read_b32 v82, a82
v_mfma_f32_16x16x32_bf16 a[124:127], v[212:215], v[164:167], a[124:127]
ds_read_b64_tr_b16 v[210:211], v248 offset:26624
v_accvgpr_read_b32 v83, a83
s_waitcnt lgkmcnt(6)
v_mfma_f32_16x16x32_bf16 a[96:99], v[216:219], v[168:171], a[96:99]
ds_read_b64_tr_b16 v[212:213], v249 offset:24576
v_accvgpr_read_b32 v84, a84
v_accvgpr_read_b32 v85, a85
v_mfma_f32_16x16x32_bf16 a[104:107], v[216:219], v[172:175], a[104:107]
ds_read_b64_tr_b16 v[214:215], v249 offset:26624
v_accvgpr_read_b32 v86, a86
v_accvgpr_read_b32 v87, a87
v_mfma_f32_16x16x32_bf16 a[100:103], v[220:223], v[168:171], a[100:103]
ds_read_b64_tr_b16 v[216:217], v242 offset:28672
s_add_u32 s8, s8, 32768
s_cmp_eq_u32 s8, 163840
s_cselect_b32 s8, 0, s8
v_mfma_f32_16x16x32_bf16 a[108:111], v[220:223], v[172:175], a[108:111]
ds_read_b64_tr_b16 v[218:219], v242 offset:30720
v_cvt_pk_bf16_f32 v80, v80, v81
v_cvt_pk_bf16_f32 v81, v82, v83
v_mfma_f32_16x16x32_bf16 a[112:115], v[192:195], v[168:171], a[112:115]
ds_read_b64_tr_b16 v[220:221], v243 offset:28672
v_cvt_pk_bf16_f32 v82, v84, v85
v_cvt_pk_bf16_f32 v83, v86, v87
v_mfma_f32_16x16x32_bf16 a[120:123], v[192:195], v[172:175], a[120:123]
ds_read_b64_tr_b16 v[222:223], v243 offset:30720
buffer_store_dwordx4 v[80:83], v236, s[36:39], s28 offen offset:2048 sc1
v_mfma_f32_16x16x32_bf16 a[116:119], v[196:199], v[168:171], a[116:119]
ds_read_b64_tr_b16 v[192:193], v248 offset:28672
)ASM"
R"ASM(
s_add_u32 s7, s7, 32768
s_cmp_eq_u32 s7, 163840
s_cselect_b32 s7, 0, s7
v_mfma_f32_16x16x32_bf16 a[124:127], v[196:199], v[172:175], a[124:127]
ds_read_b64_tr_b16 v[194:195], v248 offset:30720
s_waitcnt lgkmcnt(6)
v_mfma_f32_16x16x32_bf16 a[96:99], v[200:203], v[176:179], a[96:99]
ds_read_b64_tr_b16 v[196:197], v249 offset:28672
v_accvgpr_read_b32 v88, a88
v_mfma_f32_16x16x32_bf16 a[104:107], v[200:203], v[180:183], a[104:107]
ds_read_b64_tr_b16 v[198:199], v249 offset:30720
v_add_u32 v242, s7, v232
v_add_u32 v243, s7, v233
v_add_u32 v248, s7, v234
v_add_u32 v249, s7, v235
v_mfma_f32_16x16x32_bf16 a[100:103], v[204:207], v[176:179], a[100:103]
s_waitcnt vmcnt(25)
s_barrier
ds_read_b64_tr_b16 v[200:201], v242 offset:0
v_mfma_f32_16x16x32_bf16 a[108:111], v[204:207], v[180:183], a[108:111]
ds_read_b64_tr_b16 v[202:203], v242 offset:2048
v_accvgpr_read_b32 v89, a89
v_mfma_f32_16x16x32_bf16 a[112:115], v[208:211], v[176:179], a[112:115]
ds_read_b64_tr_b16 v[204:205], v243 offset:0
v_accvgpr_read_b32 v90, a90
v_mfma_f32_16x16x32_bf16 a[120:123], v[208:211], v[180:183], a[120:123]
ds_read_b64_tr_b16 v[206:207], v243 offset:2048
v_accvgpr_read_b32 v91, a91
v_mfma_f32_16x16x32_bf16 a[116:119], v[212:215], v[176:179], a[116:119]
ds_read_b64_tr_b16 v[208:209], v248 offset:0
v_accvgpr_read_b32 v92, a92
v_mfma_f32_16x16x32_bf16 a[124:127], v[212:215], v[180:183], a[124:127]
ds_read_b64_tr_b16 v[210:211], v248 offset:2048
s_waitcnt lgkmcnt(6)
v_mfma_f32_16x16x32_bf16 a[96:99], v[216:219], v[184:187], a[96:99]
ds_read_b64_tr_b16 v[212:213], v249 offset:0
v_accvgpr_read_b32 v93, a93
v_mfma_f32_16x16x32_bf16 a[104:107], v[216:219], v[188:191], a[104:107]
ds_read_b64_tr_b16 v[214:215], v249 offset:2048
v_accvgpr_read_b32 v94, a94
v_mfma_f32_16x16x32_bf16 a[100:103], v[220:223], v[184:187], a[100:103]
ds_read_b64_tr_b16 v[216:217], v242 offset:4096
v_accvgpr_read_b32 v95, a95
v_mfma_f32_16x16x32_bf16 a[108:111], v[220:223], v[188:191], a[108:111]
ds_read_b64_tr_b16 v[218:219], v242 offset:6144
v_cvt_pk_bf16_f32 v88, v88, v89
v_mfma_f32_16x16x32_bf16 a[112:115], v[192:195], v[184:187], a[112:115]
ds_read_b64_tr_b16 v[220:221], v243 offset:4096
v_cvt_pk_bf16_f32 v89, v90, v91
v_mfma_f32_16x16x32_bf16 a[120:123], v[192:195], v[188:191], a[120:123]
ds_read_b64_tr_b16 v[222:223], v243 offset:6144
v_cvt_pk_bf16_f32 v90, v92, v93
v_mfma_f32_16x16x32_bf16 a[116:119], v[196:199], v[184:187], a[116:119]
ds_read_b64_tr_b16 v[192:193], v248 offset:4096
v_cvt_pk_bf16_f32 v91, v94, v95
v_mfma_f32_16x16x32_bf16 a[124:127], v[196:199], v[188:191], a[124:127]
ds_read_b64_tr_b16 v[194:195], v248 offset:6144
buffer_store_dwordx4 v[88:91], v236, s[36:39], s28 offen offset:3072 sc1
s_waitcnt lgkmcnt(6)
v_mfma_f32_16x16x32_bf16 a[128:131], v[200:203], v[128:131], a[128:131]
ds_read_b64_tr_b16 v[196:197], v249 offset:4096
v_mfma_f32_16x16x32_bf16 a[136:139], v[200:203], v[132:135], a[136:139]
ds_read_b64_tr_b16 v[198:199], v249 offset:6144
v_mfma_f32_16x16x32_bf16 a[132:135], v[204:207], v[128:131], a[132:135]
ds_read_b64_tr_b16 v[200:201], v242 offset:8192
s_add_u32 s28, s80, 12288
v_mfma_f32_16x16x32_bf16 a[140:143], v[204:207], v[132:135], a[140:143]
ds_read_b64_tr_b16 v[202:203], v242 offset:10240
v_accvgpr_read_b32 v96, a96
v_mfma_f32_16x16x32_bf16 a[144:147], v[208:211], v[128:131], a[144:147]
ds_read_b64_tr_b16 v[204:205], v243 offset:8192
v_accvgpr_read_b32 v97, a97
v_mfma_f32_16x16x32_bf16 a[152:155], v[208:211], v[132:135], a[152:155]
ds_read_b64_tr_b16 v[206:207], v243 offset:10240
v_accvgpr_read_b32 v98, a98
v_mfma_f32_16x16x32_bf16 a[148:151], v[212:215], v[128:131], a[148:151]
ds_read_b64_tr_b16 v[208:209], v248 offset:8192
v_accvgpr_read_b32 v99, a99
v_mfma_f32_16x16x32_bf16 a[156:159], v[212:215], v[132:135], a[156:159]
ds_read_b64_tr_b16 v[210:211], v248 offset:10240
s_waitcnt lgkmcnt(6)
v_mfma_f32_16x16x32_bf16 a[128:131], v[216:219], v[136:139], a[128:131]
ds_read_b64_tr_b16 v[212:213], v249 offset:8192
v_accvgpr_read_b32 v100, a100
v_mfma_f32_16x16x32_bf16 a[136:139], v[216:219], v[140:143], a[136:139]
ds_read_b64_tr_b16 v[214:215], v249 offset:10240
v_accvgpr_read_b32 v101, a101
v_mfma_f32_16x16x32_bf16 a[132:135], v[220:223], v[136:139], a[132:135]
ds_read_b64_tr_b16 v[216:217], v242 offset:12288
v_accvgpr_read_b32 v102, a102
v_mfma_f32_16x16x32_bf16 a[140:143], v[220:223], v[140:143], a[140:143]
ds_read_b64_tr_b16 v[218:219], v242 offset:14336
v_accvgpr_read_b32 v103, a103
v_mfma_f32_16x16x32_bf16 a[144:147], v[192:195], v[136:139], a[144:147]
ds_read_b64_tr_b16 v[220:221], v243 offset:12288
v_cvt_pk_bf16_f32 v96, v96, v97
v_mfma_f32_16x16x32_bf16 a[152:155], v[192:195], v[140:143], a[152:155]
ds_read_b64_tr_b16 v[222:223], v243 offset:14336
v_cvt_pk_bf16_f32 v97, v98, v99
v_mfma_f32_16x16x32_bf16 a[148:151], v[196:199], v[136:139], a[148:151]
ds_read_b64_tr_b16 v[192:193], v248 offset:12288
v_cvt_pk_bf16_f32 v98, v100, v101
v_mfma_f32_16x16x32_bf16 a[156:159], v[196:199], v[140:143], a[156:159]
ds_read_b64_tr_b16 v[194:195], v248 offset:14336
s_waitcnt lgkmcnt(6)
v_mfma_f32_16x16x32_bf16 a[128:131], v[200:203], v[144:147], a[128:131]
ds_read_b64_tr_b16 v[196:197], v249 offset:12288
v_cvt_pk_bf16_f32 v99, v102, v103
v_mfma_f32_16x16x32_bf16 a[136:139], v[200:203], v[148:151], a[136:139]
ds_read_b64_tr_b16 v[198:199], v249 offset:14336
buffer_store_dwordx4 v[96:99], v236, s[36:39], s28 offen offset:0 sc1
v_mfma_f32_16x16x32_bf16 a[132:135], v[204:207], v[144:147], a[132:135]
ds_read_b64_tr_b16 v[200:201], v242 offset:16384
v_accvgpr_read_b32 v104, a104
v_mfma_f32_16x16x32_bf16 a[140:143], v[204:207], v[148:151], a[140:143]
ds_read_b64_tr_b16 v[202:203], v242 offset:18432
v_accvgpr_read_b32 v105, a105
v_mfma_f32_16x16x32_bf16 a[144:147], v[208:211], v[144:147], a[144:147]
ds_read_b64_tr_b16 v[204:205], v243 offset:16384
v_accvgpr_read_b32 v106, a106
v_mfma_f32_16x16x32_bf16 a[152:155], v[208:211], v[148:151], a[152:155]
ds_read_b64_tr_b16 v[206:207], v243 offset:18432
v_accvgpr_read_b32 v107, a107
v_mfma_f32_16x16x32_bf16 a[148:151], v[212:215], v[144:147], a[148:151]
ds_read_b64_tr_b16 v[208:209], v248 offset:16384
v_accvgpr_read_b32 v108, a108
v_mfma_f32_16x16x32_bf16 a[156:159], v[212:215], v[148:151], a[156:159]
ds_read_b64_tr_b16 v[210:211], v248 offset:18432
s_waitcnt lgkmcnt(6)
v_mfma_f32_16x16x32_bf16 a[128:131], v[216:219], v[152:155], a[128:131]
ds_read_b64_tr_b16 v[212:213], v249 offset:16384
v_accvgpr_read_b32 v109, a109
v_mfma_f32_16x16x32_bf16 a[136:139], v[216:219], v[156:159], a[136:139]
ds_read_b64_tr_b16 v[214:215], v249 offset:18432
v_accvgpr_read_b32 v110, a110
v_mfma_f32_16x16x32_bf16 a[132:135], v[220:223], v[152:155], a[132:135]
ds_read_b64_tr_b16 v[216:217], v242 offset:20480
v_accvgpr_read_b32 v111, a111
v_mfma_f32_16x16x32_bf16 a[140:143], v[220:223], v[156:159], a[140:143]
ds_read_b64_tr_b16 v[218:219], v242 offset:22528
v_cvt_pk_bf16_f32 v104, v104, v105
v_mfma_f32_16x16x32_bf16 a[144:147], v[192:195], v[152:155], a[144:147]
ds_read_b64_tr_b16 v[220:221], v243 offset:20480
v_cvt_pk_bf16_f32 v105, v106, v107
v_mfma_f32_16x16x32_bf16 a[152:155], v[192:195], v[156:159], a[152:155]
ds_read_b64_tr_b16 v[222:223], v243 offset:22528
v_cvt_pk_bf16_f32 v106, v108, v109
v_mfma_f32_16x16x32_bf16 a[148:151], v[196:199], v[152:155], a[148:151]
ds_read_b64_tr_b16 v[192:193], v248 offset:20480
v_cvt_pk_bf16_f32 v107, v110, v111
v_mfma_f32_16x16x32_bf16 a[156:159], v[196:199], v[156:159], a[156:159]
ds_read_b64_tr_b16 v[194:195], v248 offset:22528
s_waitcnt lgkmcnt(6)
v_mfma_f32_16x16x32_bf16 a[128:131], v[200:203], v[160:163], a[128:131]
ds_read_b64_tr_b16 v[196:197], v249 offset:20480
buffer_store_dwordx4 v[104:107], v236, s[36:39], s28 offen offset:1024 sc1
v_mfma_f32_16x16x32_bf16 a[136:139], v[200:203], v[164:167], a[136:139]
ds_read_b64_tr_b16 v[198:199], v249 offset:22528
v_accvgpr_read_b32 v112, a112
v_mfma_f32_16x16x32_bf16 a[132:135], v[204:207], v[160:163], a[132:135]
ds_read_b64_tr_b16 v[200:201], v242 offset:24576
v_accvgpr_read_b32 v113, a113
v_mfma_f32_16x16x32_bf16 a[140:143], v[204:207], v[164:167], a[140:143]
ds_read_b64_tr_b16 v[202:203], v242 offset:26624
v_accvgpr_read_b32 v114, a114
v_mfma_f32_16x16x32_bf16 a[144:147], v[208:211], v[160:163], a[144:147]
ds_read_b64_tr_b16 v[204:205], v243 offset:24576
v_accvgpr_read_b32 v115, a115
v_mfma_f32_16x16x32_bf16 a[152:155], v[208:211], v[164:167], a[152:155]
ds_read_b64_tr_b16 v[206:207], v243 offset:26624
v_accvgpr_read_b32 v116, a116
v_mfma_f32_16x16x32_bf16 a[148:151], v[212:215], v[160:163], a[148:151]
ds_read_b64_tr_b16 v[208:209], v248 offset:24576
v_accvgpr_read_b32 v117, a117
v_mfma_f32_16x16x32_bf16 a[156:159], v[212:215], v[164:167], a[156:159]
ds_read_b64_tr_b16 v[210:211], v248 offset:26624
s_waitcnt lgkmcnt(6)
v_mfma_f32_16x16x32_bf16 a[128:131], v[216:219], v[168:171], a[128:131]
ds_read_b64_tr_b16 v[212:213], v249 offset:24576
v_accvgpr_read_b32 v118, a118
v_mfma_f32_16x16x32_bf16 a[136:139], v[216:219], v[172:175], a[136:139]
ds_read_b64_tr_b16 v[214:215], v249 offset:26624
v_accvgpr_read_b32 v119, a119
v_mfma_f32_16x16x32_bf16 a[132:135], v[220:223], v[168:171], a[132:135]
ds_read_b64_tr_b16 v[216:217], v242 offset:28672
v_cvt_pk_bf16_f32 v112, v112, v113
v_mfma_f32_16x16x32_bf16 a[140:143], v[220:223], v[172:175], a[140:143]
ds_read_b64_tr_b16 v[218:219], v242 offset:30720
v_cvt_pk_bf16_f32 v113, v114, v115
v_mfma_f32_16x16x32_bf16 a[144:147], v[192:195], v[168:171], a[144:147]
ds_read_b64_tr_b16 v[220:221], v243 offset:28672
v_cvt_pk_bf16_f32 v114, v116, v117
v_mfma_f32_16x16x32_bf16 a[152:155], v[192:195], v[172:175], a[152:155]
ds_read_b64_tr_b16 v[222:223], v243 offset:30720
v_cvt_pk_bf16_f32 v115, v118, v119
v_mfma_f32_16x16x32_bf16 a[148:151], v[196:199], v[168:171], a[148:151]
ds_read_b64_tr_b16 v[192:193], v248 offset:28672
s_add_u32 s7, s7, 32768
s_cmp_eq_u32 s7, 163840
s_cselect_b32 s7, 0, s7
)ASM"
R"ASM(
v_mfma_f32_16x16x32_bf16 a[156:159], v[196:199], v[172:175], a[156:159]
ds_read_b64_tr_b16 v[194:195], v248 offset:30720
s_waitcnt lgkmcnt(6)
v_mfma_f32_16x16x32_bf16 a[128:131], v[200:203], v[176:179], a[128:131]
ds_read_b64_tr_b16 v[196:197], v249 offset:28672
buffer_store_dwordx4 v[112:115], v236, s[36:39], s28 offen offset:2048 sc1
v_mfma_f32_16x16x32_bf16 a[136:139], v[200:203], v[180:183], a[136:139]
ds_read_b64_tr_b16 v[198:199], v249 offset:30720
v_add_u32 v242, s7, v232
v_add_u32 v243, s7, v233
v_add_u32 v248, s7, v234
v_add_u32 v249, s7, v235
v_mfma_f32_16x16x32_bf16 a[132:135], v[204:207], v[176:179], a[132:135]
s_waitcnt vmcnt(17)
s_barrier
ds_read_b64_tr_b16 v[200:201], v242 offset:0
v_mfma_f32_16x16x32_bf16 a[140:143], v[204:207], v[180:183], a[140:143]
ds_read_b64_tr_b16 v[202:203], v242 offset:2048
v_accvgpr_read_b32 v120, a120
v_mfma_f32_16x16x32_bf16 a[144:147], v[208:211], v[176:179], a[144:147]
ds_read_b64_tr_b16 v[204:205], v243 offset:0
v_accvgpr_read_b32 v121, a121
v_mfma_f32_16x16x32_bf16 a[152:155], v[208:211], v[180:183], a[152:155]
ds_read_b64_tr_b16 v[206:207], v243 offset:2048
v_accvgpr_read_b32 v122, a122
v_mfma_f32_16x16x32_bf16 a[148:151], v[212:215], v[176:179], a[148:151]
ds_read_b64_tr_b16 v[208:209], v248 offset:0
v_accvgpr_read_b32 v123, a123
v_mfma_f32_16x16x32_bf16 a[156:159], v[212:215], v[180:183], a[156:159]
ds_read_b64_tr_b16 v[210:211], v248 offset:2048
s_waitcnt lgkmcnt(6)
v_mfma_f32_16x16x32_bf16 a[128:131], v[216:219], v[184:187], a[128:131]
ds_read_b64_tr_b16 v[212:213], v249 offset:0
v_accvgpr_read_b32 v124, a124
v_mfma_f32_16x16x32_bf16 a[136:139], v[216:219], v[188:191], a[136:139]
ds_read_b64_tr_b16 v[214:215], v249 offset:2048
v_accvgpr_read_b32 v125, a125
v_mfma_f32_16x16x32_bf16 a[132:135], v[220:223], v[184:187], a[132:135]
ds_read_b64_tr_b16 v[216:217], v242 offset:4096
v_accvgpr_read_b32 v126, a126
v_mfma_f32_16x16x32_bf16 a[140:143], v[220:223], v[188:191], a[140:143]
ds_read_b64_tr_b16 v[218:219], v242 offset:6144
v_accvgpr_read_b32 v127, a127
v_cvt_pk_bf16_f32 v120, v120, v121
v_mfma_f32_16x16x32_bf16 a[144:147], v[192:195], v[184:187], a[144:147]
ds_read_b64_tr_b16 v[220:221], v243 offset:4096
v_cvt_pk_bf16_f32 v121, v122, v123
v_mfma_f32_16x16x32_bf16 a[152:155], v[192:195], v[188:191], a[152:155]
ds_read_b64_tr_b16 v[222:223], v243 offset:6144
v_cvt_pk_bf16_f32 v122, v124, v125
v_mfma_f32_16x16x32_bf16 a[148:151], v[196:199], v[184:187], a[148:151]
ds_read_b64_tr_b16 v[192:193], v248 offset:4096
v_cvt_pk_bf16_f32 v123, v126, v127
v_mfma_f32_16x16x32_bf16 a[156:159], v[196:199], v[188:191], a[156:159]
ds_read_b64_tr_b16 v[194:195], v248 offset:6144
buffer_store_dwordx4 v[120:123], v236, s[36:39], s28 offen offset:3072 sc1
s_waitcnt lgkmcnt(6)
v_mfma_f32_16x16x32_bf16 a[160:163], v[200:203], v[128:131], a[160:163]
ds_read_b64_tr_b16 v[196:197], v249 offset:4096
v_mfma_f32_16x16x32_bf16 a[168:171], v[200:203], v[132:135], a[168:171]
ds_read_b64_tr_b16 v[198:199], v249 offset:6144
v_mfma_f32_16x16x32_bf16 a[164:167], v[204:207], v[128:131], a[164:167]
ds_read_b64_tr_b16 v[200:201], v242 offset:8192
s_add_u32 s28, s80, 16384
v_mfma_f32_16x16x32_bf16 a[172:175], v[204:207], v[132:135], a[172:175]
ds_read_b64_tr_b16 v[202:203], v242 offset:10240
v_accvgpr_read_b32 v0, a128
v_mfma_f32_16x16x32_bf16 a[176:179], v[208:211], v[128:131], a[176:179]
ds_read_b64_tr_b16 v[204:205], v243 offset:8192
v_accvgpr_read_b32 v1, a129
v_mfma_f32_16x16x32_bf16 a[184:187], v[208:211], v[132:135], a[184:187]
ds_read_b64_tr_b16 v[206:207], v243 offset:10240
v_accvgpr_read_b32 v2, a130
v_mfma_f32_16x16x32_bf16 a[180:183], v[212:215], v[128:131], a[180:183]
ds_read_b64_tr_b16 v[208:209], v248 offset:8192
v_accvgpr_read_b32 v3, a131
v_mfma_f32_16x16x32_bf16 a[188:191], v[212:215], v[132:135], a[188:191]
ds_read_b64_tr_b16 v[210:211], v248 offset:10240
s_waitcnt lgkmcnt(6)
v_mfma_f32_16x16x32_bf16 a[160:163], v[216:219], v[136:139], a[160:163]
ds_read_b64_tr_b16 v[212:213], v249 offset:8192
v_accvgpr_read_b32 v4, a132
v_mfma_f32_16x16x32_bf16 a[168:171], v[216:219], v[140:143], a[168:171]
ds_read_b64_tr_b16 v[214:215], v249 offset:10240
v_accvgpr_read_b32 v5, a133
v_mfma_f32_16x16x32_bf16 a[164:167], v[220:223], v[136:139], a[164:167]
ds_read_b64_tr_b16 v[216:217], v242 offset:12288
v_accvgpr_read_b32 v6, a134
v_mfma_f32_16x16x32_bf16 a[172:175], v[220:223], v[140:143], a[172:175]
ds_read_b64_tr_b16 v[218:219], v242 offset:14336
v_accvgpr_read_b32 v7, a135
v_mfma_f32_16x16x32_bf16 a[176:179], v[192:195], v[136:139], a[176:179]
ds_read_b64_tr_b16 v[220:221], v243 offset:12288
v_cvt_pk_bf16_f32 v0, v0, v1
v_mfma_f32_16x16x32_bf16 a[184:187], v[192:195], v[140:143], a[184:187]
ds_read_b64_tr_b16 v[222:223], v243 offset:14336
v_cvt_pk_bf16_f32 v1, v2, v3
v_mfma_f32_16x16x32_bf16 a[180:183], v[196:199], v[136:139], a[180:183]
ds_read_b64_tr_b16 v[192:193], v248 offset:12288
v_cvt_pk_bf16_f32 v2, v4, v5
v_mfma_f32_16x16x32_bf16 a[188:191], v[196:199], v[140:143], a[188:191]
ds_read_b64_tr_b16 v[194:195], v248 offset:14336
s_waitcnt lgkmcnt(6)
v_mfma_f32_16x16x32_bf16 a[160:163], v[200:203], v[144:147], a[160:163]
ds_read_b64_tr_b16 v[196:197], v249 offset:12288
v_cvt_pk_bf16_f32 v3, v6, v7
v_mfma_f32_16x16x32_bf16 a[168:171], v[200:203], v[148:151], a[168:171]
ds_read_b64_tr_b16 v[198:199], v249 offset:14336
buffer_store_dwordx4 v[0:3], v236, s[36:39], s28 offen offset:0 sc1
v_mfma_f32_16x16x32_bf16 a[164:167], v[204:207], v[144:147], a[164:167]
ds_read_b64_tr_b16 v[200:201], v242 offset:16384
v_accvgpr_read_b32 v8, a136
v_mfma_f32_16x16x32_bf16 a[172:175], v[204:207], v[148:151], a[172:175]
ds_read_b64_tr_b16 v[202:203], v242 offset:18432
v_accvgpr_read_b32 v9, a137
v_mfma_f32_16x16x32_bf16 a[176:179], v[208:211], v[144:147], a[176:179]
ds_read_b64_tr_b16 v[204:205], v243 offset:16384
v_accvgpr_read_b32 v10, a138
v_mfma_f32_16x16x32_bf16 a[184:187], v[208:211], v[148:151], a[184:187]
ds_read_b64_tr_b16 v[206:207], v243 offset:18432
v_accvgpr_read_b32 v11, a139
v_mfma_f32_16x16x32_bf16 a[180:183], v[212:215], v[144:147], a[180:183]
ds_read_b64_tr_b16 v[208:209], v248 offset:16384
v_accvgpr_read_b32 v12, a140
v_mfma_f32_16x16x32_bf16 a[188:191], v[212:215], v[148:151], a[188:191]
ds_read_b64_tr_b16 v[210:211], v248 offset:18432
s_waitcnt lgkmcnt(6)
v_mfma_f32_16x16x32_bf16 a[160:163], v[216:219], v[152:155], a[160:163]
ds_read_b64_tr_b16 v[212:213], v249 offset:16384
v_accvgpr_read_b32 v13, a141
v_mfma_f32_16x16x32_bf16 a[168:171], v[216:219], v[156:159], a[168:171]
ds_read_b64_tr_b16 v[214:215], v249 offset:18432
v_accvgpr_read_b32 v14, a142
v_mfma_f32_16x16x32_bf16 a[164:167], v[220:223], v[152:155], a[164:167]
ds_read_b64_tr_b16 v[216:217], v242 offset:20480
v_accvgpr_read_b32 v15, a143
v_mfma_f32_16x16x32_bf16 a[172:175], v[220:223], v[156:159], a[172:175]
ds_read_b64_tr_b16 v[218:219], v242 offset:22528
v_cvt_pk_bf16_f32 v8, v8, v9
v_mfma_f32_16x16x32_bf16 a[176:179], v[192:195], v[152:155], a[176:179]
ds_read_b64_tr_b16 v[220:221], v243 offset:20480
v_cvt_pk_bf16_f32 v9, v10, v11
v_mfma_f32_16x16x32_bf16 a[184:187], v[192:195], v[156:159], a[184:187]
ds_read_b64_tr_b16 v[222:223], v243 offset:22528
v_cvt_pk_bf16_f32 v10, v12, v13
v_mfma_f32_16x16x32_bf16 a[180:183], v[196:199], v[152:155], a[180:183]
ds_read_b64_tr_b16 v[192:193], v248 offset:20480
v_cvt_pk_bf16_f32 v11, v14, v15
v_mfma_f32_16x16x32_bf16 a[188:191], v[196:199], v[156:159], a[188:191]
ds_read_b64_tr_b16 v[194:195], v248 offset:22528
s_waitcnt lgkmcnt(6)
v_mfma_f32_16x16x32_bf16 a[160:163], v[200:203], v[160:163], a[160:163]
ds_read_b64_tr_b16 v[196:197], v249 offset:20480
buffer_store_dwordx4 v[8:11], v236, s[36:39], s28 offen offset:1024 sc1
v_mfma_f32_16x16x32_bf16 a[168:171], v[200:203], v[164:167], a[168:171]
ds_read_b64_tr_b16 v[198:199], v249 offset:22528
v_accvgpr_read_b32 v16, a144
v_mfma_f32_16x16x32_bf16 a[164:167], v[204:207], v[160:163], a[164:167]
ds_read_b64_tr_b16 v[200:201], v242 offset:24576
v_accvgpr_read_b32 v17, a145
v_mfma_f32_16x16x32_bf16 a[172:175], v[204:207], v[164:167], a[172:175]
ds_read_b64_tr_b16 v[202:203], v242 offset:26624
v_accvgpr_read_b32 v18, a146
v_mfma_f32_16x16x32_bf16 a[176:179], v[208:211], v[160:163], a[176:179]
ds_read_b64_tr_b16 v[204:205], v243 offset:24576
v_accvgpr_read_b32 v19, a147
v_mfma_f32_16x16x32_bf16 a[184:187], v[208:211], v[164:167], a[184:187]
ds_read_b64_tr_b16 v[206:207], v243 offset:26624
v_accvgpr_read_b32 v20, a148
v_mfma_f32_16x16x32_bf16 a[180:183], v[212:215], v[160:163], a[180:183]
ds_read_b64_tr_b16 v[208:209], v248 offset:24576
v_accvgpr_read_b32 v21, a149
v_mfma_f32_16x16x32_bf16 a[188:191], v[212:215], v[164:167], a[188:191]
ds_read_b64_tr_b16 v[210:211], v248 offset:26624
s_waitcnt lgkmcnt(6)
v_mfma_f32_16x16x32_bf16 a[160:163], v[216:219], v[168:171], a[160:163]
ds_read_b64_tr_b16 v[212:213], v249 offset:24576
v_accvgpr_read_b32 v22, a150
v_mfma_f32_16x16x32_bf16 a[168:171], v[216:219], v[172:175], a[168:171]
ds_read_b64_tr_b16 v[214:215], v249 offset:26624
v_accvgpr_read_b32 v23, a151
v_mfma_f32_16x16x32_bf16 a[164:167], v[220:223], v[168:171], a[164:167]
ds_read_b64_tr_b16 v[216:217], v242 offset:28672
v_cvt_pk_bf16_f32 v16, v16, v17
v_mfma_f32_16x16x32_bf16 a[172:175], v[220:223], v[172:175], a[172:175]
ds_read_b64_tr_b16 v[218:219], v242 offset:30720
v_cvt_pk_bf16_f32 v17, v18, v19
v_mfma_f32_16x16x32_bf16 a[176:179], v[192:195], v[168:171], a[176:179]
ds_read_b64_tr_b16 v[220:221], v243 offset:28672
v_cvt_pk_bf16_f32 v18, v20, v21
v_mfma_f32_16x16x32_bf16 a[184:187], v[192:195], v[172:175], a[184:187]
ds_read_b64_tr_b16 v[222:223], v243 offset:30720
v_cvt_pk_bf16_f32 v19, v22, v23
v_mfma_f32_16x16x32_bf16 a[180:183], v[196:199], v[168:171], a[180:183]
ds_read_b64_tr_b16 v[192:193], v248 offset:28672
s_add_u32 s7, s7, 32768
s_cmp_eq_u32 s7, 163840
s_cselect_b32 s7, 0, s7
v_mfma_f32_16x16x32_bf16 a[188:191], v[196:199], v[172:175], a[188:191]
ds_read_b64_tr_b16 v[194:195], v248 offset:30720
)ASM"
R"ASM(
s_waitcnt lgkmcnt(6)
v_mfma_f32_16x16x32_bf16 a[160:163], v[200:203], v[176:179], a[160:163]
ds_read_b64_tr_b16 v[196:197], v249 offset:28672
buffer_store_dwordx4 v[16:19], v236, s[36:39], s28 offen offset:2048 sc1
v_mfma_f32_16x16x32_bf16 a[168:171], v[200:203], v[180:183], a[168:171]
ds_read_b64_tr_b16 v[198:199], v249 offset:30720
v_add_u32 v242, s7, v232
v_add_u32 v243, s7, v233
v_add_u32 v248, s7, v234
v_add_u32 v249, s7, v235
v_mfma_f32_16x16x32_bf16 a[164:167], v[204:207], v[176:179], a[164:167]
s_waitcnt vmcnt(3)
s_barrier
ds_read_b64_tr_b16 v[200:201], v242 offset:0
v_mfma_f32_16x16x32_bf16 a[172:175], v[204:207], v[180:183], a[172:175]
ds_read_b64_tr_b16 v[202:203], v242 offset:2048
v_accvgpr_read_b32 v24, a152
v_mfma_f32_16x16x32_bf16 a[176:179], v[208:211], v[176:179], a[176:179]
ds_read_b64_tr_b16 v[204:205], v243 offset:0
v_accvgpr_read_b32 v25, a153
v_mov_b32 v239, s29
s_mov_b64 exec, 1
buffer_store_dword v239, off, s[36:39], s82 offset:0 sc1
s_mov_b64 exec, -1
v_mfma_f32_16x16x32_bf16 a[184:187], v[208:211], v[180:183], a[184:187]
ds_read_b64_tr_b16 v[206:207], v243 offset:2048
v_accvgpr_read_b32 v26, a154
v_mfma_f32_16x16x32_bf16 a[180:183], v[212:215], v[176:179], a[180:183]
ds_read_b64_tr_b16 v[208:209], v242 offset:4096
v_accvgpr_read_b32 v27, a155
v_mfma_f32_16x16x32_bf16 a[188:191], v[212:215], v[180:183], a[188:191]
ds_read_b64_tr_b16 v[210:211], v242 offset:6144
s_waitcnt lgkmcnt(6)
v_mfma_f32_16x16x32_bf16 a[160:163], v[216:219], v[184:187], a[160:163]
ds_read_b64_tr_b16 v[212:213], v243 offset:4096
v_accvgpr_read_b32 v28, a156
v_mfma_f32_16x16x32_bf16 a[168:171], v[216:219], v[188:191], a[168:171]
ds_read_b64_tr_b16 v[214:215], v243 offset:6144
v_accvgpr_read_b32 v29, a157
v_mfma_f32_16x16x32_bf16 a[164:167], v[220:223], v[184:187], a[164:167]
ds_read_b64_tr_b16 v[216:217], v242 offset:8192
v_accvgpr_read_b32 v30, a158
v_mfma_f32_16x16x32_bf16 a[172:175], v[220:223], v[188:191], a[172:175]
ds_read_b64_tr_b16 v[218:219], v242 offset:10240
v_accvgpr_read_b32 v31, a159
v_cvt_pk_bf16_f32 v24, v24, v25
v_mfma_f32_16x16x32_bf16 a[176:179], v[192:195], v[184:187], a[176:179]
ds_read_b64_tr_b16 v[220:221], v243 offset:8192
v_cvt_pk_bf16_f32 v25, v26, v27
v_mfma_f32_16x16x32_bf16 a[184:187], v[192:195], v[188:191], a[184:187]
ds_read_b64_tr_b16 v[222:223], v243 offset:10240
v_cvt_pk_bf16_f32 v26, v28, v29
v_mfma_f32_16x16x32_bf16 a[180:183], v[196:199], v[184:187], a[180:183]
ds_read_b64_tr_b16 v[192:193], v242 offset:12288
v_cvt_pk_bf16_f32 v27, v30, v31
v_mfma_f32_16x16x32_bf16 a[188:191], v[196:199], v[188:191], a[188:191]
ds_read_b64_tr_b16 v[194:195], v242 offset:14336
buffer_store_dwordx4 v[24:27], v236, s[36:39], s28 offen offset:3072 sc1
s_waitcnt lgkmcnt(6)
v_mfma_f32_16x16x32_bf16 a[192:195], v[200:203], v[128:131], a[192:195]
ds_read_b64_tr_b16 v[196:197], v243 offset:12288
v_mfma_f32_16x16x32_bf16 a[200:203], v[200:203], v[132:135], a[200:203]
ds_read_b64_tr_b16 v[198:199], v243 offset:14336
v_mfma_f32_16x16x32_bf16 a[196:199], v[204:207], v[128:131], a[196:199]
ds_read_b64_tr_b16 v[200:201], v242 offset:16384
s_add_u32 s28, s80, 20480
v_mfma_f32_16x16x32_bf16 a[204:207], v[204:207], v[132:135], a[204:207]
ds_read_b64_tr_b16 v[202:203], v242 offset:18432
v_accvgpr_read_b32 v32, a160
v_accvgpr_read_b32 v33, a161
v_mfma_f32_16x16x32_bf16 a[192:195], v[208:211], v[136:139], a[192:195]
ds_read_b64_tr_b16 v[204:205], v243 offset:16384
v_accvgpr_read_b32 v34, a162
v_accvgpr_read_b32 v35, a163
v_mfma_f32_16x16x32_bf16 a[200:203], v[208:211], v[140:143], a[200:203]
ds_read_b64_tr_b16 v[206:207], v243 offset:18432
v_accvgpr_read_b32 v36, a164
v_accvgpr_read_b32 v37, a165
v_mfma_f32_16x16x32_bf16 a[196:199], v[212:215], v[136:139], a[196:199]
ds_read_b64_tr_b16 v[208:209], v242 offset:20480
v_accvgpr_read_b32 v38, a166
v_accvgpr_read_b32 v39, a167
v_mfma_f32_16x16x32_bf16 a[204:207], v[212:215], v[140:143], a[204:207]
ds_read_b64_tr_b16 v[210:211], v242 offset:22528
s_waitcnt lgkmcnt(6)
v_mfma_f32_16x16x32_bf16 a[192:195], v[216:219], v[144:147], a[192:195]
ds_read_b64_tr_b16 v[212:213], v243 offset:20480
v_cvt_pk_bf16_f32 v32, v32, v33
v_mfma_f32_16x16x32_bf16 a[200:203], v[216:219], v[148:151], a[200:203]
ds_read_b64_tr_b16 v[214:215], v243 offset:22528
v_cvt_pk_bf16_f32 v33, v34, v35
v_mfma_f32_16x16x32_bf16 a[196:199], v[220:223], v[144:147], a[196:199]
ds_read_b64_tr_b16 v[216:217], v242 offset:24576
v_cvt_pk_bf16_f32 v34, v36, v37
v_mfma_f32_16x16x32_bf16 a[204:207], v[220:223], v[148:151], a[204:207]
ds_read_b64_tr_b16 v[218:219], v242 offset:26624
v_cvt_pk_bf16_f32 v35, v38, v39
v_mfma_f32_16x16x32_bf16 a[192:195], v[192:195], v[152:155], a[192:195]
ds_read_b64_tr_b16 v[220:221], v243 offset:24576
buffer_store_dwordx4 v[32:35], v236, s[36:39], s28 offen offset:0 sc1
v_mfma_f32_16x16x32_bf16 a[200:203], v[192:195], v[156:159], a[200:203]
ds_read_b64_tr_b16 v[222:223], v243 offset:26624
v_accvgpr_read_b32 v40, a168
v_mfma_f32_16x16x32_bf16 a[196:199], v[196:199], v[152:155], a[196:199]
ds_read_b64_tr_b16 v[192:193], v242 offset:28672
v_accvgpr_read_b32 v41, a169
v_mfma_f32_16x16x32_bf16 a[204:207], v[196:199], v[156:159], a[204:207]
ds_read_b64_tr_b16 v[194:195], v242 offset:30720
s_waitcnt lgkmcnt(6)
v_mfma_f32_16x16x32_bf16 a[192:195], v[200:203], v[160:163], a[192:195]
ds_read_b64_tr_b16 v[196:197], v243 offset:28672
v_accvgpr_read_b32 v42, a170
v_accvgpr_read_b32 v43, a171
v_mfma_f32_16x16x32_bf16 a[200:203], v[200:203], v[164:167], a[200:203]
ds_read_b64_tr_b16 v[198:199], v243 offset:30720
v_accvgpr_read_b32 v44, a172
v_accvgpr_read_b32 v45, a173
v_mfma_f32_16x16x32_bf16 a[196:199], v[204:207], v[160:163], a[196:199]
ds_read_b64_tr_b16 v[200:201], v248 offset:0
v_accvgpr_read_b32 v46, a174
v_accvgpr_read_b32 v47, a175
v_mfma_f32_16x16x32_bf16 a[204:207], v[204:207], v[164:167], a[204:207]
ds_read_b64_tr_b16 v[202:203], v248 offset:2048
v_cvt_pk_bf16_f32 v40, v40, v41
v_cvt_pk_bf16_f32 v41, v42, v43
v_mfma_f32_16x16x32_bf16 a[192:195], v[208:211], v[168:171], a[192:195]
ds_read_b64_tr_b16 v[204:205], v249 offset:0
v_cvt_pk_bf16_f32 v42, v44, v45
v_cvt_pk_bf16_f32 v43, v46, v47
v_mfma_f32_16x16x32_bf16 a[200:203], v[208:211], v[172:175], a[200:203]
ds_read_b64_tr_b16 v[206:207], v249 offset:2048
buffer_store_dwordx4 v[40:43], v236, s[36:39], s28 offen offset:1024 sc1
v_mfma_f32_16x16x32_bf16 a[196:199], v[212:215], v[168:171], a[196:199]
ds_read_b64_tr_b16 v[208:209], v248 offset:4096
v_accvgpr_read_b32 v48, a176
v_mfma_f32_16x16x32_bf16 a[204:207], v[212:215], v[172:175], a[204:207]
ds_read_b64_tr_b16 v[210:211], v248 offset:6144
v_accvgpr_read_b32 v49, a177
s_waitcnt lgkmcnt(6)
v_mfma_f32_16x16x32_bf16 a[192:195], v[216:219], v[176:179], a[192:195]
ds_read_b64_tr_b16 v[212:213], v249 offset:4096
v_accvgpr_read_b32 v50, a178
v_accvgpr_read_b32 v51, a179
v_mfma_f32_16x16x32_bf16 a[200:203], v[216:219], v[180:183], a[200:203]
ds_read_b64_tr_b16 v[214:215], v249 offset:6144
v_accvgpr_read_b32 v52, a180
v_accvgpr_read_b32 v53, a181
v_mfma_f32_16x16x32_bf16 a[196:199], v[220:223], v[176:179], a[196:199]
ds_read_b64_tr_b16 v[216:217], v248 offset:8192
v_accvgpr_read_b32 v54, a182
v_accvgpr_read_b32 v55, a183
v_mfma_f32_16x16x32_bf16 a[204:207], v[220:223], v[180:183], a[204:207]
ds_read_b64_tr_b16 v[218:219], v248 offset:10240
v_cvt_pk_bf16_f32 v48, v48, v49
v_cvt_pk_bf16_f32 v49, v50, v51
v_mfma_f32_16x16x32_bf16 a[192:195], v[192:195], v[184:187], a[192:195]
ds_read_b64_tr_b16 v[220:221], v249 offset:8192
v_cvt_pk_bf16_f32 v50, v52, v53
v_cvt_pk_bf16_f32 v51, v54, v55
v_mfma_f32_16x16x32_bf16 a[200:203], v[192:195], v[188:191], a[200:203]
ds_read_b64_tr_b16 v[222:223], v249 offset:10240
buffer_store_dwordx4 v[48:51], v236, s[36:39], s28 offen offset:2048 sc1
v_mfma_f32_16x16x32_bf16 a[196:199], v[196:199], v[184:187], a[196:199]
ds_read_b64_tr_b16 v[192:193], v248 offset:12288
v_accvgpr_read_b32 v56, a184
v_mfma_f32_16x16x32_bf16 a[204:207], v[196:199], v[188:191], a[204:207]
ds_read_b64_tr_b16 v[194:195], v248 offset:14336
v_accvgpr_read_b32 v57, a185
s_waitcnt lgkmcnt(6)
v_mfma_f32_16x16x32_bf16 a[208:211], v[200:203], v[128:131], a[208:211]
ds_read_b64_tr_b16 v[196:197], v249 offset:12288
v_accvgpr_read_b32 v58, a186
v_accvgpr_read_b32 v59, a187
v_mfma_f32_16x16x32_bf16 a[216:219], v[200:203], v[132:135], a[216:219]
ds_read_b64_tr_b16 v[198:199], v249 offset:14336
v_accvgpr_read_b32 v60, a188
v_accvgpr_read_b32 v61, a189
v_mfma_f32_16x16x32_bf16 a[212:215], v[204:207], v[128:131], a[212:215]
ds_read_b64_tr_b16 v[200:201], v248 offset:16384
v_accvgpr_read_b32 v62, a190
v_accvgpr_read_b32 v63, a191
v_mfma_f32_16x16x32_bf16 a[220:223], v[204:207], v[132:135], a[220:223]
ds_read_b64_tr_b16 v[202:203], v248 offset:18432
v_cvt_pk_bf16_f32 v56, v56, v57
v_cvt_pk_bf16_f32 v57, v58, v59
v_mfma_f32_16x16x32_bf16 a[208:211], v[208:211], v[136:139], a[208:211]
ds_read_b64_tr_b16 v[204:205], v249 offset:16384
v_cvt_pk_bf16_f32 v58, v60, v61
v_cvt_pk_bf16_f32 v59, v62, v63
v_mfma_f32_16x16x32_bf16 a[216:219], v[208:211], v[140:143], a[216:219]
ds_read_b64_tr_b16 v[206:207], v249 offset:18432
buffer_store_dwordx4 v[56:59], v236, s[36:39], s28 offen offset:3072 sc1
v_mfma_f32_16x16x32_bf16 a[212:215], v[212:215], v[136:139], a[212:215]
ds_read_b64_tr_b16 v[208:209], v248 offset:20480
v_lshlrev_b32 v224, 6, v247
v_mfma_f32_16x16x32_bf16 a[220:223], v[212:215], v[140:143], a[220:223]
ds_read_b64_tr_b16 v[210:211], v248 offset:22528
buffer_load_dword v239, v224, s[36:39], s83 offen offset:0 sc1
s_waitcnt lgkmcnt(6)
v_mfma_f32_16x16x32_bf16 a[208:211], v[216:219], v[144:147], a[208:211]
)ASM"
R"ASM(
ds_read_b64_tr_b16 v[212:213], v249 offset:20480
v_mfma_f32_16x16x32_bf16 a[216:219], v[216:219], v[148:151], a[216:219]
ds_read_b64_tr_b16 v[214:215], v249 offset:22528
v_mfma_f32_16x16x32_bf16 a[212:215], v[220:223], v[144:147], a[212:215]
ds_read_b64_tr_b16 v[216:217], v248 offset:24576
v_mfma_f32_16x16x32_bf16 a[220:223], v[220:223], v[148:151], a[220:223]
ds_read_b64_tr_b16 v[218:219], v248 offset:26624
v_mfma_f32_16x16x32_bf16 a[208:211], v[192:195], v[152:155], a[208:211]
ds_read_b64_tr_b16 v[220:221], v249 offset:24576
v_mfma_f32_16x16x32_bf16 a[216:219], v[192:195], v[156:159], a[216:219]
ds_read_b64_tr_b16 v[222:223], v249 offset:26624
v_mfma_f32_16x16x32_bf16 a[212:215], v[196:199], v[152:155], a[212:215]
ds_read_b64_tr_b16 v[192:193], v248 offset:28672
v_mfma_f32_16x16x32_bf16 a[220:223], v[196:199], v[156:159], a[220:223]
ds_read_b64_tr_b16 v[194:195], v248 offset:30720
s_waitcnt lgkmcnt(6)
v_mfma_f32_16x16x32_bf16 a[208:211], v[200:203], v[160:163], a[208:211]
ds_read_b64_tr_b16 v[196:197], v249 offset:28672
s_add_u32 s28, s80, 24576
v_mfma_f32_16x16x32_bf16 a[216:219], v[200:203], v[164:167], a[216:219]
ds_read_b64_tr_b16 v[198:199], v249 offset:30720
v_accvgpr_read_b32 v64, a192
v_mfma_f32_16x16x32_bf16 a[212:215], v[204:207], v[160:163], a[212:215]
v_accvgpr_read_b32 v65, a193
v_accvgpr_read_b32 v66, a194
v_mfma_f32_16x16x32_bf16 a[220:223], v[204:207], v[164:167], a[220:223]
v_accvgpr_read_b32 v67, a195
v_accvgpr_read_b32 v68, a196
v_mfma_f32_16x16x32_bf16 a[208:211], v[208:211], v[168:171], a[208:211]
v_accvgpr_read_b32 v69, a197
v_accvgpr_read_b32 v70, a198
v_mfma_f32_16x16x32_bf16 a[216:219], v[208:211], v[172:175], a[216:219]
v_accvgpr_read_b32 v71, a199
v_cvt_pk_bf16_f32 v64, v64, v65
v_mfma_f32_16x16x32_bf16 a[212:215], v[212:215], v[168:171], a[212:215]
v_cvt_pk_bf16_f32 v65, v66, v67
v_cvt_pk_bf16_f32 v66, v68, v69
v_mfma_f32_16x16x32_bf16 a[220:223], v[212:215], v[172:175], a[220:223]
v_cvt_pk_bf16_f32 v67, v70, v71
s_waitcnt lgkmcnt(0)
v_mfma_f32_16x16x32_bf16 a[208:211], v[216:219], v[176:179], a[208:211]
buffer_store_dwordx4 v[64:67], v236, s[36:39], s28 offen offset:0 sc1
v_mfma_f32_16x16x32_bf16 a[216:219], v[216:219], v[180:183], a[216:219]
v_accvgpr_read_b32 v72, a200
v_accvgpr_read_b32 v73, a201
v_mfma_f32_16x16x32_bf16 a[212:215], v[220:223], v[176:179], a[212:215]
v_accvgpr_read_b32 v74, a202
v_accvgpr_read_b32 v75, a203
v_mfma_f32_16x16x32_bf16 a[220:223], v[220:223], v[180:183], a[220:223]
v_accvgpr_read_b32 v76, a204
v_accvgpr_read_b32 v77, a205
v_mfma_f32_16x16x32_bf16 a[208:211], v[192:195], v[184:187], a[208:211]
v_accvgpr_read_b32 v78, a206
v_accvgpr_read_b32 v79, a207
v_mfma_f32_16x16x32_bf16 a[216:219], v[192:195], v[188:191], a[216:219]
v_cvt_pk_bf16_f32 v72, v72, v73
v_cvt_pk_bf16_f32 v73, v74, v75
v_mfma_f32_16x16x32_bf16 a[212:215], v[196:199], v[184:187], a[212:215]
v_cvt_pk_bf16_f32 v74, v76, v77
v_cvt_pk_bf16_f32 v75, v78, v79
v_mfma_f32_16x16x32_bf16 a[220:223], v[196:199], v[188:191], a[220:223]
buffer_store_dwordx4 v[72:75], v236, s[36:39], s28 offen offset:1024 sc1
s_nop 7
v_accvgpr_read_b32 v80, a208
v_accvgpr_read_b32 v81, a209
v_accvgpr_read_b32 v82, a210
v_accvgpr_read_b32 v83, a211
v_accvgpr_read_b32 v84, a212
v_accvgpr_read_b32 v85, a213
v_accvgpr_read_b32 v86, a214
v_accvgpr_read_b32 v87, a215
v_cvt_pk_bf16_f32 v80, v80, v81
v_cvt_pk_bf16_f32 v81, v82, v83
v_cvt_pk_bf16_f32 v82, v84, v85
v_cvt_pk_bf16_f32 v83, v86, v87
buffer_store_dwordx4 v[80:83], v236, s[36:39], s28 offen offset:2048 sc1
v_accvgpr_read_b32 v88, a216
v_accvgpr_read_b32 v89, a217
v_accvgpr_read_b32 v90, a218
v_accvgpr_read_b32 v91, a219
v_accvgpr_read_b32 v92, a220
v_accvgpr_read_b32 v93, a221
v_accvgpr_read_b32 v94, a222
v_accvgpr_read_b32 v95, a223
v_cvt_pk_bf16_f32 v88, v88, v89
v_cvt_pk_bf16_f32 v89, v90, v91
v_cvt_pk_bf16_f32 v90, v92, v93
v_cvt_pk_bf16_f32 v91, v94, v95
buffer_store_dwordx4 v[88:91], v236, s[36:39], s28 offen offset:3072 sc1
s_waitcnt vmcnt(4)
s_mov_b32 s97, 0
.Lp18:
v_cmp_ne_u32 vcc, s29, v239
s_add_u32 s97, s97, 1
s_nop 2
s_cbranch_vccz .Lq18
s_cmp_ge_u32 s97, 400000
s_cbranch_scc1 .Lq18
s_sleep 1
buffer_load_dword v239, v224, s[36:39], s83 offen offset:0 sc1
s_waitcnt vmcnt(0)
s_branch .Lp18
.Lq18:
buffer_load_dword v196, v247, s[36:39], s86 offen offset:0
s_add_u32 s28, s85, 52428800
buffer_load_dwordx4 v[192:195], v236, s[36:39], s28 offen offset:0 sc1
s_add_u32 s28, s85, 65011712
buffer_load_dwordx4 v[128:131], v236, s[36:39], s28 offen offset:0 sc1
s_add_u32 s28, s85, 66846720
buffer_load_dwordx4 v[132:135], v236, s[36:39], s28 offen offset:0 sc1
s_add_u32 s28, s85, 68681728
buffer_load_dwordx4 v[136:139], v236, s[36:39], s28 offen offset:0 sc1
s_add_u32 s28, s85, 70516736
buffer_load_dwordx4 v[140:143], v236, s[36:39], s28 offen offset:0 sc1
s_add_u32 s28, s85, 72351744
buffer_load_dwordx4 v[144:147], v236, s[36:39], s28 offen offset:0 sc1
s_add_u32 s28, s85, 74186752
buffer_load_dwordx4 v[148:151], v236, s[36:39], s28 offen offset:0 sc1
s_add_u32 s28, s85, 76021760
buffer_load_dwordx4 v[152:155], v236, s[36:39], s28 offen offset:0 sc1
s_add_u32 s28, s85, 77856768
buffer_load_dwordx4 v[156:159], v236, s[36:39], s28 offen offset:0 sc1
s_add_u32 s28, s85, 79691776
buffer_load_dwordx4 v[160:163], v236, s[36:39], s28 offen offset:0 sc1
s_add_u32 s28, s85, 81526784
buffer_load_dwordx4 v[164:167], v236, s[36:39], s28 offen offset:0 sc1
s_add_u32 s28, s85, 83361792
buffer_load_dwordx4 v[168:171], v236, s[36:39], s28 offen offset:0 sc1
s_add_u32 s28, s85, 85196800
buffer_load_dwordx4 v[172:175], v236, s[36:39], s28 offen offset:0 sc1
s_add_u32 s28, s85, 87031808
buffer_load_dwordx4 v[176:179], v236, s[36:39], s28 offen offset:0 sc1
s_add_u32 s28, s85, 88866816
buffer_load_dwordx4 v[180:183], v236, s[36:39], s28 offen offset:0 sc1
s_add_u32 s28, s85, 90701824
buffer_load_dwordx4 v[184:187], v236, s[36:39], s28 offen offset:0 sc1
s_add_u32 s28, s85, 92536832
buffer_load_dwordx4 v[188:191], v236, s[36:39], s28 offen offset:0 sc1
s_waitcnt vmcnt(18)
s_barrier
v_mov_b32 v239, s29
s_mov_b64 exec, 1
buffer_store_dword v239, off, s[36:39], s82 offset:128 sc1
s_mov_b64 exec, -1
s_cmp_eq_u32 s40, 1
s_cbranch_scc0 .Lnot
s_mov_b32 s28, 196608
v_mov_b32 v239, s29
s_mov_b64 exec, 1
buffer_store_dword v239, off, s[36:39], s28 offset:0 sc1
s_mov_b64 exec, -1
.Lnot:
s_waitcnt vmcnt(17)
v_mov_b32 v197, v196
v_lshlrev_b32 v8, 16, v192
v_lshlrev_b32 v10, 16, v193
v_lshlrev_b32 v12, 16, v194
v_lshlrev_b32 v14, 16, v195
v_and_b32 v9, 0xffff0000, v192
v_and_b32 v11, 0xffff0000, v193
v_and_b32 v13, 0xffff0000, v194
v_and_b32 v15, 0xffff0000, v195
v_pk_mul_f32 v[0:1], v[8:9], v[196:197]
v_pk_mul_f32 v[2:3], v[10:11], v[196:197]
v_pk_mul_f32 v[4:5], v[12:13], v[196:197]
v_pk_mul_f32 v[6:7], v[14:15], v[196:197]
s_waitcnt vmcnt(16)
v_lshlrev_b32 v8, 16, v128
v_lshlrev_b32 v10, 16, v129
v_lshlrev_b32 v12, 16, v130
v_lshlrev_b32 v14, 16, v131
v_and_b32 v9, 0xffff0000, v128
v_and_b32 v11, 0xffff0000, v129
v_and_b32 v13, 0xffff0000, v130
v_and_b32 v15, 0xffff0000, v131
v_pk_add_f32 v[0:1], v[0:1], v[8:9]
v_pk_add_f32 v[2:3], v[2:3], v[10:11]
v_pk_add_f32 v[4:5], v[4:5], v[12:13]
v_pk_add_f32 v[6:7], v[6:7], v[14:15]
s_waitcnt vmcnt(15)
v_lshlrev_b32 v8, 16, v132
v_lshlrev_b32 v10, 16, v133
v_lshlrev_b32 v12, 16, v134
v_lshlrev_b32 v14, 16, v135
v_and_b32 v9, 0xffff0000, v132
v_and_b32 v11, 0xffff0000, v133
v_and_b32 v13, 0xffff0000, v134
v_and_b32 v15, 0xffff0000, v135
v_pk_add_f32 v[0:1], v[0:1], v[8:9]
v_pk_add_f32 v[2:3], v[2:3], v[10:11]
v_pk_add_f32 v[4:5], v[4:5], v[12:13]
v_pk_add_f32 v[6:7], v[6:7], v[14:15]
s_waitcnt vmcnt(14)
v_lshlrev_b32 v8, 16, v136
v_lshlrev_b32 v10, 16, v137
v_lshlrev_b32 v12, 16, v138
v_lshlrev_b32 v14, 16, v139
v_and_b32 v9, 0xffff0000, v136
v_and_b32 v11, 0xffff0000, v137
v_and_b32 v13, 0xffff0000, v138
)ASM"
R"ASM(
v_and_b32 v15, 0xffff0000, v139
v_pk_add_f32 v[0:1], v[0:1], v[8:9]
v_pk_add_f32 v[2:3], v[2:3], v[10:11]
v_pk_add_f32 v[4:5], v[4:5], v[12:13]
v_pk_add_f32 v[6:7], v[6:7], v[14:15]
s_waitcnt vmcnt(13)
v_lshlrev_b32 v8, 16, v140
v_lshlrev_b32 v10, 16, v141
v_lshlrev_b32 v12, 16, v142
v_lshlrev_b32 v14, 16, v143
v_and_b32 v9, 0xffff0000, v140
v_and_b32 v11, 0xffff0000, v141
v_and_b32 v13, 0xffff0000, v142
v_and_b32 v15, 0xffff0000, v143
v_pk_add_f32 v[0:1], v[0:1], v[8:9]
v_pk_add_f32 v[2:3], v[2:3], v[10:11]
v_pk_add_f32 v[4:5], v[4:5], v[12:13]
v_pk_add_f32 v[6:7], v[6:7], v[14:15]
s_waitcnt vmcnt(12)
v_lshlrev_b32 v8, 16, v144
v_lshlrev_b32 v10, 16, v145
v_lshlrev_b32 v12, 16, v146
v_lshlrev_b32 v14, 16, v147
v_and_b32 v9, 0xffff0000, v144
v_and_b32 v11, 0xffff0000, v145
v_and_b32 v13, 0xffff0000, v146
v_and_b32 v15, 0xffff0000, v147
v_pk_add_f32 v[0:1], v[0:1], v[8:9]
v_pk_add_f32 v[2:3], v[2:3], v[10:11]
v_pk_add_f32 v[4:5], v[4:5], v[12:13]
v_pk_add_f32 v[6:7], v[6:7], v[14:15]
s_waitcnt vmcnt(11)
v_lshlrev_b32 v8, 16, v148
v_lshlrev_b32 v10, 16, v149
v_lshlrev_b32 v12, 16, v150
v_lshlrev_b32 v14, 16, v151
v_and_b32 v9, 0xffff0000, v148
v_and_b32 v11, 0xffff0000, v149
v_and_b32 v13, 0xffff0000, v150
v_and_b32 v15, 0xffff0000, v151
v_pk_add_f32 v[0:1], v[0:1], v[8:9]
v_pk_add_f32 v[2:3], v[2:3], v[10:11]
v_pk_add_f32 v[4:5], v[4:5], v[12:13]
v_pk_add_f32 v[6:7], v[6:7], v[14:15]
s_waitcnt vmcnt(10)
v_lshlrev_b32 v8, 16, v152
v_lshlrev_b32 v10, 16, v153
v_lshlrev_b32 v12, 16, v154
v_lshlrev_b32 v14, 16, v155
v_and_b32 v9, 0xffff0000, v152
v_and_b32 v11, 0xffff0000, v153
v_and_b32 v13, 0xffff0000, v154
v_and_b32 v15, 0xffff0000, v155
v_pk_add_f32 v[0:1], v[0:1], v[8:9]
v_pk_add_f32 v[2:3], v[2:3], v[10:11]
v_pk_add_f32 v[4:5], v[4:5], v[12:13]
v_pk_add_f32 v[6:7], v[6:7], v[14:15]
s_waitcnt vmcnt(9)
v_lshlrev_b32 v8, 16, v156
v_lshlrev_b32 v10, 16, v157
v_lshlrev_b32 v12, 16, v158
v_lshlrev_b32 v14, 16, v159
v_and_b32 v9, 0xffff0000, v156
v_and_b32 v11, 0xffff0000, v157
v_and_b32 v13, 0xffff0000, v158
v_and_b32 v15, 0xffff0000, v159
v_pk_add_f32 v[0:1], v[0:1], v[8:9]
v_pk_add_f32 v[2:3], v[2:3], v[10:11]
v_pk_add_f32 v[4:5], v[4:5], v[12:13]
v_pk_add_f32 v[6:7], v[6:7], v[14:15]
s_waitcnt vmcnt(8)
v_lshlrev_b32 v8, 16, v160
v_lshlrev_b32 v10, 16, v161
v_lshlrev_b32 v12, 16, v162
v_lshlrev_b32 v14, 16, v163
v_and_b32 v9, 0xffff0000, v160
v_and_b32 v11, 0xffff0000, v161
v_and_b32 v13, 0xffff0000, v162
v_and_b32 v15, 0xffff0000, v163
v_pk_add_f32 v[0:1], v[0:1], v[8:9]
v_pk_add_f32 v[2:3], v[2:3], v[10:11]
v_pk_add_f32 v[4:5], v[4:5], v[12:13]
v_pk_add_f32 v[6:7], v[6:7], v[14:15]
s_waitcnt vmcnt(7)
v_lshlrev_b32 v8, 16, v164
v_lshlrev_b32 v10, 16, v165
v_lshlrev_b32 v12, 16, v166
v_lshlrev_b32 v14, 16, v167
v_and_b32 v9, 0xffff0000, v164
v_and_b32 v11, 0xffff0000, v165
v_and_b32 v13, 0xffff0000, v166
v_and_b32 v15, 0xffff0000, v167
v_pk_add_f32 v[0:1], v[0:1], v[8:9]
v_pk_add_f32 v[2:3], v[2:3], v[10:11]
v_pk_add_f32 v[4:5], v[4:5], v[12:13]
v_pk_add_f32 v[6:7], v[6:7], v[14:15]
s_waitcnt vmcnt(6)
v_lshlrev_b32 v8, 16, v168
v_lshlrev_b32 v10, 16, v169
v_lshlrev_b32 v12, 16, v170
v_lshlrev_b32 v14, 16, v171
v_and_b32 v9, 0xffff0000, v168
v_and_b32 v11, 0xffff0000, v169
v_and_b32 v13, 0xffff0000, v170
v_and_b32 v15, 0xffff0000, v171
v_pk_add_f32 v[0:1], v[0:1], v[8:9]
v_pk_add_f32 v[2:3], v[2:3], v[10:11]
v_pk_add_f32 v[4:5], v[4:5], v[12:13]
v_pk_add_f32 v[6:7], v[6:7], v[14:15]
buffer_load_dword v239, v224, s[36:39], s83 offen offset:128 sc1
s_waitcnt vmcnt(6)
v_lshlrev_b32 v8, 16, v172
v_lshlrev_b32 v10, 16, v173
v_lshlrev_b32 v12, 16, v174
v_lshlrev_b32 v14, 16, v175
v_and_b32 v9, 0xffff0000, v172
v_and_b32 v11, 0xffff0000, v173
v_and_b32 v13, 0xffff0000, v174
v_and_b32 v15, 0xffff0000, v175
v_pk_add_f32 v[0:1], v[0:1], v[8:9]
v_pk_add_f32 v[2:3], v[2:3], v[10:11]
v_pk_add_f32 v[4:5], v[4:5], v[12:13]
v_pk_add_f32 v[6:7], v[6:7], v[14:15]
s_waitcnt vmcnt(5)
v_lshlrev_b32 v8, 16, v176
v_lshlrev_b32 v10, 16, v177
v_lshlrev_b32 v12, 16, v178
v_lshlrev_b32 v14, 16, v179
v_and_b32 v9, 0xffff0000, v176
v_and_b32 v11, 0xffff0000, v177
v_and_b32 v13, 0xffff0000, v178
v_and_b32 v15, 0xffff0000, v179
v_pk_add_f32 v[0:1], v[0:1], v[8:9]
v_pk_add_f32 v[2:3], v[2:3], v[10:11]
v_pk_add_f32 v[4:5], v[4:5], v[12:13]
v_pk_add_f32 v[6:7], v[6:7], v[14:15]
s_waitcnt vmcnt(4)
v_lshlrev_b32 v8, 16, v180
v_lshlrev_b32 v10, 16, v181
v_lshlrev_b32 v12, 16, v182
v_lshlrev_b32 v14, 16, v183
v_and_b32 v9, 0xffff0000, v180
v_and_b32 v11, 0xffff0000, v181
v_and_b32 v13, 0xffff0000, v182
v_and_b32 v15, 0xffff0000, v183
v_pk_add_f32 v[0:1], v[0:1], v[8:9]
v_pk_add_f32 v[2:3], v[2:3], v[10:11]
v_pk_add_f32 v[4:5], v[4:5], v[12:13]
v_pk_add_f32 v[6:7], v[6:7], v[14:15]
s_waitcnt vmcnt(3)
v_lshlrev_b32 v8, 16, v184
v_lshlrev_b32 v10, 16, v185
v_lshlrev_b32 v12, 16, v186
v_lshlrev_b32 v14, 16, v187
v_and_b32 v9, 0xffff0000, v184
v_and_b32 v11, 0xffff0000, v185
v_and_b32 v13, 0xffff0000, v186
v_and_b32 v15, 0xffff0000, v187
v_pk_add_f32 v[0:1], v[0:1], v[8:9]
v_pk_add_f32 v[2:3], v[2:3], v[10:11]
v_pk_add_f32 v[4:5], v[4:5], v[12:13]
v_pk_add_f32 v[6:7], v[6:7], v[14:15]
s_waitcnt vmcnt(2)
v_lshlrev_b32 v8, 16, v188
v_lshlrev_b32 v10, 16, v189
v_lshlrev_b32 v12, 16, v190
v_lshlrev_b32 v14, 16, v191
v_and_b32 v9, 0xffff0000, v188
v_and_b32 v11, 0xffff0000, v189
v_and_b32 v13, 0xffff0000, v190
v_and_b32 v15, 0xffff0000, v191
v_pk_add_f32 v[0:1], v[0:1], v[8:9]
v_pk_add_f32 v[2:3], v[2:3], v[10:11]
v_pk_add_f32 v[4:5], v[4:5], v[12:13]
v_pk_add_f32 v[6:7], v[6:7], v[14:15]
v_cvt_pk_bf16_f32 v16, v0, v1
v_cvt_pk_bf16_f32 v17, v2, v3
v_cvt_pk_bf16_f32 v18, v4, v5
v_cvt_pk_bf16_f32 v19, v6, v7
buffer_store_dwordx2 v[16:17], v250, s[48:51], 0 offen offset:0
buffer_store_dwordx2 v[18:19], v250, s[48:51], 0 offen offset:32
s_cmp_eq_u32 s40, 2
s_cbranch_scc1 .Lend
s_waitcnt vmcnt(2)
s_mov_b32 s97, 0
.Lp19:
v_cmp_ne_u32 vcc, s29, v239
s_add_u32 s97, s97, 1
s_nop 2
s_cbranch_vccz .Lq19
s_cmp_ge_u32 s97, 400000
s_cbranch_scc1 .Lq19
s_sleep 1
buffer_load_dword v239, v224, s[36:39], s83 offen offset:128 sc1
s_waitcnt vmcnt(0)
s_branch .Lp19
.Lq19:
s_add_u32 s28, s85, 52445184
buffer_load_dwordx4 v[192:195], v236, s[36:39], s28 offen offset:0 sc1
s_add_u32 s28, s85, 65028096
)ASM"
R"ASM(
buffer_load_dwordx4 v[128:131], v236, s[36:39], s28 offen offset:0 sc1
s_add_u32 s28, s85, 66863104
buffer_load_dwordx4 v[132:135], v236, s[36:39], s28 offen offset:0 sc1
s_add_u32 s28, s85, 68698112
buffer_load_dwordx4 v[136:139], v236, s[36:39], s28 offen offset:0 sc1
s_add_u32 s28, s85, 70533120
buffer_load_dwordx4 v[140:143], v236, s[36:39], s28 offen offset:0 sc1
s_add_u32 s28, s85, 72368128
buffer_load_dwordx4 v[144:147], v236, s[36:39], s28 offen offset:0 sc1
s_add_u32 s28, s85, 74203136
buffer_load_dwordx4 v[148:151], v236, s[36:39], s28 offen offset:0 sc1
s_add_u32 s28, s85, 76038144
buffer_load_dwordx4 v[152:155], v236, s[36:39], s28 offen offset:0 sc1
s_add_u32 s28, s85, 77873152
buffer_load_dwordx4 v[156:159], v236, s[36:39], s28 offen offset:0 sc1
s_add_u32 s28, s85, 79708160
buffer_load_dwordx4 v[160:163], v236, s[36:39], s28 offen offset:0 sc1
s_add_u32 s28, s85, 81543168
buffer_load_dwordx4 v[164:167], v236, s[36:39], s28 offen offset:0 sc1
s_add_u32 s28, s85, 83378176
buffer_load_dwordx4 v[168:171], v236, s[36:39], s28 offen offset:0 sc1
s_add_u32 s28, s85, 85213184
buffer_load_dwordx4 v[172:175], v236, s[36:39], s28 offen offset:0 sc1
s_add_u32 s28, s85, 87048192
buffer_load_dwordx4 v[176:179], v236, s[36:39], s28 offen offset:0 sc1
s_add_u32 s28, s85, 88883200
buffer_load_dwordx4 v[180:183], v236, s[36:39], s28 offen offset:0 sc1
s_add_u32 s28, s85, 90718208
buffer_load_dwordx4 v[184:187], v236, s[36:39], s28 offen offset:0 sc1
s_add_u32 s28, s85, 92553216
buffer_load_dwordx4 v[188:191], v236, s[36:39], s28 offen offset:0 sc1
s_waitcnt vmcnt(16)
v_mov_b32 v197, v196
v_lshlrev_b32 v8, 16, v192
v_lshlrev_b32 v10, 16, v193
v_lshlrev_b32 v12, 16, v194
v_lshlrev_b32 v14, 16, v195
v_and_b32 v9, 0xffff0000, v192
v_and_b32 v11, 0xffff0000, v193
v_and_b32 v13, 0xffff0000, v194
v_and_b32 v15, 0xffff0000, v195
v_pk_mul_f32 v[0:1], v[8:9], v[196:197]
v_pk_mul_f32 v[2:3], v[10:11], v[196:197]
v_pk_mul_f32 v[4:5], v[12:13], v[196:197]
v_pk_mul_f32 v[6:7], v[14:15], v[196:197]
s_waitcnt vmcnt(15)
v_lshlrev_b32 v8, 16, v128
v_lshlrev_b32 v10, 16, v129
v_lshlrev_b32 v12, 16, v130
v_lshlrev_b32 v14, 16, v131
v_and_b32 v9, 0xffff0000, v128
v_and_b32 v11, 0xffff0000, v129
v_and_b32 v13, 0xffff0000, v130
v_and_b32 v15, 0xffff0000, v131
v_pk_add_f32 v[0:1], v[0:1], v[8:9]
v_pk_add_f32 v[2:3], v[2:3], v[10:11]
v_pk_add_f32 v[4:5], v[4:5], v[12:13]
v_pk_add_f32 v[6:7], v[6:7], v[14:15]
s_waitcnt vmcnt(14)
v_lshlrev_b32 v8, 16, v132
v_lshlrev_b32 v10, 16, v133
v_lshlrev_b32 v12, 16, v134
v_lshlrev_b32 v14, 16, v135
v_and_b32 v9, 0xffff0000, v132
v_and_b32 v11, 0xffff0000, v133
v_and_b32 v13, 0xffff0000, v134
v_and_b32 v15, 0xffff0000, v135
v_pk_add_f32 v[0:1], v[0:1], v[8:9]
v_pk_add_f32 v[2:3], v[2:3], v[10:11]
v_pk_add_f32 v[4:5], v[4:5], v[12:13]
v_pk_add_f32 v[6:7], v[6:7], v[14:15]
s_waitcnt vmcnt(13)
v_lshlrev_b32 v8, 16, v136
v_lshlrev_b32 v10, 16, v137
v_lshlrev_b32 v12, 16, v138
v_lshlrev_b32 v14, 16, v139
v_and_b32 v9, 0xffff0000, v136
v_and_b32 v11, 0xffff0000, v137
v_and_b32 v13, 0xffff0000, v138
v_and_b32 v15, 0xffff0000, v139
v_pk_add_f32 v[0:1], v[0:1], v[8:9]
v_pk_add_f32 v[2:3], v[2:3], v[10:11]
v_pk_add_f32 v[4:5], v[4:5], v[12:13]
v_pk_add_f32 v[6:7], v[6:7], v[14:15]
s_waitcnt vmcnt(12)
v_lshlrev_b32 v8, 16, v140
v_lshlrev_b32 v10, 16, v141
v_lshlrev_b32 v12, 16, v142
v_lshlrev_b32 v14, 16, v143
v_and_b32 v9, 0xffff0000, v140
v_and_b32 v11, 0xffff0000, v141
v_and_b32 v13, 0xffff0000, v142
v_and_b32 v15, 0xffff0000, v143
v_pk_add_f32 v[0:1], v[0:1], v[8:9]
v_pk_add_f32 v[2:3], v[2:3], v[10:11]
v_pk_add_f32 v[4:5], v[4:5], v[12:13]
v_pk_add_f32 v[6:7], v[6:7], v[14:15]
s_waitcnt vmcnt(11)
v_lshlrev_b32 v8, 16, v144
v_lshlrev_b32 v10, 16, v145
v_lshlrev_b32 v12, 16, v146
v_lshlrev_b32 v14, 16, v147
v_and_b32 v9, 0xffff0000, v144
v_and_b32 v11, 0xffff0000, v145
v_and_b32 v13, 0xffff0000, v146
v_and_b32 v15, 0xffff0000, v147
v_pk_add_f32 v[0:1], v[0:1], v[8:9]
v_pk_add_f32 v[2:3], v[2:3], v[10:11]
v_pk_add_f32 v[4:5], v[4:5], v[12:13]
v_pk_add_f32 v[6:7], v[6:7], v[14:15]
s_waitcnt vmcnt(10)
v_lshlrev_b32 v8, 16, v148
v_lshlrev_b32 v10, 16, v149
v_lshlrev_b32 v12, 16, v150
v_lshlrev_b32 v14, 16, v151
v_and_b32 v9, 0xffff0000, v148
v_and_b32 v11, 0xffff0000, v149
v_and_b32 v13, 0xffff0000, v150
v_and_b32 v15, 0xffff0000, v151
v_pk_add_f32 v[0:1], v[0:1], v[8:9]
v_pk_add_f32 v[2:3], v[2:3], v[10:11]
v_pk_add_f32 v[4:5], v[4:5], v[12:13]
v_pk_add_f32 v[6:7], v[6:7], v[14:15]
s_waitcnt vmcnt(9)
v_lshlrev_b32 v8, 16, v152
v_lshlrev_b32 v10, 16, v153
v_lshlrev_b32 v12, 16, v154
v_lshlrev_b32 v14, 16, v155
v_and_b32 v9, 0xffff0000, v152
v_and_b32 v11, 0xffff0000, v153
v_and_b32 v13, 0xffff0000, v154
v_and_b32 v15, 0xffff0000, v155
v_pk_add_f32 v[0:1], v[0:1], v[8:9]
v_pk_add_f32 v[2:3], v[2:3], v[10:11]
v_pk_add_f32 v[4:5], v[4:5], v[12:13]
v_pk_add_f32 v[6:7], v[6:7], v[14:15]
s_waitcnt vmcnt(8)
v_lshlrev_b32 v8, 16, v156
v_lshlrev_b32 v10, 16, v157
v_lshlrev_b32 v12, 16, v158
v_lshlrev_b32 v14, 16, v159
v_and_b32 v9, 0xffff0000, v156
v_and_b32 v11, 0xffff0000, v157
v_and_b32 v13, 0xffff0000, v158
v_and_b32 v15, 0xffff0000, v159
v_pk_add_f32 v[0:1], v[0:1], v[8:9]
v_pk_add_f32 v[2:3], v[2:3], v[10:11]
v_pk_add_f32 v[4:5], v[4:5], v[12:13]
v_pk_add_f32 v[6:7], v[6:7], v[14:15]
s_waitcnt vmcnt(7)
v_lshlrev_b32 v8, 16, v160
v_lshlrev_b32 v10, 16, v161
v_lshlrev_b32 v12, 16, v162
v_lshlrev_b32 v14, 16, v163
v_and_b32 v9, 0xffff0000, v160
v_and_b32 v11, 0xffff0000, v161
v_and_b32 v13, 0xffff0000, v162
v_and_b32 v15, 0xffff0000, v163
v_pk_add_f32 v[0:1], v[0:1], v[8:9]
v_pk_add_f32 v[2:3], v[2:3], v[10:11]
v_pk_add_f32 v[4:5], v[4:5], v[12:13]
v_pk_add_f32 v[6:7], v[6:7], v[14:15]
s_waitcnt vmcnt(6)
v_lshlrev_b32 v8, 16, v164
v_lshlrev_b32 v10, 16, v165
v_lshlrev_b32 v12, 16, v166
v_lshlrev_b32 v14, 16, v167
v_and_b32 v9, 0xffff0000, v164
v_and_b32 v11, 0xffff0000, v165
v_and_b32 v13, 0xffff0000, v166
v_and_b32 v15, 0xffff0000, v167
v_pk_add_f32 v[0:1], v[0:1], v[8:9]
v_pk_add_f32 v[2:3], v[2:3], v[10:11]
v_pk_add_f32 v[4:5], v[4:5], v[12:13]
v_pk_add_f32 v[6:7], v[6:7], v[14:15]
s_waitcnt vmcnt(5)
v_lshlrev_b32 v8, 16, v168
v_lshlrev_b32 v10, 16, v169
v_lshlrev_b32 v12, 16, v170
v_lshlrev_b32 v14, 16, v171
v_and_b32 v9, 0xffff0000, v168
v_and_b32 v11, 0xffff0000, v169
v_and_b32 v13, 0xffff0000, v170
v_and_b32 v15, 0xffff0000, v171
v_pk_add_f32 v[0:1], v[0:1], v[8:9]
v_pk_add_f32 v[2:3], v[2:3], v[10:11]
v_pk_add_f32 v[4:5], v[4:5], v[12:13]
v_pk_add_f32 v[6:7], v[6:7], v[14:15]
s_waitcnt vmcnt(4)
v_lshlrev_b32 v8, 16, v172
v_lshlrev_b32 v10, 16, v173
v_lshlrev_b32 v12, 16, v174
v_lshlrev_b32 v14, 16, v175
v_and_b32 v9, 0xffff0000, v172
v_and_b32 v11, 0xffff0000, v173
v_and_b32 v13, 0xffff0000, v174
v_and_b32 v15, 0xffff0000, v175
v_pk_add_f32 v[0:1], v[0:1], v[8:9]
v_pk_add_f32 v[2:3], v[2:3], v[10:11]
v_pk_add_f32 v[4:5], v[4:5], v[12:13]
)ASM"
R"ASM(
v_pk_add_f32 v[6:7], v[6:7], v[14:15]
s_waitcnt vmcnt(3)
v_lshlrev_b32 v8, 16, v176
v_lshlrev_b32 v10, 16, v177
v_lshlrev_b32 v12, 16, v178
v_lshlrev_b32 v14, 16, v179
v_and_b32 v9, 0xffff0000, v176
v_and_b32 v11, 0xffff0000, v177
v_and_b32 v13, 0xffff0000, v178
v_and_b32 v15, 0xffff0000, v179
v_pk_add_f32 v[0:1], v[0:1], v[8:9]
v_pk_add_f32 v[2:3], v[2:3], v[10:11]
v_pk_add_f32 v[4:5], v[4:5], v[12:13]
v_pk_add_f32 v[6:7], v[6:7], v[14:15]
s_waitcnt vmcnt(2)
v_lshlrev_b32 v8, 16, v180
v_lshlrev_b32 v10, 16, v181
v_lshlrev_b32 v12, 16, v182
v_lshlrev_b32 v14, 16, v183
v_and_b32 v9, 0xffff0000, v180
v_and_b32 v11, 0xffff0000, v181
v_and_b32 v13, 0xffff0000, v182
v_and_b32 v15, 0xffff0000, v183
v_pk_add_f32 v[0:1], v[0:1], v[8:9]
v_pk_add_f32 v[2:3], v[2:3], v[10:11]
v_pk_add_f32 v[4:5], v[4:5], v[12:13]
v_pk_add_f32 v[6:7], v[6:7], v[14:15]
s_waitcnt vmcnt(1)
v_lshlrev_b32 v8, 16, v184
v_lshlrev_b32 v10, 16, v185
v_lshlrev_b32 v12, 16, v186
v_lshlrev_b32 v14, 16, v187
v_and_b32 v9, 0xffff0000, v184
v_and_b32 v11, 0xffff0000, v185
v_and_b32 v13, 0xffff0000, v186
v_and_b32 v15, 0xffff0000, v187
v_pk_add_f32 v[0:1], v[0:1], v[8:9]
v_pk_add_f32 v[2:3], v[2:3], v[10:11]
v_pk_add_f32 v[4:5], v[4:5], v[12:13]
v_pk_add_f32 v[6:7], v[6:7], v[14:15]
s_waitcnt vmcnt(0)
v_lshlrev_b32 v8, 16, v188
v_lshlrev_b32 v10, 16, v189
v_lshlrev_b32 v12, 16, v190
v_lshlrev_b32 v14, 16, v191
v_and_b32 v9, 0xffff0000, v188
v_and_b32 v11, 0xffff0000, v189
v_and_b32 v13, 0xffff0000, v190
v_and_b32 v15, 0xffff0000, v191
v_pk_add_f32 v[0:1], v[0:1], v[8:9]
v_pk_add_f32 v[2:3], v[2:3], v[10:11]
v_pk_add_f32 v[4:5], v[4:5], v[12:13]
v_pk_add_f32 v[6:7], v[6:7], v[14:15]
v_cvt_pk_bf16_f32 v16, v0, v1
v_cvt_pk_bf16_f32 v17, v2, v3
v_cvt_pk_bf16_f32 v18, v4, v5
v_cvt_pk_bf16_f32 v19, v6, v7
buffer_store_dwordx2 v[16:17], v250, s[48:51], 0 offen offset:512
buffer_store_dwordx2 v[18:19], v250, s[48:51], 0 offen offset:544
s_branch .Lend
.Lmask:
s_xnor_b32 s28, s6, s81
s_and_b32 s28, s28, 1
s_lshl_b32 s28, s28, 14
s_sub_i32 s28, s68, s28
v_mov_b32 v184, 0xff61b1e6
v_mov_b32 v185, 0xff61b1e6
s_cmp_gt_i32 s28, 8192
s_cbranch_scc1 .Lm3
s_cmp_eq_u32 s28, 8192
s_cbranch_scc1 .Lm2
s_cmp_eq_u32 s28, 0
s_cbranch_scc1 .Lm1
v_mov_b64 v[128:129], v[184:185]
v_mov_b64 v[130:131], v[184:185]
v_mov_b64 v[132:133], v[184:185]
v_mov_b64 v[134:135], v[184:185]
v_mov_b64 v[136:137], v[184:185]
v_mov_b64 v[138:139], v[184:185]
v_mov_b64 v[140:141], v[184:185]
v_mov_b64 v[142:143], v[184:185]
v_mov_b64 v[144:145], v[184:185]
v_mov_b64 v[146:147], v[184:185]
v_mov_b64 v[148:149], v[184:185]
v_mov_b64 v[150:151], v[184:185]
v_mov_b64 v[152:153], v[184:185]
v_mov_b64 v[154:155], v[184:185]
v_mov_b64 v[156:157], v[184:185]
v_mov_b64 v[158:159], v[184:185]
v_mov_b32 v186, v184
s_setpc_b64 s[24:25]
.Lm1:
v_cndmask_b32 v128, v128, v184, s[16:17]
v_cndmask_b32 v129, v129, v184, s[18:19]
v_cndmask_b32 v130, v130, v184, s[20:21]
v_cndmask_b32 v131, v131, v184, s[22:23]
v_mov_b64 v[132:133], v[184:185]
v_mov_b64 v[134:135], v[184:185]
v_cndmask_b32 v140, v140, v184, s[16:17]
v_cndmask_b32 v141, v141, v184, s[18:19]
v_cndmask_b32 v142, v142, v184, s[20:21]
v_cndmask_b32 v143, v143, v184, s[22:23]
v_mov_b64 v[144:145], v[184:185]
v_mov_b64 v[146:147], v[184:185]
v_mov_b64 v[148:149], v[184:185]
v_mov_b64 v[150:151], v[184:185]
v_mov_b64 v[152:153], v[184:185]
v_mov_b64 v[154:155], v[184:185]
v_mov_b64 v[156:157], v[184:185]
v_mov_b64 v[158:159], v[184:185]
v_max3_f32 v186, v128, v129, v130
v_max3_f32 v186, v186, v131, v132
v_max3_f32 v186, v186, v133, v134
v_max3_f32 v186, v186, v135, v136
v_max3_f32 v186, v186, v137, v138
v_max3_f32 v186, v186, v139, v140
v_max3_f32 v186, v186, v141, v142
v_max_f32 v186, v186, v143
s_setpc_b64 s[24:25]
.Lm2:
v_cndmask_b32 v144, v144, v184, s[16:17]
v_cndmask_b32 v145, v145, v184, s[18:19]
v_cndmask_b32 v146, v146, v184, s[20:21]
v_cndmask_b32 v147, v147, v184, s[22:23]
v_mov_b64 v[148:149], v[184:185]
v_mov_b64 v[150:151], v[184:185]
v_cndmask_b32 v156, v156, v184, s[16:17]
v_cndmask_b32 v157, v157, v184, s[18:19]
v_cndmask_b32 v158, v158, v184, s[20:21]
v_cndmask_b32 v159, v159, v184, s[22:23]
.Lm3:
s_setpc_b64 s[24:25]
.Lslow:
v_max_f32 v254, v128, v129
v_max3_f32 v254, v254, v130, v131
v_max3_f32 v254, v254, v132, v133
v_max3_f32 v254, v254, v134, v135
v_max3_f32 v254, v254, v144, v145
v_max3_f32 v254, v254, v146, v147
v_max3_f32 v254, v254, v148, v149
v_max3_f32 v254, v254, v150, v151
v_mov_b32 v251, v254
s_nop 1
v_permlane32_swap_b32 v251, v254
s_nop 1
v_max_f32 v254, v254, v251
v_mov_b32 v251, v254
s_nop 1
v_permlane16_swap_b32 v251, v254
s_nop 1
v_max_f32 v254, v254, v251
v_max_f32 v254, s10, v254
v_sub_f32 v128, v128, v254
v_sub_f32 v129, v129, v254
v_sub_f32 v130, v130, v254
v_sub_f32 v131, v131, v254
v_sub_f32 v132, v132, v254
v_sub_f32 v133, v133, v254
v_sub_f32 v134, v134, v254
v_sub_f32 v135, v135, v254
v_sub_f32 v144, v144, v254
v_sub_f32 v145, v145, v254
v_sub_f32 v146, v146, v254
v_sub_f32 v147, v147, v254
v_sub_f32 v148, v148, v254
v_sub_f32 v149, v149, v254
v_sub_f32 v150, v150, v254
v_sub_f32 v151, v151, v254
v_sub_f32 v176, v176, v254
v_sub_f32 v177, v177, v254
v_sub_f32 v178, v178, v254
v_sub_f32 v179, v179, v254
v_max_f32 v255, v136, v137
v_max3_f32 v255, v255, v138, v139
v_max3_f32 v255, v255, v140, v141
v_max3_f32 v255, v255, v142, v143
v_max3_f32 v255, v255, v152, v153
v_max3_f32 v255, v255, v154, v155
v_max3_f32 v255, v255, v156, v157
v_max3_f32 v255, v255, v158, v159
v_mov_b32 v251, v255
s_nop 1
v_permlane32_swap_b32 v251, v255
s_nop 1
v_max_f32 v255, v255, v251
v_mov_b32 v251, v255
s_nop 1
v_permlane16_swap_b32 v251, v255
s_nop 1
v_max_f32 v255, v255, v251
v_max_f32 v255, s10, v255
v_sub_f32 v136, v136, v255
v_sub_f32 v137, v137, v255
v_sub_f32 v138, v138, v255
v_sub_f32 v139, v139, v255
v_sub_f32 v140, v140, v255
v_sub_f32 v141, v141, v255
v_sub_f32 v142, v142, v255
v_sub_f32 v143, v143, v255
v_sub_f32 v152, v152, v255
)ASM"
R"ASM(
v_sub_f32 v153, v153, v255
v_sub_f32 v154, v154, v255
v_sub_f32 v155, v155, v255
v_sub_f32 v156, v156, v255
v_sub_f32 v157, v157, v255
v_sub_f32 v158, v158, v255
v_sub_f32 v159, v159, v255
v_sub_f32 v180, v180, v255
v_sub_f32 v181, v181, v255
v_sub_f32 v182, v182, v255
v_sub_f32 v183, v183, v255
s_cmp_eq_u32 s11, 0
s_cbranch_scc1 .Lresc
v_mov_b64 v[228:229], v[226:227]
v_mov_b64 v[226:227], 0
s_mov_b32 s11, 0
s_mov_b32 s10, 0
s_mov_b32 s13, 0x41800000
s_setpc_b64 s[24:25]
.Lresc:
v_exp_f32 v230, -v254
v_exp_f32 v252, -v255
s_nop 0
v_mov_b32 v231, v230
v_mul_f32 v226, v226, v230
v_mov_b32 v253, v252
v_mul_f32 v227, v227, v252
v_pk_mul_f32 v[0:1], v[0:1], v[230:231]
v_pk_mul_f32 v[2:3], v[2:3], v[230:231]
v_pk_mul_f32 v[4:5], v[4:5], v[230:231]
v_pk_mul_f32 v[6:7], v[6:7], v[230:231]
v_pk_mul_f32 v[8:9], v[8:9], v[252:253]
v_pk_mul_f32 v[10:11], v[10:11], v[252:253]
v_pk_mul_f32 v[12:13], v[12:13], v[252:253]
v_pk_mul_f32 v[14:15], v[14:15], v[252:253]
v_pk_mul_f32 v[16:17], v[16:17], v[230:231]
v_pk_mul_f32 v[18:19], v[18:19], v[230:231]
v_pk_mul_f32 v[20:21], v[20:21], v[230:231]
v_pk_mul_f32 v[22:23], v[22:23], v[230:231]
v_pk_mul_f32 v[24:25], v[24:25], v[252:253]
v_pk_mul_f32 v[26:27], v[26:27], v[252:253]
v_pk_mul_f32 v[28:29], v[28:29], v[252:253]
v_pk_mul_f32 v[30:31], v[30:31], v[252:253]
v_pk_mul_f32 v[32:33], v[32:33], v[230:231]
v_pk_mul_f32 v[34:35], v[34:35], v[230:231]
v_pk_mul_f32 v[36:37], v[36:37], v[230:231]
v_pk_mul_f32 v[38:39], v[38:39], v[230:231]
v_pk_mul_f32 v[40:41], v[40:41], v[252:253]
v_pk_mul_f32 v[42:43], v[42:43], v[252:253]
v_pk_mul_f32 v[44:45], v[44:45], v[252:253]
v_pk_mul_f32 v[46:47], v[46:47], v[252:253]
v_pk_mul_f32 v[48:49], v[48:49], v[230:231]
v_pk_mul_f32 v[50:51], v[50:51], v[230:231]
v_pk_mul_f32 v[52:53], v[52:53], v[230:231]
v_pk_mul_f32 v[54:55], v[54:55], v[230:231]
v_pk_mul_f32 v[56:57], v[56:57], v[252:253]
v_pk_mul_f32 v[58:59], v[58:59], v[252:253]
v_pk_mul_f32 v[60:61], v[60:61], v[252:253]
v_pk_mul_f32 v[62:63], v[62:63], v[252:253]
v_pk_mul_f32 v[64:65], v[64:65], v[230:231]
v_pk_mul_f32 v[66:67], v[66:67], v[230:231]
v_pk_mul_f32 v[68:69], v[68:69], v[230:231]
v_pk_mul_f32 v[70:71], v[70:71], v[230:231]
v_pk_mul_f32 v[72:73], v[72:73], v[252:253]
v_pk_mul_f32 v[74:75], v[74:75], v[252:253]
v_pk_mul_f32 v[76:77], v[76:77], v[252:253]
v_pk_mul_f32 v[78:79], v[78:79], v[252:253]
v_pk_mul_f32 v[80:81], v[80:81], v[230:231]
v_pk_mul_f32 v[82:83], v[82:83], v[230:231]
v_pk_mul_f32 v[84:85], v[84:85], v[230:231]
v_pk_mul_f32 v[86:87], v[86:87], v[230:231]
v_pk_mul_f32 v[88:89], v[88:89], v[252:253]
v_pk_mul_f32 v[90:91], v[90:91], v[252:253]
v_pk_mul_f32 v[92:93], v[92:93], v[252:253]
v_pk_mul_f32 v[94:95], v[94:95], v[252:253]
v_pk_mul_f32 v[96:97], v[96:97], v[230:231]
v_pk_mul_f32 v[98:99], v[98:99], v[230:231]
v_pk_mul_f32 v[100:101], v[100:101], v[230:231]
v_pk_mul_f32 v[102:103], v[102:103], v[230:231]
v_pk_mul_f32 v[104:105], v[104:105], v[252:253]
v_pk_mul_f32 v[106:107], v[106:107], v[252:253]
v_pk_mul_f32 v[108:109], v[108:109], v[252:253]
v_pk_mul_f32 v[110:111], v[110:111], v[252:253]
v_pk_mul_f32 v[112:113], v[112:113], v[230:231]
v_pk_mul_f32 v[114:115], v[114:115], v[230:231]
v_pk_mul_f32 v[116:117], v[116:117], v[230:231]
v_pk_mul_f32 v[118:119], v[118:119], v[230:231]
v_pk_mul_f32 v[120:121], v[120:121], v[252:253]
v_pk_mul_f32 v[122:123], v[122:123], v[252:253]
v_pk_mul_f32 v[124:125], v[124:125], v[252:253]
v_pk_mul_f32 v[126:127], v[126:127], v[252:253]
v_lshlrev_b32 v184, 16, v160
v_and_b32 v185, 0xffff0000, v160
v_pk_mul_f32 v[184:185], v[184:185], v[230:231]
v_cvt_pk_bf16_f32 v160, v184, v185
v_lshlrev_b32 v184, 16, v161
v_and_b32 v185, 0xffff0000, v161
v_pk_mul_f32 v[184:185], v[184:185], v[230:231]
v_cvt_pk_bf16_f32 v161, v184, v185
v_lshlrev_b32 v184, 16, v162
v_and_b32 v185, 0xffff0000, v162
v_pk_mul_f32 v[184:185], v[184:185], v[230:231]
v_cvt_pk_bf16_f32 v162, v184, v185
v_lshlrev_b32 v184, 16, v163
v_and_b32 v185, 0xffff0000, v163
v_pk_mul_f32 v[184:185], v[184:185], v[230:231]
v_cvt_pk_bf16_f32 v163, v184, v185
v_lshlrev_b32 v184, 16, v164
v_and_b32 v185, 0xffff0000, v164
v_pk_mul_f32 v[184:185], v[184:185], v[252:253]
v_cvt_pk_bf16_f32 v164, v184, v185
v_lshlrev_b32 v184, 16, v165
v_and_b32 v185, 0xffff0000, v165
v_pk_mul_f32 v[184:185], v[184:185], v[252:253]
v_cvt_pk_bf16_f32 v165, v184, v185
v_lshlrev_b32 v184, 16, v166
v_and_b32 v185, 0xffff0000, v166
v_pk_mul_f32 v[184:185], v[184:185], v[252:253]
v_cvt_pk_bf16_f32 v166, v184, v185
v_lshlrev_b32 v184, 16, v167
v_and_b32 v185, 0xffff0000, v167
v_pk_mul_f32 v[184:185], v[184:185], v[252:253]
v_cvt_pk_bf16_f32 v167, v184, v185
v_lshlrev_b32 v184, 16, v168
v_and_b32 v185, 0xffff0000, v168
v_pk_mul_f32 v[184:185], v[184:185], v[230:231]
v_cvt_pk_bf16_f32 v168, v184, v185
v_lshlrev_b32 v184, 16, v169
v_and_b32 v185, 0xffff0000, v169
v_pk_mul_f32 v[184:185], v[184:185], v[230:231]
v_cvt_pk_bf16_f32 v169, v184, v185
v_lshlrev_b32 v184, 16, v170
v_and_b32 v185, 0xffff0000, v170
v_pk_mul_f32 v[184:185], v[184:185], v[230:231]
v_cvt_pk_bf16_f32 v170, v184, v185
v_lshlrev_b32 v184, 16, v171
v_and_b32 v185, 0xffff0000, v171
v_pk_mul_f32 v[184:185], v[184:185], v[230:231]
v_cvt_pk_bf16_f32 v171, v184, v185
v_lshlrev_b32 v184, 16, v172
v_and_b32 v185, 0xffff0000, v172
v_pk_mul_f32 v[184:185], v[184:185], v[252:253]
v_cvt_pk_bf16_f32 v172, v184, v185
v_lshlrev_b32 v184, 16, v173
v_and_b32 v185, 0xffff0000, v173
v_pk_mul_f32 v[184:185], v[184:185], v[252:253]
v_cvt_pk_bf16_f32 v173, v184, v185
v_lshlrev_b32 v184, 16, v174
v_and_b32 v185, 0xffff0000, v174
v_pk_mul_f32 v[184:185], v[184:185], v[252:253]
v_cvt_pk_bf16_f32 v174, v184, v185
v_lshlrev_b32 v184, 16, v175
v_and_b32 v185, 0xffff0000, v175
v_pk_mul_f32 v[184:185], v[184:185], v[252:253]
v_cvt_pk_bf16_f32 v175, v184, v185
s_nop 1
s_setpc_b64 s[24:25]
.Lev2:
s_sub_u32 s28, s6, s66
s_cmp_eq_u32 s28, -1
s_cbranch_scc0 .Le3
v_mul_f32 v224, -1.0, v176
v_mul_f32 v225, -1.0, v180
v_mov_b32 v176, 0
v_mov_b32 v177, 0
v_mov_b32 v178, 0
v_mov_b32 v179, 0
v_mov_b32 v180, 0
v_mov_b32 v181, 0
v_mov_b32 v182, 0
v_mov_b32 v183, 0
v_accvgpr_mov_b32 a64, a0
v_accvgpr_mov_b32 a65, a1
v_accvgpr_mov_b32 a66, a2
v_accvgpr_mov_b32 a67, a3
v_accvgpr_mov_b32 a68, a4
v_accvgpr_mov_b32 a69, a5
v_accvgpr_mov_b32 a70, a6
v_accvgpr_mov_b32 a71, a7
v_accvgpr_mov_b32 a72, a8
v_accvgpr_mov_b32 a73, a9
v_accvgpr_mov_b32 a74, a10
v_accvgpr_mov_b32 a75, a11
v_accvgpr_mov_b32 a76, a12
v_accvgpr_mov_b32 a77, a13
v_accvgpr_mov_b32 a78, a14
v_accvgpr_mov_b32 a79, a15
v_accvgpr_mov_b32 a80, a16
v_accvgpr_mov_b32 a81, a17
v_accvgpr_mov_b32 a82, a18
v_accvgpr_mov_b32 a83, a19
v_accvgpr_mov_b32 a84, a20
v_accvgpr_mov_b32 a85, a21
v_accvgpr_mov_b32 a86, a22
v_accvgpr_mov_b32 a87, a23
v_accvgpr_mov_b32 a88, a24
v_accvgpr_mov_b32 a89, a25
v_accvgpr_mov_b32 a90, a26
v_accvgpr_mov_b32 a91, a27
v_accvgpr_mov_b32 a92, a28
)ASM"
R"ASM(
v_accvgpr_mov_b32 a93, a29
v_accvgpr_mov_b32 a94, a30
v_accvgpr_mov_b32 a95, a31
v_accvgpr_mov_b32 a96, a32
v_accvgpr_mov_b32 a97, a33
v_accvgpr_mov_b32 a98, a34
v_accvgpr_mov_b32 a99, a35
v_accvgpr_mov_b32 a100, a36
v_accvgpr_mov_b32 a101, a37
v_accvgpr_mov_b32 a102, a38
v_accvgpr_mov_b32 a103, a39
v_accvgpr_mov_b32 a104, a40
v_accvgpr_mov_b32 a105, a41
v_accvgpr_mov_b32 a106, a42
v_accvgpr_mov_b32 a107, a43
v_accvgpr_mov_b32 a108, a44
v_accvgpr_mov_b32 a109, a45
v_accvgpr_mov_b32 a110, a46
v_accvgpr_mov_b32 a111, a47
v_accvgpr_mov_b32 a112, a48
v_accvgpr_mov_b32 a113, a49
v_accvgpr_mov_b32 a114, a50
v_accvgpr_mov_b32 a115, a51
v_accvgpr_mov_b32 a116, a52
v_accvgpr_mov_b32 a117, a53
v_accvgpr_mov_b32 a118, a54
v_accvgpr_mov_b32 a119, a55
v_accvgpr_mov_b32 a120, a56
v_accvgpr_mov_b32 a121, a57
v_accvgpr_mov_b32 a122, a58
v_accvgpr_mov_b32 a123, a59
v_accvgpr_mov_b32 a124, a60
v_accvgpr_mov_b32 a125, a61
v_accvgpr_mov_b32 a126, a62
v_accvgpr_mov_b32 a127, a63
s_mov_b32 s10, 0xe0ad78ec
s_mov_b32 s11, 1
s_mov_b32 s13, 0xff800000
s_setpc_b64 s[24:25]
.Le3:
s_cmp_eq_u32 s28, 0
s_cbranch_scc0 .Le4
v_cvt_pk_bf16_f32 v0, v0, v1
v_cvt_pk_bf16_f32 v1, v2, v3
v_cvt_pk_bf16_f32 v2, v4, v5
v_cvt_pk_bf16_f32 v3, v6, v7
s_add_u32 s28, s78, 0
buffer_store_dwordx4 v[0:3], v236, s[36:39], s28 offen offset:0 sc1
v_mov_b32 v251, v228
s_nop 1
v_permlane32_swap_b32 v251, v228
s_nop 1
v_add_f32 v228, v228, v251
v_mov_b32 v251, v228
s_nop 1
v_permlane16_swap_b32 v251, v228
s_nop 1
v_add_f32 v228, v228, v251
v_cvt_pk_bf16_f32 v8, v8, v9
v_cvt_pk_bf16_f32 v9, v10, v11
v_cvt_pk_bf16_f32 v10, v12, v13
v_cvt_pk_bf16_f32 v11, v14, v15
buffer_store_dwordx4 v[8:11], v236, s[36:39], s28 offen offset:1024 sc1
v_mov_b32 v251, v229
s_nop 1
v_permlane32_swap_b32 v251, v229
s_nop 1
v_add_f32 v229, v229, v251
v_mov_b32 v251, v229
s_nop 1
v_permlane16_swap_b32 v251, v229
s_nop 1
v_add_f32 v229, v229, v251
v_mov_b64 v[4:5], 0
v_mov_b64 v[6:7], 0
v_mov_b64 v[0:1], 0
v_mov_b64 v[2:3], 0
v_cvt_pk_bf16_f32 v16, v16, v17
v_cvt_pk_bf16_f32 v17, v18, v19
v_cvt_pk_bf16_f32 v18, v20, v21
v_cvt_pk_bf16_f32 v19, v22, v23
buffer_store_dwordx4 v[16:19], v236, s[36:39], s28 offen offset:2048 sc1
v_mov_b64 v[12:13], 0
v_mov_b64 v[14:15], 0
v_mov_b64 v[8:9], 0
v_mov_b64 v[10:11], 0
v_cvt_pk_bf16_f32 v24, v24, v25
v_cvt_pk_bf16_f32 v25, v26, v27
v_cvt_pk_bf16_f32 v26, v28, v29
v_cvt_pk_bf16_f32 v27, v30, v31
buffer_store_dwordx4 v[24:27], v236, s[36:39], s28 offen offset:3072 sc1
v_mov_b64 v[20:21], 0
v_mov_b64 v[22:23], 0
v_mov_b64 v[16:17], 0
v_mov_b64 v[18:19], 0
v_cvt_pk_bf16_f32 v32, v32, v33
v_cvt_pk_bf16_f32 v33, v34, v35
v_cvt_pk_bf16_f32 v34, v36, v37
v_cvt_pk_bf16_f32 v35, v38, v39
s_add_u32 s28, s78, 4096
buffer_store_dwordx4 v[32:35], v236, s[36:39], s28 offen offset:0 sc1
v_mov_b64 v[28:29], 0
v_mov_b64 v[30:31], 0
v_mov_b64 v[24:25], 0
v_mov_b64 v[26:27], 0
v_cvt_pk_bf16_f32 v40, v40, v41
v_cvt_pk_bf16_f32 v41, v42, v43
v_cvt_pk_bf16_f32 v42, v44, v45
v_cvt_pk_bf16_f32 v43, v46, v47
buffer_store_dwordx4 v[40:43], v236, s[36:39], s28 offen offset:1024 sc1
v_mov_b64 v[36:37], 0
v_mov_b64 v[38:39], 0
v_mov_b64 v[32:33], 0
v_mov_b64 v[34:35], 0
v_cvt_pk_bf16_f32 v48, v48, v49
v_cvt_pk_bf16_f32 v49, v50, v51
v_cvt_pk_bf16_f32 v50, v52, v53
v_cvt_pk_bf16_f32 v51, v54, v55
buffer_store_dwordx4 v[48:51], v236, s[36:39], s28 offen offset:2048 sc1
v_mov_b64 v[44:45], 0
v_mov_b64 v[46:47], 0
v_mov_b64 v[40:41], 0
v_mov_b64 v[42:43], 0
v_cvt_pk_bf16_f32 v56, v56, v57
v_cvt_pk_bf16_f32 v57, v58, v59
v_cvt_pk_bf16_f32 v58, v60, v61
v_cvt_pk_bf16_f32 v59, v62, v63
buffer_store_dwordx4 v[56:59], v236, s[36:39], s28 offen offset:3072 sc1
v_mov_b64 v[52:53], 0
v_mov_b64 v[54:55], 0
v_mov_b64 v[48:49], 0
v_mov_b64 v[50:51], 0
v_cvt_pk_bf16_f32 v64, v64, v65
v_cvt_pk_bf16_f32 v65, v66, v67
v_cvt_pk_bf16_f32 v66, v68, v69
v_cvt_pk_bf16_f32 v67, v70, v71
s_add_u32 s28, s78, 8192
buffer_store_dwordx4 v[64:67], v236, s[36:39], s28 offen offset:0 sc1
v_mov_b64 v[60:61], 0
v_mov_b64 v[62:63], 0
v_mov_b64 v[56:57], 0
v_mov_b64 v[58:59], 0
v_cvt_pk_bf16_f32 v72, v72, v73
v_cvt_pk_bf16_f32 v73, v74, v75
v_cvt_pk_bf16_f32 v74, v76, v77
v_cvt_pk_bf16_f32 v75, v78, v79
buffer_store_dwordx4 v[72:75], v236, s[36:39], s28 offen offset:1024 sc1
v_mov_b64 v[68:69], 0
v_mov_b64 v[70:71], 0
v_mov_b64 v[64:65], 0
v_mov_b64 v[66:67], 0
v_cvt_pk_bf16_f32 v80, v80, v81
v_cvt_pk_bf16_f32 v81, v82, v83
v_cvt_pk_bf16_f32 v82, v84, v85
v_cvt_pk_bf16_f32 v83, v86, v87
buffer_store_dwordx4 v[80:83], v236, s[36:39], s28 offen offset:2048 sc1
v_mov_b64 v[76:77], 0
v_mov_b64 v[78:79], 0
v_mov_b64 v[72:73], 0
v_mov_b64 v[74:75], 0
v_cvt_pk_bf16_f32 v88, v88, v89
v_cvt_pk_bf16_f32 v89, v90, v91
v_cvt_pk_bf16_f32 v90, v92, v93
v_cvt_pk_bf16_f32 v91, v94, v95
buffer_store_dwordx4 v[88:91], v236, s[36:39], s28 offen offset:3072 sc1
v_mov_b64 v[84:85], 0
v_mov_b64 v[86:87], 0
v_mov_b64 v[80:81], 0
v_mov_b64 v[82:83], 0
v_cvt_pk_bf16_f32 v96, v96, v97
v_cvt_pk_bf16_f32 v97, v98, v99
v_cvt_pk_bf16_f32 v98, v100, v101
v_cvt_pk_bf16_f32 v99, v102, v103
s_add_u32 s28, s78, 12288
buffer_store_dwordx4 v[96:99], v236, s[36:39], s28 offen offset:0 sc1
v_mov_b64 v[92:93], 0
v_mov_b64 v[94:95], 0
v_mov_b64 v[88:89], 0
v_mov_b64 v[90:91], 0
v_cvt_pk_bf16_f32 v104, v104, v105
v_cvt_pk_bf16_f32 v105, v106, v107
v_cvt_pk_bf16_f32 v106, v108, v109
v_cvt_pk_bf16_f32 v107, v110, v111
buffer_store_dwordx4 v[104:107], v236, s[36:39], s28 offen offset:1024 sc1
v_mov_b64 v[100:101], 0
v_mov_b64 v[102:103], 0
v_mov_b64 v[96:97], 0
v_mov_b64 v[98:99], 0
v_cvt_pk_bf16_f32 v112, v112, v113
v_cvt_pk_bf16_f32 v113, v114, v115
v_cvt_pk_bf16_f32 v114, v116, v117
v_cvt_pk_bf16_f32 v115, v118, v119
buffer_store_dwordx4 v[112:115], v236, s[36:39], s28 offen offset:2048 sc1
v_mov_b64 v[108:109], 0
v_mov_b64 v[110:111], 0
v_mov_b64 v[104:105], 0
v_mov_b64 v[106:107], 0
v_cvt_pk_bf16_f32 v120, v120, v121
v_cvt_pk_bf16_f32 v121, v122, v123
v_cvt_pk_bf16_f32 v122, v124, v125
)ASM"
R"ASM(
v_cvt_pk_bf16_f32 v123, v126, v127
buffer_store_dwordx4 v[120:123], v236, s[36:39], s28 offen offset:3072 sc1
v_mov_b64 v[116:117], 0
v_mov_b64 v[118:119], 0
v_mov_b64 v[112:113], 0
v_mov_b64 v[114:115], 0
buffer_store_dwordx2 v[224:225], v236, s[36:39], s79 offen offset:0 sc1
buffer_store_dwordx2 v[228:229], v236, s[36:39], s79 offen offset:8 sc1
v_mov_b64 v[124:125], 0
v_mov_b64 v[126:127], 0
v_mov_b64 v[120:121], 0
v_mov_b64 v[122:123], 0
s_nop 0
s_setpc_b64 s[24:25]
.Le4:
v_mov_b32 v239, s29
s_mov_b64 exec, 1
buffer_store_dword v239, off, s[36:39], s75 offset:0 sc1
s_mov_b64 exec, -1
s_setpc_b64 s[24:25]
.Lend:
)ASM"
  : "+{v[208:223]}"(va0), "+{v[224:239]}"(va1), "+{v[240:255]}"(va2)
  : "{s[36:51]}"(sa0), "{s[52:67]}"(sa1), "{s[68:83]}"(sa2), "{s[84:99]}"(sa3)
  : "memory","vcc","scc","v0","v1","v2","v3","v4","v5","v6","v7","v8","v9","v10","v11","v12","v13","v14","v15","v16","v17","v18","v19","v20","v21","v22","v23","v24","v25","v26","v27","v28","v29","v30","v31","v32","v33","v34","v35","v36","v37","v38","v39","v40","v41","v42","v43","v44","v45","v46","v47","v48","v49","v50","v51","v52","v53","v54","v55","v56","v57","v58","v59","v60","v61","v62","v63","v64","v65","v66","v67","v68","v69","v70","v71","v72","v73","v74","v75","v76","v77","v78","v79","v80","v81","v82","v83","v84","v85","v86","v87","v88","v89","v90","v91","v92","v93","v94","v95","v96","v97","v98","v99","v100","v101","v102","v103","v104","v105","v106","v107","v108","v109","v110","v111","v112","v113","v114","v115","v116","v117","v118","v119","v120","v121","v122","v123","v124","v125","v126","v127","v128","v129","v130","v131","v132","v133","v134","v135","v136","v137","v138","v139","v140","v141","v142","v143","v144","v145","v146","v147","v148","v149","v150","v151","v152","v153","v154","v155","v156","v157","v158","v159","v160","v161","v162","v163","v164","v165","v166","v167","v168","v169","v170","v171","v172","v173","v174","v175","v176","v177","v178","v179","v180","v181","v182","v183","v184","v185","v186","v187","v188","v189","v190","v191","v192","v193","v194","v195","v196","v197","v198","v199","v200","v201","v202","v203","v204","v205","v206","v207","a0","a1","a2","a3","a4","a5","a6","a7","a8","a9","a10","a11","a12","a13","a14","a15","a16","a17","a18","a19","a20","a21","a22","a23","a24","a25","a26","a27","a28","a29","a30","a31","a32","a33","a34","a35","a36","a37","a38","a39","a40","a41","a42","a43","a44","a45","a46","a47","a48","a49","a50","a51","a52","a53","a54","a55","a56","a57","a58","a59","a60","a61","a62","a63","a64","a65","a66","a67","a68","a69","a70","a71","a72","a73","a74","a75","a76","a77","a78","a79","a80","a81","a82","a83","a84","a85","a86","a87","a88","a89","a90","a91","a92","a93","a94","a95","a96","a97","a98","a99","a100","a101","a102","a103","a104","a105","a106","a107","a108","a109","a110","a111","a112","a113","a114","a115","a116","a117","a118","a119","a120","a121","a122","a123","a124","a125","a126","a127","a128","a129","a130","a131","a132","a133","a134","a135","a136","a137","a138","a139","a140","a141","a142","a143","a144","a145","a146","a147","a148","a149","a150","a151","a152","a153","a154","a155","a156","a157","a158","a159","a160","a161","a162","a163","a164","a165","a166","a167","a168","a169","a170","a171","a172","a173","a174","a175","a176","a177","a178","a179","a180","a181","a182","a183","a184","a185","a186","a187","a188","a189","a190","a191","a192","a193","a194","a195","a196","a197","a198","a199","a200","a201","a202","a203","a204","a205","a206","a207","a208","a209","a210","a211","a212","a213","a214","a215","a216","a217","a218","a219","a220","a221","a222","a223","a224","a225","a226","a227","a228","a229","a230","a231","a232","a233","a234","a235","a236","a237","a238","a239","a240","a241","a242","a243","a244","a245","a246","a247","a248","a249","a250","a251","a252","a253","a254","a255","s4","s5","s6","s7","s8","s9","s10","s11","s12","s13","s14","s15","s16","s17","s18","s19","s20","s21","s22","s23","s24","s25","s26","s27","s28","s29","s30","s31");
}
extern "C" void kernel_launch(void* const* d_in, const int* in_sizes, int n_in, void* d_out, int out_size, void* d_ws, size_t ws_size,
                              hipStream_t stream) {
  const u16* const* in = (const u16* const*)d_in;
  k1_fused<<<256, 256, 0, stream>>>(in[0], in[1], in[2], in[3], in[4], in[5], in[7], in[8], in[9], in[10], in[11], (char*)d_ws, (u16*)d_out);
}
```
